```python
import torch

def _softmax(x, axis=-1):
    return torch.softmax(x, dim=axis)

def _gelu(x):
    return torch.nn.functional.gelu(x, approximate='tanh')

def _silu(x):
    return torch.nn.functional.silu(x)

def _sigmoid(x):
    return torch.sigmoid(x)

def _layernorm(x, axis=-1, eps=1e-05):
    m = x.mean(dim=axis, keepdim=True)
    v = x.var(dim=axis, keepdim=True, unbiased=False)
    return (x - m) / torch.sqrt(v + eps)

def _scatter_add(tgt, idx, src, axis=0):
    return tgt.clone().index_add_(axis, idx, src)

def _segment_sum(src, seg, n):
    out = torch.zeros((n,) + src.shape[1:], dtype=src.dtype)
    return out.index_add_(0, seg, src)

def _pad(x, pw):
    flat = tuple((v for pair in reversed(pw) for v in pair))
    return torch.nn.functional.pad(x, flat)
kernel_name = 'gated_mix__ssm_conv_s5__mixer_gmlp__prompt'

def setup_inputs():

    _w = lambda a: (lambda t: t.to(torch.bfloat16) if t.is_floating_point() else t)(torch.as_tensor(a))

    def _A_setup():
        B, S, D = (1, 2048, 512)
        P = D
        Lambda_re = -0.5 * (lambda _x: torch.exp(torch.as_tensor(_x)))((lambda *s: torch.randn(*s, dtype=torch.bfloat16) * 0.8)(P))
        Lambda_im = (lambda *s: torch.randn(*s, dtype=torch.bfloat16) * 0.8)(P) * 1.0
        B_re = (lambda *s: torch.randn(*s, dtype=torch.bfloat16) * 0.8)(P, D) * D ** (-0.5)
        B_im = (lambda *s: torch.randn(*s, dtype=torch.bfloat16) * 0.8)(P, D) * D ** (-0.5)
        C_re = (lambda *s: torch.randn(*s, dtype=torch.bfloat16) * 0.8)(D, P) * P ** (-0.5)
        C_im = (lambda *s: torch.randn(*s, dtype=torch.bfloat16) * 0.8)(D, P) * P ** (-0.5)
        log_dt = (lambda *s: torch.randn(*s, dtype=torch.bfloat16) * 0.8)(P) * 0.3 - 2.0
        Dskip = (lambda *s: torch.randn(*s, dtype=torch.bfloat16) * 0.8)(D)
        return {'x': _w((lambda *s: torch.randn(*s, dtype=torch.bfloat16) * 0.05)(B, S, D)), 'Lambda_re': _w(Lambda_re), 'Lambda_im': _w(Lambda_im), 'B_re': _w(B_re), 'B_im': _w(B_im), 'C_re': _w(C_re), 'C_im': _w(C_im), 'log_dt': _w(log_dt), 'Dskip': _w(Dskip)}
    _A_in = _A_setup()

    def _B_setup():
        B, S, D, F = (1, 2048, 512, 1376)
        Fh = F // 2
        return {'x': _w((lambda *s: torch.randn(*s, dtype=torch.bfloat16) * 0.05)(B, S, D)), 'w_in': _w((lambda *s: torch.randn(*s, dtype=torch.bfloat16) * 0.2)(D, 2 * Fh)), 'w_sp': _w(0.01 * (lambda *s: torch.randn(*s, dtype=torch.bfloat16) * 0.2)(S, S)), 'b_sp': _w(1.0 + 0.01 * (lambda *s: torch.randn(*s, dtype=torch.bfloat16) * 0.05)(S)), 'w_out': _w((lambda *s: torch.randn(*s, dtype=torch.bfloat16) * 0.05)(Fh, D))}
    _B_in = _B_setup()
    return {'A_x': _A_in['x'], 'A_Lambda_re': _A_in['Lambda_re'], 'A_Lambda_im': _A_in['Lambda_im'], 'A_B_re': _A_in['B_re'], 'A_B_im': _A_in['B_im'], 'A_C_re': _A_in['C_re'], 'A_C_im': _A_in['C_im'], 'A_log_dt': _A_in['log_dt'], 'A_Dskip': _A_in['Dskip'], 'B_x': _B_in['x'], 'B_w_in': _B_in['w_in'], 'B_w_sp': _B_in['w_sp'], 'B_b_sp': _B_in['b_sp'], 'B_w_out': _B_in['w_out'], 'gate_w': _w((lambda *s: torch.randn(*s, dtype=torch.bfloat16) * 0.05)(512) * 16.0)}

def reference(A_x, A_Lambda_re, A_Lambda_im, A_B_re, A_B_im, A_C_re, A_C_im, A_log_dt, A_Dskip, B_x, B_w_in, B_w_sp, B_b_sp, B_w_out, gate_w):

    def _A_ref(x, Lambda_re, Lambda_im, B_re, B_im, C_re, C_im, log_dt, Dskip):
        Bsz, L, H = x.shape
        Lam = Lambda_re + 1j * Lambda_im
        Bmat = B_re + 1j * B_im
        Cmat = C_re + 1j * C_im
        dt = torch.exp(log_dt)
        Lam_bar = torch.exp(dt * Lam)
        B_bar = ((Lam_bar - 1.0) / Lam)[:, None] * Bmat
        Bu = torch.einsum('bld,pd->blp', x + 0j, B_bar)
        h = torch.zeros_like(Bu[:, 0, :])
        hs = []
        for t in range(L):
            h = Lam_bar[None, :] * h + Bu[:, t, :]
            hs.append(h)
        hseq = torch.stack(hs, 1)
        y = 2.0 * torch.einsum('blp,dp->bld', hseq, Cmat).real + x * Dskip[None, None, :]
        return y

    def _B_ref(x, w_in, w_sp, b_sp, w_out):
        B, S, D = x.shape
        F = w_in.shape[-1]
        Fh = F // 2
        z = _gelu(torch.einsum('bsd,df->bsf', x, w_in))
        z1 = z[..., :Fh].clone()
        z2 = _layernorm(z[..., Fh:], axis=-1)
        g = torch.einsum('ts,bsf->btf', w_sp, z2) + b_sp[None, :, None]
        return torch.einsum('bsf,fd->bsd', z1 * g, w_out)
    x = A_x
    g = _sigmoid((x * gate_w).sum(-1, keepdims=True))
    a = _A_ref(x, A_Lambda_re, A_Lambda_im, A_B_re, A_B_im, A_C_re, A_C_im, A_log_dt, A_Dskip)
    b = _B_ref(x, B_w_in, B_w_sp, B_b_sp, B_w_out)
    return g * a + (1 - g) * b
```

```cpp
#include <hip/hip_runtime.h>
#include <hip/hip_bf16.h>
#include <stdint.h>

#define ROUND_DT 0

typedef unsigned short u16;
__device__ __forceinline__ float bf2f(u16 v) { return __uint_as_float(((unsigned)v) << 16); }
__device__ __forceinline__ float rbf(float x) { return __bfloat162float(__float2bfloat16(x)); }

constexpr int L = 2048, D = 512, P = 512, F = 1376, FH = 688;

__global__ void k_proj(const u16* x, const u16* Bre, const u16* Bim, const u16* win, float* ure, float* uim, float* z) {
    int t = blockIdx.y;
    int j = blockIdx.x * blockDim.x + threadIdx.x;
    if (j >= 2 * P + F) return;
    float acc = 0.f;
    if (j < 2 * P) {
        const u16* b = j < P ? Bre + j * D : Bim + (j - P) * D;
        for (int d = 0; d < D; ++d) acc += bf2f(x[t * D + d]) * bf2f(b[d]);
        if (j < P) ure[t * P + j] = acc; else uim[t * P + j - P] = acc;
    } else {
        int f = j - 2 * P;
        for (int d = 0; d < D; ++d) acc += bf2f(x[t * D + d]) * bf2f(win[d * F + f]);
        float g = 0.5f * acc * (1.f + tanhf(0.7978845608028654f * (acc + 0.044715f * acc * acc * acc)));
        z[t * F + f] = g;
    }
}

__global__ void k_scan(const u16* lre, const u16* lim, const u16* ldt, float* ure, float* uim) {
    int p = blockIdx.x * blockDim.x + threadIdx.x;
    if (p >= P) return;
    float a = bf2f(lre[p]), b = bf2f(lim[p]);
    float dt = expf(bf2f(ldt[p]));
    if (ROUND_DT) dt = rbf(dt);
    float e = expf(dt * a);
    float lr = e * cosf(dt * b), li = e * sinf(dt * b);
    float nr = lr - 1.f, ni = li;
    float den = a * a + b * b;
    float cr = (nr * a + ni * b) / den, ci = (ni * a - nr * b) / den;
    float hr = 0.f, hi = 0.f;
    for (int t = 0; t < L; ++t) {
        float ur = ure[t * P + p], ui = uim[t * P + p];
        float br = cr * ur - ci * ui, bi = cr * ui + ci * ur;
        float nhr = lr * hr - li * hi + br;
        float nhi = lr * hi + li * hr + bi;
        hr = nhr; hi = nhi;
        ure[t * P + p] = hr; uim[t * P + p] = hi;
    }
}

__global__ void k_ln(float* z) {
    int t = blockIdx.x;
    __shared__ float red[256];
    float s = 0.f;
    for (int f = threadIdx.x; f < FH; f += 256) s += z[t * F + FH + f];
    red[threadIdx.x] = s; __syncthreads();
    for (int o = 128; o > 0; o >>= 1) { if (threadIdx.x < o) red[threadIdx.x] += red[threadIdx.x + o]; __syncthreads(); }
    float m = red[0] / FH; __syncthreads();
    s = 0.f;
    for (int f = threadIdx.x; f < FH; f += 256) { float d = z[t * F + FH + f] - m; s += d * d; }
    red[threadIdx.x] = s; __syncthreads();
    for (int o = 128; o > 0; o >>= 1) { if (threadIdx.x < o) red[threadIdx.x] += red[threadIdx.x + o]; __syncthreads(); }
    float r = 1.f / sqrtf(red[0] / FH + 1e-5f);
    for (int f = threadIdx.x; f < FH; f += 256) z[t * F + FH + f] = (z[t * F + FH + f] - m) * r;
}

__global__ void k_sp(const u16* wsp, const u16* bsp, const float* z, float* zg) {
    int t = blockIdx.y;
    int f = blockIdx.x * blockDim.x + threadIdx.x;
    if (f >= FH) return;
    float acc = 0.f;
    for (int s = 0; s < L; ++s) acc += bf2f(wsp[t * L + s]) * z[s * F + FH + f];
    zg[t * FH + f] = (acc + bf2f(bsp[t])) * z[t * F + f];
}

__global__ void k_out(const u16* x, const u16* Cre, const u16* Cim, const u16* Dsk, const u16* wout, const u16* gw,
                      const float* hre, const float* him, const float* zg, float* out) {
    int t = blockIdx.y;
    int d = blockIdx.x * blockDim.x + threadIdx.x;
    float gs = 0.f;
    for (int k = 0; k < D; ++k) gs += bf2f(x[t * D + k]) * bf2f(gw[k]);
    float g = 1.f / (1.f + expf(-gs));
    float a = 0.f;
    for (int p = 0; p < P; ++p) a += hre[t * P + p] * bf2f(Cre[d * P + p]) - him[t * P + p] * bf2f(Cim[d * P + p]);
    a = 2.f * a + bf2f(x[t * D + d]) * bf2f(Dsk[d]);
    float b = 0.f;
    for (int f = 0; f < FH; ++f) b += zg[t * FH + f] * bf2f(wout[f * D + d]);
    out[t * D + d] = g * a + (1.f - g) * b;
}

extern "C" void kernel_launch(void* const* d_in, const int* in_sizes, int n_in,
                              void* d_out, int out_size, void* d_ws, size_t ws_size,
                              hipStream_t stream) {
    const u16* x = (const u16*)d_in[0];
    const u16* lre = (const u16*)d_in[1];
    const u16* lim = (const u16*)d_in[2];
    const u16* Bre = (const u16*)d_in[3];
    const u16* Bim = (const u16*)d_in[4];
    const u16* Cre = (const u16*)d_in[5];
    const u16* Cim = (const u16*)d_in[6];
    const u16* ldt = (const u16*)d_in[7];
    const u16* Dsk = (const u16*)d_in[8];
    const u16* win = (const u16*)d_in[10];
    const u16* wsp = (const u16*)d_in[11];
    const u16* bsp = (const u16*)d_in[12];
    const u16* wout = (const u16*)d_in[13];
    const u16* gw = (const u16*)d_in[14];
    float* ws = (float*)d_ws;
    float* ure = ws;
    float* uim = ure + L * P;
    float* z = uim + L * P;
    float* zg = z + L * F;
    k_proj<<<dim3((2 * P + F + 255) / 256, L), 256, 0, stream>>>(x, Bre, Bim, win, ure, uim, z);
    k_scan<<<P / 64, 64, 0, stream>>>(lre, lim, ldt, ure, uim);
    k_ln<<<L, 256, 0, stream>>>(z);
    k_sp<<<dim3((FH + 255) / 256, L), 256, 0, stream>>>(wsp, bsp, z, zg);
    k_out<<<dim3(D / 256, L), 256, 0, stream>>>(x, Cre, Cim, Dsk, wout, gw, ure, uim, zg, (float*)d_out);
}
```

```cpp
#include <hip/hip_runtime.h>
#include <hip/hip_bf16.h>
#include <stdint.h>

#define ROUND_DT 1

typedef unsigned short u16;
__device__ __forceinline__ float bf2f(u16 v) { return __uint_as_float(((unsigned)v) << 16); }
__device__ __forceinline__ float rbf(float x) { return __bfloat162float(__float2bfloat16(x)); }

constexpr int L = 2048, D = 512, P = 512, F = 1376, FH = 688;

__global__ void k_proj(const u16* x, const u16* Bre, const u16* Bim, const u16* win, float* ure, float* uim, float* z) {
    int t = blockIdx.y;
    int j = blockIdx.x * blockDim.x + threadIdx.x;
    if (j >= 2 * P + F) return;
    float acc = 0.f;
    if (j < 2 * P) {
        const u16* b = j < P ? Bre + j * D : Bim + (j - P) * D;
        for (int d = 0; d < D; ++d) acc += bf2f(x[t * D + d]) * bf2f(b[d]);
        if (j < P) ure[t * P + j] = acc; else uim[t * P + j - P] = acc;
    } else {
        int f = j - 2 * P;
        for (int d = 0; d < D; ++d) acc += bf2f(x[t * D + d]) * bf2f(win[d * F + f]);
        float g = 0.5f * acc * (1.f + tanhf(0.7978845608028654f * (acc + 0.044715f * acc * acc * acc)));
        z[t * F + f] = g;
    }
}

__global__ void k_scan(const u16* lre, const u16* lim, const u16* ldt, float* ure, float* uim) {
    int p = blockIdx.x * blockDim.x + threadIdx.x;
    if (p >= P) return;
    float a = bf2f(lre[p]), b = bf2f(lim[p]);
    float dt = expf(bf2f(ldt[p]));
    if (ROUND_DT) dt = rbf(dt);
    float e = expf(dt * a);
    float lr = e * cosf(dt * b), li = e * sinf(dt * b);
    float nr = lr - 1.f, ni = li;
    float den = a * a + b * b;
    float cr = (nr * a + ni * b) / den, ci = (ni * a - nr * b) / den;
    float hr = 0.f, hi = 0.f;
    for (int t = 0; t < L; ++t) {
        float ur = ure[t * P + p], ui = uim[t * P + p];
        float br = cr * ur - ci * ui, bi = cr * ui + ci * ur;
        float nhr = lr * hr - li * hi + br;
        float nhi = lr * hi + li * hr + bi;
        hr = nhr; hi = nhi;
        ure[t * P + p] = hr; uim[t * P + p] = hi;
    }
}

__global__ void k_ln(float* z) {
    int t = blockIdx.x;
    __shared__ float red[256];
    float s = 0.f;
    for (int f = threadIdx.x; f < FH; f += 256) s += z[t * F + FH + f];
    red[threadIdx.x] = s; __syncthreads();
    for (int o = 128; o > 0; o >>= 1) { if (threadIdx.x < o) red[threadIdx.x] += red[threadIdx.x + o]; __syncthreads(); }
    float m = red[0] / FH; __syncthreads();
    s = 0.f;
    for (int f = threadIdx.x; f < FH; f += 256) { float d = z[t * F + FH + f] - m; s += d * d; }
    red[threadIdx.x] = s; __syncthreads();
    for (int o = 128; o > 0; o >>= 1) { if (threadIdx.x < o) red[threadIdx.x] += red[threadIdx.x + o]; __syncthreads(); }
    float r = 1.f / sqrtf(red[0] / FH + 1e-5f);
    for (int f = threadIdx.x; f < FH; f += 256) z[t * F + FH + f] = (z[t * F + FH + f] - m) * r;
}

__global__ void k_sp(const u16* wsp, const u16* bsp, const float* z, float* zg) {
    int t = blockIdx.y;
    int f = blockIdx.x * blockDim.x + threadIdx.x;
    if (f >= FH) return;
    float acc = 0.f;
    for (int s = 0; s < L; ++s) acc += bf2f(wsp[t * L + s]) * z[s * F + FH + f];
    zg[t * FH + f] = (acc + bf2f(bsp[t])) * z[t * F + f];
}

__global__ void k_out(const u16* x, const u16* Cre, const u16* Cim, const u16* Dsk, const u16* wout, const u16* gw,
                      const float* hre, const float* him, const float* zg, float* out) {
    int t = blockIdx.y;
    int d = blockIdx.x * blockDim.x + threadIdx.x;
    float gs = 0.f;
    for (int k = 0; k < D; ++k) gs += bf2f(x[t * D + k]) * bf2f(gw[k]);
    float g = 1.f / (1.f + expf(-gs));
    float a = 0.f;
    for (int p = 0; p < P; ++p) a += hre[t * P + p] * bf2f(Cre[d * P + p]) - him[t * P + p] * bf2f(Cim[d * P + p]);
    a = 2.f * a + bf2f(x[t * D + d]) * bf2f(Dsk[d]);
    float b = 0.f;
    for (int f = 0; f < FH; ++f) b += zg[t * FH + f] * bf2f(wout[f * D + d]);
    out[t * D + d] = g * a + (1.f - g) * b;
}

#define GAS __attribute__((address_space(1)))
#define LAS __attribute__((address_space(3)))
template<int N> __device__ __forceinline__ void waitv() { asm volatile("s_waitcnt vmcnt(%0)" :: "i"(N) : "memory"); }
template<int MODE>
__global__ __launch_bounds__(256, 1) void k_probe(const char* __restrict__ A, const char* __restrict__ X, unsigned long long* rec) {
    extern __shared__ __attribute__((aligned(16))) char shm[];
    const int wid = __builtin_amdgcn_readfirstlane(threadIdx.x >> 6), lane = threadIdx.x & 63;
    const int b = blockIdx.x, xcd = b & 7, idx = b >> 3;
    const int rb = xcd * 4 + (idx >> 3), cg = idx & 7;
    unsigned long long r0 = __builtin_amdgcn_s_memrealtime(), c0 = __builtin_amdgcn_s_memtime();
    if (MODE == 0) {
        for (int j = 0; j < 8; ++j)
            __builtin_amdgcn_global_load_lds((const GAS unsigned*)(A + (size_t)b * 32768 + (wid * 8 + j) * 1024 + lane * 16), (LAS unsigned*)((char LAS*)shm + (wid * 8 + j) * 1024), 16, 0, 0);
    } else if (MODE == 1) {
        for (int s = 0; s < 32; ++s) {
            char LAS* slot = (char LAS*)shm + (s % 7) * 20480 + wid * 5120;
#pragma unroll
            for (int j = 0; j < 5; ++j) {
                int gg = j * 4 + wid; const char* p;
                if (gg < 8) p = A + (size_t)(rb * 64 + gg * 8 + (lane >> 3)) * 4096 + s * 128 + (lane & 7) * 16;
                else p = X + (size_t)(cg * 262144 + ((s * 12288 + (gg - 8) * 1024) & 262143)) + lane * 16;
                __builtin_amdgcn_global_load_lds((const GAS unsigned*)p, (LAS unsigned*)(slot + j * 1024), 16, 0, 0);
            }
            waitv<30>();
        }
    } else if (MODE == 2) {
        __builtin_amdgcn_global_load_lds((const GAS unsigned*)(X + (size_t)b * 4096 + wid * 1024 + lane * 16), (LAS unsigned*)((char LAS*)shm + wid * 1024), 16, 0, 0);
    }
    waitv<0>();
    __syncthreads();
    unsigned long long r1 = __builtin_amdgcn_s_memrealtime(), c1 = __builtin_amdgcn_s_memtime();
    if (threadIdx.x == 0) { rec[b * 4] = r0; rec[b * 4 + 1] = r1; rec[b * 4 + 2] = c1 - c0; }
}
template<int WHAT>
__global__ void k_spin(const unsigned long long* rec) {
    unsigned long long mn = ~0ull, mx = 0, sum = 0, csum = 0, smx = 0;
    for (int b = 0; b < 256; ++b) { unsigned long long a = rec[b * 4], e = rec[b * 4 + 1]; mn = a < mn ? a : mn; mx = e > mx ? e : mx; smx = a > smx ? a : smx; sum += e - a; csum += rec[b * 4 + 2]; }
    unsigned long long t = WHAT == 0 ? (mx - mn) * 100 : WHAT == 1 ? sum * 100 / 256 : WHAT == 2 ? (smx - mn) * 100 : csum * 10000 / (sum ? sum : 1);
    if (t > 400000) t = 400000;
    unsigned long long s = __builtin_amdgcn_s_memrealtime();
    while (__builtin_amdgcn_s_memrealtime() - s < t) __builtin_amdgcn_s_sleep(4);
}
extern "C" void kernel_launch(void* const* d_in, const int* in_sizes, int n_in,
                              void* d_out, int out_size, void* d_ws, size_t ws_size,
                              hipStream_t stream) {
    const u16* x = (const u16*)d_in[0];
    const u16* lre = (const u16*)d_in[1];
    const u16* lim = (const u16*)d_in[2];
    const u16* Bre = (const u16*)d_in[3];
    const u16* Bim = (const u16*)d_in[4];
    const u16* Cre = (const u16*)d_in[5];
    const u16* Cim = (const u16*)d_in[6];
    const u16* ldt = (const u16*)d_in[7];
    const u16* Dsk = (const u16*)d_in[8];
    const u16* win = (const u16*)d_in[10];
    const u16* wsp = (const u16*)d_in[11];
    const u16* bsp = (const u16*)d_in[12];
    const u16* wout = (const u16*)d_in[13];
    const u16* gw = (const u16*)d_in[14];
    float* ws = (float*)d_ws;
    float* ure = ws;
    float* uim = ure + L * P;
    float* z = uim + L * P;
    float* zg = z + L * F;
    unsigned long long* rec = (unsigned long long*)(ws + 32 * 1024 * 1024);
    hipFuncSetAttribute((const void*)k_probe<2>, hipFuncAttributeMaxDynamicSharedMemorySize, 143360);
    k_probe<2><<<256, 256, 143360, stream>>>((const char*)wsp, (const char*)x, rec);
    k_spin<2><<<1, 64, 0, stream>>>(rec);
    k_proj<<<dim3((2 * P + F + 255) / 256, L), 256, 0, stream>>>(x, Bre, Bim, win, ure, uim, z);
    k_scan<<<P / 64, 64, 0, stream>>>(lre, lim, ldt, ure, uim);
    k_ln<<<L, 256, 0, stream>>>(z);
    k_sp<<<dim3((FH + 255) / 256, L), 256, 0, stream>>>(wsp, bsp, z, zg);
    k_out<<<dim3(D / 256, L), 256, 0, stream>>>(x, Cre, Cim, Dsk, wout, gw, ure, uim, zg, (float*)d_out);
}
```

```cpp
#include <hip/hip_runtime.h>
#include <hip/hip_bf16.h>
#include <stdint.h>

#define ROUND_DT 1

typedef unsigned short u16;
__device__ __forceinline__ float bf2f(u16 v) { return __uint_as_float(((unsigned)v) << 16); }
__device__ __forceinline__ float rbf(float x) { return __bfloat162float(__float2bfloat16(x)); }

constexpr int L = 2048, D = 512, P = 512, F = 1376, FH = 688;

__global__ void k_proj(const u16* x, const u16* Bre, const u16* Bim, const u16* win, float* ure, float* uim, float* z) {
    int t = blockIdx.y;
    int j = blockIdx.x * blockDim.x + threadIdx.x;
    if (j >= 2 * P + F) return;
    float acc = 0.f;
    if (j < 2 * P) {
        const u16* b = j < P ? Bre + j * D : Bim + (j - P) * D;
        for (int d = 0; d < D; ++d) acc += bf2f(x[t * D + d]) * bf2f(b[d]);
        if (j < P) ure[t * P + j] = acc; else uim[t * P + j - P] = acc;
    } else {
        int f = j - 2 * P;
        for (int d = 0; d < D; ++d) acc += bf2f(x[t * D + d]) * bf2f(win[d * F + f]);
        float g = 0.5f * acc * (1.f + tanhf(0.7978845608028654f * (acc + 0.044715f * acc * acc * acc)));
        z[t * F + f] = g;
    }
}

__global__ void k_scan(const u16* lre, const u16* lim, const u16* ldt, float* ure, float* uim) {
    int p = blockIdx.x * blockDim.x + threadIdx.x;
    if (p >= P) return;
    float a = bf2f(lre[p]), b = bf2f(lim[p]);
    float dt = expf(bf2f(ldt[p]));
    if (ROUND_DT) dt = rbf(dt);
    float e = expf(dt * a);
    float lr = e * cosf(dt * b), li = e * sinf(dt * b);
    float nr = lr - 1.f, ni = li;
    float den = a * a + b * b;
    float cr = (nr * a + ni * b) / den, ci = (ni * a - nr * b) / den;
    float hr = 0.f, hi = 0.f;
    for (int t = 0; t < L; ++t) {
        float ur = ure[t * P + p], ui = uim[t * P + p];
        float br = cr * ur - ci * ui, bi = cr * ui + ci * ur;
        float nhr = lr * hr - li * hi + br;
        float nhi = lr * hi + li * hr + bi;
        hr = nhr; hi = nhi;
        ure[t * P + p] = hr; uim[t * P + p] = hi;
    }
}

__global__ void k_ln(float* z) {
    int t = blockIdx.x;
    __shared__ float red[256];
    float s = 0.f;
    for (int f = threadIdx.x; f < FH; f += 256) s += z[t * F + FH + f];
    red[threadIdx.x] = s; __syncthreads();
    for (int o = 128; o > 0; o >>= 1) { if (threadIdx.x < o) red[threadIdx.x] += red[threadIdx.x + o]; __syncthreads(); }
    float m = red[0] / FH; __syncthreads();
    s = 0.f;
    for (int f = threadIdx.x; f < FH; f += 256) { float d = z[t * F + FH + f] - m; s += d * d; }
    red[threadIdx.x] = s; __syncthreads();
    for (int o = 128; o > 0; o >>= 1) { if (threadIdx.x < o) red[threadIdx.x] += red[threadIdx.x + o]; __syncthreads(); }
    float r = 1.f / sqrtf(red[0] / FH + 1e-5f);
    for (int f = threadIdx.x; f < FH; f += 256) z[t * F + FH + f] = (z[t * F + FH + f] - m) * r;
}

__global__ void k_sp(const u16* wsp, const u16* bsp, const float* z, float* zg) {
    int t = blockIdx.y;
    int f = blockIdx.x * blockDim.x + threadIdx.x;
    if (f >= FH) return;
    float acc = 0.f;
    for (int s = 0; s < L; ++s) acc += bf2f(wsp[t * L + s]) * z[s * F + FH + f];
    zg[t * FH + f] = (acc + bf2f(bsp[t])) * z[t * F + f];
}

__global__ void k_out(const u16* x, const u16* Cre, const u16* Cim, const u16* Dsk, const u16* wout, const u16* gw,
                      const float* hre, const float* him, const float* zg, float* out) {
    int t = blockIdx.y;
    int d = blockIdx.x * blockDim.x + threadIdx.x;
    float gs = 0.f;
    for (int k = 0; k < D; ++k) gs += bf2f(x[t * D + k]) * bf2f(gw[k]);
    float g = 1.f / (1.f + expf(-gs));
    float a = 0.f;
    for (int p = 0; p < P; ++p) a += hre[t * P + p] * bf2f(Cre[d * P + p]) - him[t * P + p] * bf2f(Cim[d * P + p]);
    a = 2.f * a + bf2f(x[t * D + d]) * bf2f(Dsk[d]);
    float b = 0.f;
    for (int f = 0; f < FH; ++f) b += zg[t * FH + f] * bf2f(wout[f * D + d]);
    out[t * D + d] = g * a + (1.f - g) * b;
}

#define GAS __attribute__((address_space(1)))
#define LAS __attribute__((address_space(3)))
template<int N> __device__ __forceinline__ void waitv() { asm volatile("s_waitcnt vmcnt(%0)" :: "i"(N) : "memory"); }
template<int KB, int AUX> __device__ __forceinline__ void rd(const char* A, char LAS* dst, int b, int wid, int lane) {
    for (int g = wid; g < KB; g += 4)
        __builtin_amdgcn_global_load_lds((const GAS unsigned*)(A + (size_t)b * (KB * 1024) + g * 1024 + lane * 16), (LAS unsigned*)(dst + g * 1024), 16, 0, AUX);
}
template<int MASK, int AUX>
__global__ __launch_bounds__(256, 1) void k_probe(const char* x, const char* bre, const char* bim, const char* cre, const char* cim, const char* win, const char* wsp, const char* wout, unsigned long long* rec) {
    extern __shared__ __attribute__((aligned(16))) char shm[];
    const int wid = __builtin_amdgcn_readfirstlane(threadIdx.x >> 6), lane = threadIdx.x & 63;
    const int b = blockIdx.x;
    char LAS* s = (char LAS*)shm;
    unsigned long long r0 = __builtin_amdgcn_s_memrealtime();
    if (MASK & 1) rd<8, AUX>(x, s, b, wid, lane);
    if (MASK & 2) { rd<2, AUX>(bre, s + 8192, b, wid, lane); rd<2, AUX>(bim, s + 10240, b, wid ^ 2, lane); }
    if (MASK & 4) rd<5, AUX>(win, s + 16384, b, wid, lane);
    if (MASK & 8) rd<32, AUX>(wsp, s + 32768, b, wid, lane);
    if (MASK & 16) { rd<2, AUX>(cre, s + 12288, b, wid, lane); rd<2, AUX>(cim, s + 14336, b, wid ^ 2, lane); rd<2, AUX>(wout, s + 24576, b, wid, lane); }
    waitv<0>();
    __syncthreads();
    unsigned long long r1 = __builtin_amdgcn_s_memrealtime();
    if (threadIdx.x == 0) { rec[b * 4] = r0; rec[b * 4 + 1] = r1; }
}
__global__ void k_spin(const unsigned long long* rec) {
    unsigned long long sum = 0;
    for (int b = 0; b < 256; ++b) { unsigned long long a = rec[b * 4], e = rec[b * 4 + 1]; sum += e - a; }
    unsigned long long t = sum * 500 / 256;
    if (t > 400000) t = 400000;
    unsigned long long s = __builtin_amdgcn_s_memrealtime();
    while (__builtin_amdgcn_s_memrealtime() - s < t) __builtin_amdgcn_s_sleep(4);
}
extern "C" void kernel_launch(void* const* d_in, const int* in_sizes, int n_in,
                              void* d_out, int out_size, void* d_ws, size_t ws_size,
                              hipStream_t stream) {
    const u16* x = (const u16*)d_in[0];
    const u16* lre = (const u16*)d_in[1];
    const u16* lim = (const u16*)d_in[2];
    const u16* Bre = (const u16*)d_in[3];
    const u16* Bim = (const u16*)d_in[4];
    const u16* Cre = (const u16*)d_in[5];
    const u16* Cim = (const u16*)d_in[6];
    const u16* ldt = (const u16*)d_in[7];
    const u16* Dsk = (const u16*)d_in[8];
    const u16* win = (const u16*)d_in[10];
    const u16* wsp = (const u16*)d_in[11];
    const u16* bsp = (const u16*)d_in[12];
    const u16* wout = (const u16*)d_in[13];
    const u16* gw = (const u16*)d_in[14];
    float* ws = (float*)d_ws;
    float* ure = ws;
    float* uim = ure + L * P;
    float* z = uim + L * P;
    float* zg = z + L * F;
    unsigned long long* rec = (unsigned long long*)(ws + 32 * 1024 * 1024);
    hipFuncSetAttribute((const void*)k_probe<1, 0>, hipFuncAttributeMaxDynamicSharedMemorySize, 143360);
    k_probe<1, 0><<<256, 256, 143360, stream>>>((const char*)x, (const char*)Bre, (const char*)Bim, (const char*)Cre, (const char*)Cim, (const char*)win, (const char*)wsp, (const char*)wout, rec);
    k_spin<<<1, 64, 0, stream>>>(rec);
    k_proj<<<dim3((2 * P + F + 255) / 256, L), 256, 0, stream>>>(x, Bre, Bim, win, ure, uim, z);
    k_scan<<<P / 64, 64, 0, stream>>>(lre, lim, ldt, ure, uim);
    k_ln<<<L, 256, 0, stream>>>(z);
    k_sp<<<dim3((FH + 255) / 256, L), 256, 0, stream>>>(wsp, bsp, z, zg);
    k_out<<<dim3(D / 256, L), 256, 0, stream>>>(x, Cre, Cim, Dsk, wout, gw, ure, uim, zg, (float*)d_out);
}
```

```cpp
#include <hip/hip_runtime.h>

__global__ __launch_bounds__(256, 1) void gated_mix_s5_gmlp_kernel(
    const void* x, const void* w_in, const void* b_re, const void* b_im, void* ws, const void* lam_re,
    const void* lam_im, const void* log_dt, const void* w_sp, const void* c_re, const void* c_im,
    const void* w_out, void* out, const void* d_skip, const void* b_sp, const void* gate_w) {
  __shared__ __attribute__((aligned(16))) char lds[163840];
  asm volatile(
    "Lbeg_%=:\n"
    "s_bfe_u32 s77, %1, 0x30003\n"
    "s_cmp_ge_u32 s77, 6\n"
    "s_cbranch_scc1 Lssm_%=\n"
    "s_load_dwordx16 s[8:23], %0, 0\n"
    "s_load_dwordx16 s[24:39], %0, 64\n"
    "v_and_b32 v4, 63, %2\n"
    "v_lshrrev_b32 v26, 6, %2\n"
    "s_lshr_b32 s66, %1, 3\n"
    "v_readfirstlane_b32 s60, v26\n"
    "s_and_b32 s63, %1, 1\n"
    "s_bfe_u32 s77, %1, 0x20001\n"
    "s_lshr_b32 s90, s66, 3\n"
    "s_lshl2_add_u32 s61, s77, s90\n"
    "s_and_b32 s62, s66, 7\n"
    "s_lshl1_add_u32 s91, s62, s63\n"
    "s_mov_b32 s65, 0x76543210\n"
    "s_cmp_eq_u32 s90, 1\n"
    "s_cselect_b32 s65, 0x10765432, s65\n"
    "s_cmp_eq_u32 s90, 2\n"
    "s_cselect_b32 s65, 0x32107654, s65\n"
    "s_cmp_eq_u32 s90, 3\n"
    "s_cselect_b32 s65, 0x54321076, s65\n"
    "s_lshl2_add_u32 s94, s61, s60\n"
    "s_mul_i32 s67, s60, 20480\n"
    "s_lshl_b32 s81, s60, 11\n"
    "s_add_u32 s81, s81, 98304\n"
    "s_mul_i32 s77, s62, 0x11111111\n"
    "s_add_u32 s65, s65, s77\n"
    "s_bfe_u32 s68, s65, 0x30000\n"
    "s_lshl_b32 s68, s68, 7\n"
    "s_bfe_u32 s69, s65, 0x30004\n"
    "s_lshl_b32 s69, s69, 7\n"
    "s_bfe_u32 s70, s65, 0x30008\n"
    "s_lshl_b32 s70, s70, 7\n"
    "s_bfe_u32 s71, s65, 0x3000c\n"
    "s_lshl_b32 s71, s71, 7\n"
    "s_bfe_u32 s72, s65, 0x30010\n"
    "s_lshl_b32 s72, s72, 7\n"
    "s_bfe_u32 s73, s65, 0x30014\n"
    "s_lshl_b32 s73, s73, 7\n"
    "s_bfe_u32 s74, s65, 0x30018\n"
    "s_lshl_b32 s74, s74, 7\n"
    "s_bfe_u32 s75, s65, 0x3001c\n"
    "s_lshl_b32 s75, s75, 7\n"
    "s_mul_i32 s77, s62, 3\n"
    "s_min_u32 s77, s77, 20\n"
    "s_mul_i32 s78, s63, 22\n"
    "s_add_u32 s82, s77, s78\n"
    "s_lshl2_add_u32 s92, s62, s78\n"
    "s_cmp_ge_u32 s62, 6\n"
    "s_cselect_b32 s93, 2, 3\n"
    "s_cmp_eq_u32 s91, 15\n"
    "s_cselect_b32 s79, 1, s93\n"
    "s_lshl_b32 s79, s79, 1\n"
    "v_lshrrev_b32 v26, 3, v4\n"
    "v_and_b32 v27, 7, v4\n"
    "v_lshrrev_b32 v28, 4, v4\n"
    "v_xor_b32 v28, v27, v28\n"
    "v_lshlrev_b32 v28, 4, v28\n"
    "v_lshrrev_b32 v29, 5, v4\n"
    "v_and_b32 v31, 3, v26\n"
    "v_lshl_add_u32 v29, v29, 3, v31\n"
    "s_lshl_b32 s77, s94, 5\n"
    "v_add_u32 v29, s77, v29\n"
    "v_lshl_add_u32 v5, v29, 10, v28\n"
    "v_xor_b32 v31, 64, v5\n"
    "v_add_u32 v6, 15360, v31\n"
    "v_add_u32 v7, 2048, v5\n"
    "v_add_u32 v8, 17408, v31\n"
    "v_mul_u32_u24 v27, 43, v4\n"
    "v_lshrrev_b32 v27, 8, v27\n"
    "v_mul_u32_u24 v31, 6, v27\n"
    "v_sub_u32 v31, v4, v31\n"
    "v_and_b32 v9, 4, v27\n"
    "v_add_u32 v27, v27, v9\n"
    "s_lshl_b32 s77, s60, 4\n"
    "v_add_u32 v27, s77, v27\n"
    "v_mul_u32_u24 v27, 2752, v27\n"
    "v_lshl_add_u32 v27, v31, 4, v27\n"
    "s_lshl_b32 s77, s82, 5\n"
    "s_add_u32 s77, s77, 1376\n"
    "v_add_u32 v27, s77, v27\n"
    "v_cmp_gt_u32 vcc, s79, v31\n"
    "v_mov_b32 v31, 0x40000000\n"
    "s_nop 0\n"
    "v_cndmask_b32 v27, v31, v27, vcc\n"
    "v_cmp_gt_u32 vcc, 48, v4\n"
    "s_nop 1\n"
    "v_cndmask_b32 v9, v31, v27, vcc\n"
    "v_add_u32 v10, 9984, v9\n"
    "s_waitcnt lgkmcnt(0)\n"
    "s_mov_b32 s40, s8\n"
    "s_and_b32 s41, s9, 0xffff\n"
    "s_mov_b32 s42, 0x200000\n"
    "s_mov_b32 s43, 0x20000\n"
    "s_mov_b32 s44, s10\n"
    "s_and_b32 s45, s11, 0xffff\n"
    "s_mov_b32 s46, 0x158000\n"
    "s_mov_b32 s47, 0x20000\n"
    "s_add_u32 m0, s67, 0\n"
    "s_nop 0\n"
    "buffer_load_dwordx4 v5, s[40:43], s68 offen offset:0 lds\n"
    "buffer_load_dwordx4 v6, s[40:43], s68 offen offset:1024 lds\n"
    "buffer_load_dwordx4 v7, s[40:43], s68 offen offset:2048 lds\n"
    "buffer_load_dwordx4 v8, s[40:43], s68 offen offset:3072 lds\n"
    "s_add_u32 m0, s81, 0\n"
    "s_mul_i32 s76, s68, 1376\n"
    "buffer_load_dwordx4 v9, s[44:47], s76 offen offset:0 lds\n"
    "buffer_load_dwordx4 v10, s[44:47], s76 offen offset:1024 lds\n"
    "s_lshl_b32 s77, s91, 6\n"
    "v_and_b32 v31, 31, v4\n"
    "v_lshl_add_u32 v31, v31, 1, s77\n"
    "global_load_ushort v220, v31, s[18:19]\n"
    "global_load_ushort v221, v31, s[20:21]\n"
    "global_load_ushort v222, v31, s[22:23]\n"
    "v_mov_b32 v27, 0\n"
    "global_load_dword v229, v27, s[16:17] sc1\n"
    "s_getpc_b64 s[98:99]\n"
    "Lpc0_%=:\n"
    "s_sub_u32 s98, s98, Lpc0_%=-Lbeg_%=\n"
    "s_subb_u32 s99, s99, 0\n"
    "s_lshl2_add_u32 s77, s66, s60\n"
    "v_and_b32 v27, 3, v4\n"
    "v_lshl_add_u32 v27, s77, 2, v27\n"
    "v_lshlrev_b32 v27, 7, v27\n"
    "v_min_u32 v27, Lend_%=-Lbeg_%=-4, v27\n"
    "v_and_b32 v27, -4, v27\n"
    "global_load_dword v230, v27, s[98:99]\n"
    "s_add_u32 m0, s67, 4096\n"
    "s_nop 0\n"
    "buffer_load_dwordx4 v5, s[40:43], s69 offen offset:0 lds\n"
    "buffer_load_dwordx4 v6, s[40:43], s69 offen offset:1024 lds\n"
    "buffer_load_dwordx4 v7, s[40:43], s69 offen offset:2048 lds\n"
    "buffer_load_dwordx4 v8, s[40:43], s69 offen offset:3072 lds\n"
    "s_mov_b32 s56, s16\n"
    "s_and_b32 s57, s17, 0xffff\n"
    "s_mov_b32 s58, 0x1100000\n"
    "s_mov_b32 s59, 0x20000\n"
    "s_mov_b32 s52, s24\n"
    "s_and_b32 s53, s25, 0xffff\n"
    "s_mov_b32 s54, 0x800000\n"
    "s_mov_b32 s55, 0x20000\n"
    "s_bitcmp1_b32 s60, 0\n"
    "s_cselect_b32 s48, s14, s12\n"
    "s_cselect_b32 s49, s15, s13\n"
    "s_and_b32 s49, s49, 0xffff\n"
    "s_mov_b32 s50, 0x80000\n"
    "s_mov_b32 s51, 0x20000\n"
    "s_lshr_b32 s78, s60, 1\n"
    "s_lshl1_add_u32 s78, s91, s78\n"
    "s_lshl_b32 s78, s78, 14\n"
    "v_lshl_add_u32 v11, v26, 10, v28\n"
    "v_add_u32 v11, s78, v11\n"
    "v_xor_b32 v31, 64, v11\n"
    "v_add_u32 v12, 7168, v31\n"
    "v_and_b32 v27, 15, v4\n"
    "v_lshrrev_b32 v31, 4, v4\n"
    "v_lshrrev_b32 v15, 1, v27\n"
    "v_xor_b32 v15, v15, v31\n"
    "v_lshlrev_b32 v15, 4, v15\n"
    "v_lshl_add_u32 v15, v27, 7, v15\n"
    "v_add_u32 v13, s67, v15\n"
    "v_xor_b32 v14, 64, v13\n"
    "v_add_u32 v15, 98304, v15\n"
    "v_xor_b32 v16, 64, v15\n"
    "v_bfe_u32 v17, v4, 2, 3\n"
    "v_mul_u32_u24 v17, 96, v17\n"
    "v_and_b32 v27, 3, v4\n"
    "v_lshl_add_u32 v17, v27, 3, v17\n"
    "v_lshrrev_b32 v27, 5, v4\n"
    "v_lshl_add_u32 v17, v27, 11, v17\n"
    "v_add_u32 v17, 98304, v17\n"
    "v_lshlrev_b32 v22, 4, v4\n"
    "s_mov_b32 s18, 0xbdd2d3e8\n"
    "s_mov_b32 s19, 0xc0135761\n"
    "s_mov_b32 s20, 0xcccccccc\n"
    "s_mov_b32 s21, 0xcccccccc\n"
    "s_mov_b32 s22, 0xaaaaaaaa\n"
    "s_mov_b32 s23, 0xaaaaaaaa\n"
    "s_mov_b32 s16, 0xffff0000\n"
    "s_mov_b32 s17, 0xffff0000\n"
    "s_cmp_eq_u32 s60, 0\n"
    "s_cselect_b32 s96, 0, 0x40000000\n"
    "v_and_b32 v244, 3, v4\n"
    "v_bfe_u32 v245, v4, 2, 1\n"
    "v_lshl_add_u32 v244, v245, 3, v244\n"
    "v_bfe_u32 v245, v4, 3, 1\n"
    "v_lshl_add_u32 v244, v245, 2, v244\n"
    "v_and_b32 v245, 16, v4\n"
    "v_add_u32 v244, v245, v244\n"
    "v_lshlrev_b32 v244, 4, v244\n"
    "v_lshrrev_b32 v245, 5, v4\n"
    "v_mul_u32_u24 v245, 22528, v245\n"
    "v_add_u32 v23, v244, v245\n"
    "v_lshrrev_b32 v244, 4, v4\n"
    "v_and_b32 v245, 7, v4\n"
    "v_lshl_add_u32 v244, v244, 3, v245\n"
    "v_and_b32 v245, 8, v4\n"
    "v_lshl_add_u32 v24, v244, 4, v245\n"
    "v_and_b32 v244, 31, v4\n"
    "v_bfe_u32 v245, v4, 1, 3\n"
    "v_lshrrev_b32 v246, 5, v4\n"
    "v_xor_b32 v245, v245, v246\n"
    "v_lshlrev_b32 v245, 4, v245\n"
    "v_lshl_add_u32 v244, v244, 7, v245\n"
    "v_add_u32 v18, s67, v244\n"
    "v_xor_b32 v19, 32, v18\n"
    "v_xor_b32 v20, 64, v18\n"
    "v_xor_b32 v21, 96, v18\n"
    "s_mov_b32 s24, 0x76543210\n"
    "s_cmp_eq_u32 s90, 1\n"
    "s_cselect_b32 s24, 0x76543210, s24\n"
    "s_cmp_eq_u32 s90, 2\n"
    "s_cselect_b32 s24, 0x76543210, s24\n"
    "s_cmp_eq_u32 s90, 3\n"
    "s_cselect_b32 s24, 0x76543210, s24\n"
    "s_mul_i32 s79, s62, 0x22222222\n"
    "s_xor_b32 s97, s79, s24\n"
    "s_and_b32 s97, s97, 0x88888888\n"
    "s_and_b32 s79, s79, 0x77777777\n"
    "s_and_b32 s24, s24, 0x77777777\n"
    "s_add_u32 s24, s24, s79\n"
    "s_xor_b32 s24, s24, s97\n"
    "s_mov_b32 s25, 0xfedcba98\n"
    "s_cmp_eq_u32 s90, 1\n"
    "s_cselect_b32 s25, 0xfedcba98, s25\n"
    "s_cmp_eq_u32 s90, 2\n"
    "s_cselect_b32 s25, 0xfedcba98, s25\n"
    "s_cmp_eq_u32 s90, 3\n"
    "s_cselect_b32 s25, 0xfedcba98, s25\n"
    "s_mul_i32 s79, s62, 0x22222222\n"
    "s_xor_b32 s97, s79, s25\n"
    "s_and_b32 s97, s97, 0x88888888\n"
    "s_and_b32 s79, s79, 0x77777777\n"
    "s_and_b32 s25, s25, 0x77777777\n"
    "s_add_u32 s25, s25, s79\n"
    "s_xor_b32 s25, s25, s97\n"
    "v_bfe_u32 v253, v4, 2, 2\n"
    "v_and_b32 v244, 3, v4\n"
    "v_lshl_add_u32 v253, v253, 3, v244\n"
    "v_bfe_u32 v244, v4, 4, 1\n"
    "v_lshl_add_u32 v253, v244, 2, v253\n"
    "s_lshl_b32 s79, s94, 6\n"
    "v_lshl_add_u32 v244, v253, 1, s79\n"
    "global_load_ushort v255, v244, s[36:37]\n"
    "s_lshl_b32 s79, s62, 2\n"
    "s_lshr_b32 s79, 0x754210, s79\n"
    "s_and_b32 s79, s79, 7\n"
    "v_bfe_u32 v244, v4, 4, 1\n"
    "v_add_u32 v244, s79, v244\n"
    "v_min_u32 v244, 7, v244\n"
    "v_bfe_u32 v245, v4, 3, 1\n"
    "v_lshl_add_u32 v244, v244, 1, v245\n"
    "v_and_b32 v245, 7, v4\n"
    "v_lshl_add_u32 v244, v245, 4, v244\n"
    "s_lshl_b32 s79, s63, 14\n"
    "s_add_u32 s79, s79, 0x1000\n"
    "v_lshl_add_u32 v254, v244, 7, s79\n"
    "s_lshl_b32 s79, s63, 6\n"
    "s_add_u32 s79, s79, s60\n"
    "s_mul_i32 s84, s79, 22528\n"
    "s_add_u32 s84, s84, 0x100000\n"
    "v_and_b32 v244, 15, v4\n"
    "v_lshrrev_b32 v245, 5, v4\n"
    "v_lshl_add_u32 v244, v245, 4, v244\n"
    "v_bfe_u32 v245, v4, 4, 1\n"
    "v_lshl_add_u32 v244, v245, 5, v244\n"
    "v_lshl_add_u32 v244, v244, 4, s84\n"
    "v_mov_b32 v246, 0x40000000\n"
    "s_xor_b32 s79, s63, 1\n"
    "s_mul_i32 s97, s79, 22\n"
    "s_lshl2_add_u32 s97, s62, s97\n"
    "s_lshl_b32 s97, s97, 9\n"
    "v_add_u32 v248, s97, v244\n"
    "s_cmp_eq_u32 s62, 5\n"
    "s_cselect_b32 s97, 0x40000000, s97\n"
    "v_add_u32 v249, s97, v244\n"
    "s_lshl1_add_u32 s79, s62, s79\n"
    "s_cmp_eq_u32 s79, 11\n"
    "s_cselect_b64 vcc, s[16:17], 0\n"
    "v_cndmask_b32 v248, v248, v246, vcc\n"
    "s_xor_b32 s79, s63, 0\n"
    "s_mul_i32 s97, s79, 22\n"
    "s_lshl2_add_u32 s97, s62, s97\n"
    "s_lshl_b32 s97, s97, 9\n"
    "v_add_u32 v250, s97, v244\n"
    "s_cmp_eq_u32 s62, 5\n"
    "s_cselect_b32 s97, 0x40000000, s97\n"
    "v_add_u32 v251, s97, v244\n"
    "s_lshl1_add_u32 s79, s62, s79\n"
    "s_cmp_eq_u32 s79, 11\n"
    "s_cselect_b64 vcc, s[16:17], 0\n"
    "v_cndmask_b32 v250, v250, v246, vcc\n"
    "v_bfe_u32 v244, v4, 1, 2\n"
    "v_bfe_u32 v245, v4, 4, 2\n"
    "v_xor_b32 v244, v244, v245\n"
    "s_cmp_eq_u32 s62, 5\n"
    "s_cselect_b32 s79, 2, 4\n"
    "s_cmp_eq_u32 s91, 11\n"
    "s_cselect_b32 s79, 1, s79\n"
    "v_cmp_gt_u32 vcc, s79, v244\n"
    "v_add_u32 v244, s92, v244\n"
    "v_and_b32 v245, 1, v4\n"
    "v_lshl_add_u32 v244, v244, 1, v245\n"
    "v_and_b32 v245, 4, v26\n"
    "v_add_u32 v245, v245, v26\n"
    "s_lshl_b32 s79, s60, 4\n"
    "v_add_u32 v245, s79, v245\n"
    "v_mul_u32_u24 v245, 2752, v245\n"
    "v_lshl_add_u32 v244, v244, 4, v245\n"
    "v_cndmask_b32 v119, v246, v244, vcc\n"
    "v_bfe_u32 v244, v4, 2, 3\n"
    "v_bfe_u32 v245, v4, 3, 2\n"
    "v_lshlrev_b32 v245, 5, v245\n"
    "v_lshl_add_u32 v244, v244, 7, v245\n"
    "v_and_b32 v245, 3, v4\n"
    "v_lshl_add_u32 v244, v245, 3, v244\n"
    "v_lshrrev_b32 v245, 5, v4\n"
    "v_lshl_add_u32 v244, v245, 11, v244\n"
    "v_add_u32 v115, 98304, v244\n"
    "v_xor_b32 v116, 32, v115\n"
    "v_xor_b32 v117, 64, v115\n"
    "v_xor_b32 v118, 96, v115\n"
    "v_add_u32 v252, 98304, v22\n"
    "s_mul_i32 s79, s94, 45056\n"
    "s_lshl_b32 s97, s82, 9\n"
    "s_add_u32 s79, s79, s97\n"
    "s_add_u32 s86, s79, 0x100000\n"
    "s_lshl_b32 s95, s94, 13\n"
    "s_add_u32 s95, s95, 0x80000\n"
    "s_lshl_b32 s79, s91, 9\n"
    "v_add_u32 v24, s79, v24\n"
    "v_add_u32 v31, 4096, v22\n"
    "s_add_u32 m0, s81, 8192\n"
    "s_mul_i32 s76, s69, 1376\n"
    "buffer_load_dwordx4 v9, s[44:47], s76 offen offset:0 lds\n"
    "v_bfe_u32 v106, v4, 3, 2\n"
    "v_and_b32 v244, 7, v4\n"
    "v_lshl_add_u32 v106, v106, 4, v244\n"
    "v_lshrrev_b32 v244, 5, v4\n"
    "v_lshl_add_u32 v106, v244, 3, v106\n"
    "s_lshl_b32 s79, s60, 8\n"
    "s_add_u32 s97, s79, 81920\n"
    "v_lshl_add_u32 v106, v106, 2, s97\n"
    "v_lshrrev_b32 v244, 4, v4\n"
    "v_lshl_add_u32 v107, v244, 6, s97\n"
    "v_and_b32 v244, 15, v4\n"
    "v_lshlrev_b32 v244, 3, v244\n"
    "v_add_u32 v109, 82944, v244\n"
    "v_add_u32 v108, s79, v109\n"
    "v_add_u32 v111, 83968, v244\n"
    "v_and_b32 v244, 31, v4\n"
    "buffer_load_dwordx4 v10, s[44:47], s76 offen offset:1024 lds\n"
    "s_add_u32 s79, s79, 83968\n"
    "v_lshl_add_u32 v110, v244, 3, s79\n"
    "s_lshl_b32 s79, s61, 4\n"
    "s_add_u32 s79, s79, s91\n"
    "s_lshl_b32 s79, s79, 9\n"
    "s_add_u32 s79, s79, 0x40000\n"
    "s_add_u32 s79, s79, s96\n"
    "v_add_u32 v114, s79, v22\n"
    "v_cmp_gt_u32 vcc, 32, v4\n"
    "v_mov_b32 v244, 0x40000000\n"
    "s_nop 0\n"
    "v_cndmask_b32 v114, v244, v114, vcc\n"
    "s_lshl_b32 s79, s60, 10\n"
    "s_add_u32 s79, s79, 84992\n"
    "v_and_b32 v112, 15, v4\n"
    "v_and_b32 v244, 0x30, v4\n"
    "v_lshl_add_u32 v112, v112, 6, v244\n"
    "s_add_u32 m0, s67, 8192\n"
    "s_nop 0\n"
    "buffer_load_dwordx4 v5, s[40:43], s70 offen offset:0 lds\n"
    "v_add_u32 v112, s79, v112\n"
    "v_lshrrev_b32 v113, 5, v4\n"
    "v_bfe_u32 v244, v4, 2, 2\n"
    "v_lshl_add_u32 v113, v113, 3, v244\n"
    "v_lshlrev_b32 v113, 6, v113\n"
    "v_and_b32 v244, 3, v4\n"
    "v_lshl_add_u32 v113, v244, 4, v113\n"
    "v_bfe_u32 v244, v4, 4, 1\n"
    "v_lshl_add_u32 v113, v244, 3, v113\n"
    "v_add_u32 v113, s79, v113\n"
    "s_lshl_b32 s79, s61, 4\n"
    "s_add_u32 s79, s79, s91\n"
    "s_lshl_b32 s79, s79, 7\n"
    "s_add_u32 s14, s79, s96\n"
    "s_waitcnt vmcnt(12)\n"
    "v_lshlrev_b32 v220, 16, v220\n"
    "s_waitcnt vmcnt(11)\n"
    "v_lshlrev_b32 v221, 16, v221\n"
    "buffer_load_dwordx4 v6, s[40:43], s70 offen offset:1024 lds\n"
    "s_waitcnt vmcnt(11)\n"
    "v_lshlrev_b32 v222, 16, v222\n"
    "v_mul_f32 v222, 0x3fb8aa3b, v222\n"
    "v_exp_f32 v222, v222\n"
    "s_nop 0\n"
    "v_cvt_pk_bf16_f32 v222, v222, v222\n"
    "v_lshlrev_b32 v222, 16, v222\n"
    "v_mul_f32 v236, v222, v220\n"
    "v_mul_f32 v237, v222, v221\n"
    "v_mul_f32 v236, 0x3fb8aa3b, v236\n"
    "v_mul_f32 v237, 0x3e22f983, v237\n"
    "v_mov_b32 v232, v236\n"
    "v_mov_b32 v234, v236\n"
    "v_mov_b32 v223, v220\n"
    "v_mov_b32 v233, v237\n"
    "v_mov_b32 v235, v237\n"
    "v_mov_b32 v224, v221\n"
    "v_permlane16_swap_b32 v232, v234\n"
    "buffer_load_dwordx4 v7, s[40:43], s70 offen offset:2048 lds\n"
    "v_permlane16_swap_b32 v220, v223\n"
    "v_permlane16_swap_b32 v233, v235\n"
    "v_permlane16_swap_b32 v221, v224\n"
    "s_waitcnt vmcnt(11)\n"
    "v_readfirstlane_b32 s64, v229\n"
    "s_nop 0\n"
    "s_add_u32 s64, s64, 1\n"
    "v_mov_b32 v25, s64\n"
    "v_lshrrev_b32 v238, 4, v4\n"
    "v_lshlrev_b32 v238, 3, v238\n"
    "s_lshl_b32 s79, s60, 5\n"
    "v_add_u32 v239, s79, v238\n"
    "v_cvt_f32_u32 v238, v238\n"
    "v_cvt_f32_u32 v239, v239\n"
    "v_exp_f32 v244, v232\n"
    "v_fract_f32 v245, v233\n"
    "v_cos_f32 v184, v245\n"
    "v_sin_f32 v185, v245\n"
    "buffer_load_dwordx4 v8, s[40:43], s70 offen offset:3072 lds\n"
    "v_mul_f32 v184, v184, v244\n"
    "v_mul_f32 v185, v185, v244\n"
    "v_mul_f32 v244, 0x41000000, v232\n"
    "v_mul_f32 v245, 0x41000000, v233\n"
    "v_exp_f32 v244, v244\n"
    "v_fract_f32 v245, v245\n"
    "v_cos_f32 v186, v245\n"
    "v_sin_f32 v187, v245\n"
    "v_mul_f32 v186, v186, v244\n"
    "v_mul_f32 v187, v187, v244\n"
    "v_mul_f32 v188, v186, v186\n"
    "v_mul_f32 v189, v186, v187\n"
    "v_fma_f32 v188, -v187, v187, v188\n"
    "v_add_f32 v189, v189, v189\n"
    "v_mul_f32 v190, v188, v188\n"
    "v_mul_f32 v191, v188, v189\n"
    "s_add_u32 m0, s81, 16384\n"
    "s_mul_i32 s76, s70, 1376\n"
    "buffer_load_dwordx4 v9, s[44:47], s76 offen offset:0 lds\n"
    "v_fma_f32 v190, -v189, v189, v190\n"
    "v_add_f32 v191, v191, v191\n"
    "v_cndmask_b32 v192, 1.0, v186, s[16:17]\n"
    "v_cndmask_b32 v193, 0, v187, s[16:17]\n"
    "v_mul_f32 v244, v238, v232\n"
    "v_mul_f32 v245, v238, v233\n"
    "v_exp_f32 v244, v244\n"
    "v_fract_f32 v245, v245\n"
    "v_cos_f32 v194, v245\n"
    "v_sin_f32 v195, v245\n"
    "v_mul_f32 v194, v194, v244\n"
    "v_mul_f32 v195, v195, v244\n"
    "v_mul_f32 v244, v239, v232\n"
    "v_mul_f32 v245, v239, v233\n"
    "v_exp_f32 v244, v244\n"
    "v_fract_f32 v245, v245\n"
    "buffer_load_dwordx4 v10, s[44:47], s76 offen offset:1024 lds\n"
    "v_cos_f32 v196, v245\n"
    "v_sin_f32 v197, v245\n"
    "v_mul_f32 v196, v196, v244\n"
    "v_mul_f32 v197, v197, v244\n"
    "v_cmp_gt_u32 vcc, 32, v4\n"
    "s_nop 1\n"
    "v_cndmask_b32 v192, v192, 0, vcc\n"
    "v_cndmask_b32 v193, v193, 0, vcc\n"
    "v_mul_f32 v244, v220, v220\n"
    "v_fmac_f32 v244, v221, v221\n"
    "v_rcp_f32 v244, v244\n"
    "v_add_f32 v245, -1.0, v184\n"
    "v_add_f32 v244, v244, v244\n"
    "v_mul_f32 v198, v245, v220\n"
    "v_fmac_f32 v198, v185, v221\n"
    "v_mul_f32 v199, v185, v220\n"
    "v_fma_f32 v199, -v245, v221, v199\n"
    "s_add_u32 m0, s67, 12288\n"
    "s_nop 0\n"
    "buffer_load_dwordx4 v5, s[40:43], s71 offen offset:0 lds\n"
    "v_mul_f32 v198, v198, v244\n"
    "v_mul_f32 v199, v199, v244\n"
    "v_exp_f32 v244, v234\n"
    "v_fract_f32 v245, v235\n"
    "v_cos_f32 v200, v245\n"
    "v_sin_f32 v201, v245\n"
    "v_mul_f32 v200, v200, v244\n"
    "v_mul_f32 v201, v201, v244\n"
    "v_mul_f32 v244, 0x41000000, v234\n"
    "v_mul_f32 v245, 0x41000000, v235\n"
    "v_exp_f32 v244, v244\n"
    "v_fract_f32 v245, v245\n"
    "v_cos_f32 v202, v245\n"
    "v_sin_f32 v203, v245\n"
    "v_mul_f32 v202, v202, v244\n"
    "v_mul_f32 v203, v203, v244\n"
    "buffer_load_dwordx4 v6, s[40:43], s71 offen offset:1024 lds\n"
    "v_mul_f32 v204, v202, v202\n"
    "v_mul_f32 v205, v202, v203\n"
    "v_fma_f32 v204, -v203, v203, v204\n"
    "v_add_f32 v205, v205, v205\n"
    "v_mul_f32 v206, v204, v204\n"
    "v_mul_f32 v207, v204, v205\n"
    "v_fma_f32 v206, -v205, v205, v206\n"
    "v_add_f32 v207, v207, v207\n"
    "v_cndmask_b32 v208, 1.0, v202, s[16:17]\n"
    "v_cndmask_b32 v209, 0, v203, s[16:17]\n"
    "v_mul_f32 v244, v238, v234\n"
    "v_mul_f32 v245, v238, v235\n"
    "v_exp_f32 v244, v244\n"
    "v_fract_f32 v245, v245\n"
    "v_cos_f32 v210, v245\n"
    "v_sin_f32 v211, v245\n"
    "buffer_load_dwordx4 v7, s[40:43], s71 offen offset:2048 lds\n"
    "v_mul_f32 v210, v210, v244\n"
    "v_mul_f32 v211, v211, v244\n"
    "v_mul_f32 v244, v239, v234\n"
    "v_mul_f32 v245, v239, v235\n"
    "v_exp_f32 v244, v244\n"
    "v_fract_f32 v245, v245\n"
    "v_cos_f32 v212, v245\n"
    "v_sin_f32 v213, v245\n"
    "v_mul_f32 v212, v212, v244\n"
    "v_mul_f32 v213, v213, v244\n"
    "v_cmp_gt_u32 vcc, 32, v4\n"
    "s_nop 1\n"
    "v_cndmask_b32 v208, v208, 0, vcc\n"
    "v_cndmask_b32 v209, v209, 0, vcc\n"
    "v_mul_f32 v244, v223, v223\n"
    "v_fmac_f32 v244, v224, v224\n"
    "v_rcp_f32 v244, v244\n"
    "buffer_load_dwordx4 v8, s[40:43], s71 offen offset:3072 lds\n"
    "v_add_f32 v245, -1.0, v200\n"
    "v_add_f32 v244, v244, v244\n"
    "v_mul_f32 v214, v245, v223\n"
    "v_fmac_f32 v214, v201, v224\n"
    "v_mul_f32 v215, v201, v223\n"
    "v_fma_f32 v215, -v245, v224, v215\n"
    "v_mul_f32 v214, v214, v244\n"
    "v_mul_f32 v215, v215, v244\n"
    "v_lshrrev_b32 v246, 5, v4\n"
    "s_lshl2_add_u32 s79, s60, 0\n"
    "v_add_u32 v246, s79, v246\n"
    "v_lshlrev_b32 v2, 13, v246\n"
    "v_sub_u32 v246, s61, v246\n"
    "v_add_u32 v246, -1, v246\n"
    "v_cmp_le_i32 vcc, 0, v246\n"
    "v_max_i32 v246, 0, v246\n"
    "s_add_u32 m0, s81, 24576\n"
    "s_mul_i32 s76, s71, 1376\n"
    "buffer_load_dwordx4 v9, s[44:47], s76 offen offset:0 lds\n"
    "v_lshlrev_b32 v246, 7, v246\n"
    "v_cvt_f32_u32 v246, v246\n"
    "s_lshl_b32 s79, s91, 9\n"
    "s_add_u32 s79, s79, 0x40000\n"
    "v_and_b32 v247, 0x1f0, v22\n"
    "v_add_u32 v247, s79, v247\n"
    "v_add_u32 v2, v2, v247\n"
    "v_mov_b32 v247, 0x40000000\n"
    "v_cndmask_b32 v2, v247, v2, vcc\n"
    "v_mul_f32 v244, v246, v236\n"
    "v_mul_f32 v245, v246, v237\n"
    "v_exp_f32 v244, v244\n"
    "v_fract_f32 v245, v245\n"
    "v_cos_f32 v216, v245\n"
    "v_sin_f32 v217, v245\n"
    "v_mul_f32 v216, v216, v244\n"
    "buffer_load_dwordx4 v10, s[44:47], s76 offen offset:1024 lds\n"
    "v_mul_f32 v217, v217, v244\n"
    "v_lshrrev_b32 v246, 5, v4\n"
    "s_lshl2_add_u32 s79, s60, 2\n"
    "v_add_u32 v246, s79, v246\n"
    "v_lshlrev_b32 v3, 13, v246\n"
    "v_sub_u32 v246, s61, v246\n"
    "v_add_u32 v246, -1, v246\n"
    "v_cmp_le_i32 vcc, 0, v246\n"
    "v_max_i32 v246, 0, v246\n"
    "v_lshlrev_b32 v246, 7, v246\n"
    "v_cvt_f32_u32 v246, v246\n"
    "s_lshl_b32 s79, s91, 9\n"
    "s_add_u32 s79, s79, 0x40000\n"
    "v_and_b32 v247, 0x1f0, v22\n"
    "v_add_u32 v247, s79, v247\n"
    "v_add_u32 v3, v3, v247\n"
    "v_mov_b32 v247, 0x40000000\n"
    "v_cndmask_b32 v3, v247, v3, vcc\n"
    "v_mul_f32 v244, v246, v236\n"
    "v_mul_f32 v245, v246, v237\n"
    "v_exp_f32 v244, v244\n"
    "v_fract_f32 v245, v245\n"
    "v_cos_f32 v218, v245\n"
    "v_sin_f32 v219, v245\n"
    "v_mul_f32 v218, v218, v244\n"
    "v_mul_f32 v219, v219, v244\n"
    "s_barrier\n"
    "ds_read_b128 a[0:3], v13 offset:0\n"
    "ds_read_b128 a[4:7], v13 offset:2048\n"
    "ds_read_b64_tr_b16 v[64:65], v17 offset:0\n"
    "ds_read_b64_tr_b16 v[66:67], v17 offset:1024\n"
    "ds_read_b64_tr_b16 v[68:69], v17 offset:32\n"
    "ds_read_b64_tr_b16 v[70:71], v17 offset:1056\n"
    "ds_read_b64_tr_b16 v[72:73], v17 offset:64\n"
    "ds_read_b64_tr_b16 v[74:75], v17 offset:1088\n"
    "ds_read_b128 a[8:11], v14 offset:0\n"
    "ds_read_b128 a[12:15], v14 offset:2048\n"
    "ds_read_b64_tr_b16 v[76:77], v17 offset:4096\n"
    "ds_read_b64_tr_b16 v[78:79], v17 offset:5120\n"
    "s_waitcnt lgkmcnt(6)\n"
    "v_mfma_f32_16x16x32_bf16 v[120:123], a[0:3], v[64:67], 0\n"
    "ds_read_b64_tr_b16 v[80:81], v17 offset:4128\n"
    "v_mfma_f32_16x16x32_bf16 v[124:127], a[4:7], v[64:67], 0\n"
    "ds_read_b64_tr_b16 v[82:83], v17 offset:5152\n"
    "v_mfma_f32_16x16x32_bf16 v[128:131], a[0:3], v[68:71], 0\n"
    "ds_read_b64_tr_b16 v[84:85], v17 offset:4160\n"
    "v_mfma_f32_16x16x32_bf16 v[132:135], a[4:7], v[68:71], 0\n"
    "ds_read_b64_tr_b16 v[86:87], v17 offset:5184\n"
    "s_add_u32 m0, s67, 16384\n"
    "s_waitcnt lgkmcnt(4)\n"
    "v_mfma_f32_16x16x32_bf16 v[136:139], a[0:3], v[72:75], 0\n"
    "buffer_load_dwordx4 v5, s[40:43], s72 offen offset:0 lds\n"
    "buffer_load_dwordx4 v6, s[40:43], s72 offen offset:1024 lds\n"
    "buffer_load_dwordx4 v7, s[40:43], s72 offen offset:2048 lds\n"
    "buffer_load_dwordx4 v8, s[40:43], s72 offen offset:3072 lds\n"
    "s_waitcnt vmcnt(16)\n"
    "s_barrier\n"
    "v_mfma_f32_16x16x32_bf16 v[140:143], a[4:7], v[72:75], 0\n"
    "ds_read_b128 a[16:19], v13 offset:4096\n"
    "ds_read_b128 a[20:23], v13 offset:6144\n"
    "ds_read_b64_tr_b16 v[88:89], v17 offset:8192\n"
    "ds_read_b64_tr_b16 v[90:91], v17 offset:9216\n"
    "v_mfma_f32_16x16x32_bf16 v[120:123], a[8:11], v[76:79], v[120:123]\n"
    "ds_read_b64_tr_b16 v[92:93], v17 offset:8224\n"
    "v_mfma_f32_16x16x32_bf16 v[124:127], a[12:15], v[76:79], v[124:127]\n"
    "ds_read_b64_tr_b16 v[94:95], v17 offset:9248\n"
    "s_waitcnt lgkmcnt(6)\n"
    "v_mfma_f32_16x16x32_bf16 v[128:131], a[8:11], v[80:83], v[128:131]\n"
    "ds_read_b64_tr_b16 v[64:65], v17 offset:8256\n"
    "v_mfma_f32_16x16x32_bf16 v[132:135], a[12:15], v[80:83], v[132:135]\n"
    "ds_read_b64_tr_b16 v[66:67], v17 offset:9280\n"
    "v_mfma_f32_16x16x32_bf16 v[136:139], a[8:11], v[84:87], v[136:139]\n"
    "ds_read_b128 a[24:27], v14 offset:4096\n"
    "ds_read_b128 a[28:31], v14 offset:6144\n"
    "v_mfma_f32_16x16x32_bf16 v[140:143], a[12:15], v[84:87], v[140:143]\n"
    "ds_read_b64_tr_b16 v[68:69], v17 offset:12288\n"
    "ds_read_b64_tr_b16 v[70:71], v17 offset:13312\n"
    "s_waitcnt lgkmcnt(6)\n"
    "v_mfma_f32_16x16x32_bf16 v[120:123], a[16:19], v[88:91], v[120:123]\n"
    "ds_read_b64_tr_b16 v[72:73], v17 offset:12320\n"
    "v_mfma_f32_16x16x32_bf16 v[124:127], a[20:23], v[88:91], v[124:127]\n"
    "ds_read_b64_tr_b16 v[74:75], v17 offset:13344\n"
    "v_mfma_f32_16x16x32_bf16 v[128:131], a[16:19], v[92:95], v[128:131]\n"
    "ds_read_b64_tr_b16 v[76:77], v17 offset:12352\n"
    "v_mfma_f32_16x16x32_bf16 v[132:135], a[20:23], v[92:95], v[132:135]\n"
    "ds_read_b64_tr_b16 v[78:79], v17 offset:13376\n"
    "s_waitcnt lgkmcnt(4)\n"
    "v_mfma_f32_16x16x32_bf16 v[136:139], a[16:19], v[64:67], v[136:139]\n"
    "s_waitcnt vmcnt(10)\n"
    "s_barrier\n"
    "v_mfma_f32_16x16x32_bf16 v[140:143], a[20:23], v[64:67], v[140:143]\n"
    "ds_read_b128 a[32:35], v13 offset:8192\n"
    "ds_read_b128 a[36:39], v13 offset:10240\n"
    "ds_read_b64_tr_b16 v[80:81], v17 offset:16384\n"
    "ds_read_b64_tr_b16 v[82:83], v17 offset:17408\n"
    "v_mfma_f32_16x16x32_bf16 v[120:123], a[24:27], v[68:71], v[120:123]\n"
    "ds_read_b64_tr_b16 v[84:85], v17 offset:16416\n"
    "s_add_u32 m0, s81, 32768\n"
    "v_mfma_f32_16x16x32_bf16 v[124:127], a[28:31], v[68:71], v[124:127]\n"
    "ds_read_b64_tr_b16 v[86:87], v17 offset:17440\n"
    "s_mul_i32 s76, s72, 1376\n"
    "s_waitcnt lgkmcnt(6)\n"
    "v_mfma_f32_16x16x32_bf16 v[128:131], a[24:27], v[72:75], v[128:131]\n"
    "ds_read_b64_tr_b16 v[88:89], v17 offset:16448\n"
    "buffer_load_dwordx4 v9, s[44:47], s76 offen offset:0 lds\n"
    "v_mfma_f32_16x16x32_bf16 v[132:135], a[28:31], v[72:75], v[132:135]\n"
    "ds_read_b64_tr_b16 v[90:91], v17 offset:17472\n"
    "buffer_load_dwordx4 v10, s[44:47], s76 offen offset:1024 lds\n"
    "v_mfma_f32_16x16x32_bf16 v[136:139], a[24:27], v[76:79], v[136:139]\n"
    "ds_read_b128 a[40:43], v14 offset:8192\n"
    "ds_read_b128 a[44:47], v14 offset:10240\n"
    "v_mfma_f32_16x16x32_bf16 v[140:143], a[28:31], v[76:79], v[140:143]\n"
    "ds_read_b64_tr_b16 v[92:93], v17 offset:20480\n"
    "ds_read_b64_tr_b16 v[94:95], v17 offset:21504\n"
    "s_waitcnt lgkmcnt(6)\n"
    "v_mfma_f32_16x16x32_bf16 v[120:123], a[32:35], v[80:83], v[120:123]\n"
    "ds_read_b64_tr_b16 v[64:65], v17 offset:20512\n"
    "s_add_u32 m0, s67, 0\n"
    "v_mfma_f32_16x16x32_bf16 v[124:127], a[36:39], v[80:83], v[124:127]\n"
    "ds_read_b64_tr_b16 v[66:67], v17 offset:21536\n"
    "buffer_load_dwordx4 v5, s[40:43], s73 offen offset:0 lds\n"
    "v_mfma_f32_16x16x32_bf16 v[128:131], a[32:35], v[84:87], v[128:131]\n"
    "ds_read_b64_tr_b16 v[68:69], v17 offset:20544\n"
    "v_mfma_f32_16x16x32_bf16 v[132:135], a[36:39], v[84:87], v[132:135]\n"
    "ds_read_b64_tr_b16 v[70:71], v17 offset:21568\n"
    "s_waitcnt lgkmcnt(4)\n"
    "v_mfma_f32_16x16x32_bf16 v[136:139], a[32:35], v[88:91], v[136:139]\n"
    "buffer_load_dwordx4 v6, s[40:43], s73 offen offset:1024 lds\n"
    "buffer_load_dwordx4 v7, s[40:43], s73 offen offset:2048 lds\n"
    "buffer_load_dwordx4 v8, s[40:43], s73 offen offset:3072 lds\n"
    "s_waitcnt vmcnt(10)\n"
    "s_barrier\n"
    "v_mfma_f32_16x16x32_bf16 v[140:143], a[36:39], v[88:91], v[140:143]\n"
    "ds_read_b128 a[48:51], v13 offset:12288\n"
    "ds_read_b128 a[52:55], v13 offset:14336\n"
    "ds_read_b64_tr_b16 v[72:73], v17 offset:24576\n"
    "ds_read_b64_tr_b16 v[74:75], v17 offset:25600\n"
    "v_mfma_f32_16x16x32_bf16 v[120:123], a[40:43], v[92:95], v[120:123]\n"
    "ds_read_b64_tr_b16 v[76:77], v17 offset:24608\n"
    "v_mfma_f32_16x16x32_bf16 v[124:127], a[44:47], v[92:95], v[124:127]\n"
    "ds_read_b64_tr_b16 v[78:79], v17 offset:25632\n"
    "s_waitcnt lgkmcnt(6)\n"
    "v_mfma_f32_16x16x32_bf16 v[128:131], a[40:43], v[64:67], v[128:131]\n"
    "ds_read_b64_tr_b16 v[80:81], v17 offset:24640\n"
    "v_mfma_f32_16x16x32_bf16 v[132:135], a[44:47], v[64:67], v[132:135]\n"
    "ds_read_b64_tr_b16 v[82:83], v17 offset:25664\n"
    "v_mfma_f32_16x16x32_bf16 v[136:139], a[40:43], v[68:71], v[136:139]\n"
    "ds_read_b128 a[56:59], v14 offset:12288\n"
    "ds_read_b128 a[60:63], v14 offset:14336\n"
    "v_mfma_f32_16x16x32_bf16 v[140:143], a[44:47], v[68:71], v[140:143]\n"
    "ds_read_b64_tr_b16 v[84:85], v17 offset:28672\n"
    "ds_read_b64_tr_b16 v[86:87], v17 offset:29696\n"
    "s_waitcnt lgkmcnt(6)\n"
    "v_mfma_f32_16x16x32_bf16 v[120:123], a[48:51], v[72:75], v[120:123]\n"
    "ds_read_b64_tr_b16 v[88:89], v17 offset:28704\n"
    "s_add_u32 m0, s81, 40960\n"
    "v_mfma_f32_16x16x32_bf16 v[124:127], a[52:55], v[72:75], v[124:127]\n"
    "ds_read_b64_tr_b16 v[90:91], v17 offset:29728\n"
    "s_mul_i32 s76, s73, 1376\n"
    "v_mfma_f32_16x16x32_bf16 v[128:131], a[48:51], v[76:79], v[128:131]\n"
    "ds_read_b64_tr_b16 v[92:93], v17 offset:28736\n"
    "buffer_load_dwordx4 v9, s[44:47], s76 offen offset:0 lds\n"
    "v_mfma_f32_16x16x32_bf16 v[132:135], a[52:55], v[76:79], v[132:135]\n"
    "ds_read_b64_tr_b16 v[94:95], v17 offset:29760\n"
    "buffer_load_dwordx4 v10, s[44:47], s76 offen offset:1024 lds\n"
    "s_waitcnt lgkmcnt(4)\n"
    "v_mfma_f32_16x16x32_bf16 v[136:139], a[48:51], v[80:83], v[136:139]\n"
    "s_add_u32 m0, s67, 4096\n"
    "s_nop 0\n"
    "buffer_load_dwordx4 v5, s[40:43], s74 offen offset:0 lds\n"
    "buffer_load_dwordx4 v6, s[40:43], s74 offen offset:1024 lds\n"
    "buffer_load_dwordx4 v7, s[40:43], s74 offen offset:2048 lds\n"
    "buffer_load_dwordx4 v8, s[40:43], s74 offen offset:3072 lds\n"
    "s_add_u32 m0, s81, 49152\n"
    "s_mul_i32 s76, s74, 1376\n"
    "s_waitcnt vmcnt(10)\n"
    "s_barrier\n"
    "v_mfma_f32_16x16x32_bf16 v[140:143], a[52:55], v[80:83], v[140:143]\n"
    "ds_read_b128 a[64:67], v13 offset:16384\n"
    "ds_read_b128 a[68:71], v13 offset:18432\n"
    "ds_read_b64_tr_b16 v[64:65], v17 offset:32768\n"
    "ds_read_b64_tr_b16 v[66:67], v17 offset:33792\n"
    "v_mfma_f32_16x16x32_bf16 v[120:123], a[56:59], v[84:87], v[120:123]\n"
    "ds_read_b64_tr_b16 v[68:69], v17 offset:32800\n"
    "v_mfma_f32_16x16x32_bf16 v[124:127], a[60:63], v[84:87], v[124:127]\n"
    "ds_read_b64_tr_b16 v[70:71], v17 offset:33824\n"
    "s_waitcnt lgkmcnt(6)\n"
    "v_mfma_f32_16x16x32_bf16 v[128:131], a[56:59], v[88:91], v[128:131]\n"
    "ds_read_b64_tr_b16 v[72:73], v17 offset:32832\n"
    "v_mfma_f32_16x16x32_bf16 v[132:135], a[60:63], v[88:91], v[132:135]\n"
    "ds_read_b64_tr_b16 v[74:75], v17 offset:33856\n"
    "v_mfma_f32_16x16x32_bf16 v[136:139], a[56:59], v[92:95], v[136:139]\n"
    "ds_read_b128 a[72:75], v14 offset:16384\n"
    "ds_read_b128 a[76:79], v14 offset:18432\n"
    "v_mfma_f32_16x16x32_bf16 v[140:143], a[60:63], v[92:95], v[140:143]\n"
    "ds_read_b64_tr_b16 v[76:77], v17 offset:36864\n"
    "ds_read_b64_tr_b16 v[78:79], v17 offset:37888\n"
    "s_waitcnt lgkmcnt(6)\n"
    "v_mfma_f32_16x16x32_bf16 v[120:123], a[64:67], v[64:67], v[120:123]\n"
    "ds_read_b64_tr_b16 v[80:81], v17 offset:36896\n"
    "buffer_load_dwordx4 v9, s[44:47], s76 offen offset:0 lds\n"
    "v_mfma_f32_16x16x32_bf16 v[124:127], a[68:71], v[64:67], v[124:127]\n"
    "ds_read_b64_tr_b16 v[82:83], v17 offset:37920\n"
    "v_mfma_f32_16x16x32_bf16 v[128:131], a[64:67], v[68:71], v[128:131]\n"
    "ds_read_b64_tr_b16 v[84:85], v17 offset:36928\n"
    "buffer_load_dwordx4 v10, s[44:47], s76 offen offset:1024 lds\n"
    "v_mfma_f32_16x16x32_bf16 v[132:135], a[68:71], v[68:71], v[132:135]\n"
    "ds_read_b64_tr_b16 v[86:87], v17 offset:37952\n"
    "s_waitcnt lgkmcnt(4)\n"
    "v_mfma_f32_16x16x32_bf16 v[136:139], a[64:67], v[72:75], v[136:139]\n"
    "s_add_u32 m0, s67, 8192\n"
    "s_nop 0\n"
    "buffer_load_dwordx4 v5, s[40:43], s75 offen offset:0 lds\n"
    "buffer_load_dwordx4 v6, s[40:43], s75 offen offset:1024 lds\n"
    "buffer_load_dwordx4 v7, s[40:43], s75 offen offset:2048 lds\n"
    "buffer_load_dwordx4 v8, s[40:43], s75 offen offset:3072 lds\n"
    "s_add_u32 m0, s81, 57344\n"
    "s_mul_i32 s76, s75, 1376\n"
    "s_waitcnt vmcnt(10)\n"
    "s_barrier\n"
    "v_mfma_f32_16x16x32_bf16 v[140:143], a[68:71], v[72:75], v[140:143]\n"
    "ds_read_b128 a[80:83], v13 offset:0\n"
    "ds_read_b128 a[84:87], v13 offset:2048\n"
    "ds_read_b64_tr_b16 v[88:89], v17 offset:40960\n"
    "ds_read_b64_tr_b16 v[90:91], v17 offset:41984\n"
    "v_mfma_f32_16x16x32_bf16 v[120:123], a[72:75], v[76:79], v[120:123]\n"
    "ds_read_b64_tr_b16 v[92:93], v17 offset:40992\n"
    "v_mfma_f32_16x16x32_bf16 v[124:127], a[76:79], v[76:79], v[124:127]\n"
    "ds_read_b64_tr_b16 v[94:95], v17 offset:42016\n"
    "s_waitcnt lgkmcnt(6)\n"
    "v_mfma_f32_16x16x32_bf16 v[128:131], a[72:75], v[80:83], v[128:131]\n"
    "ds_read_b64_tr_b16 v[64:65], v17 offset:41024\n"
    "v_mfma_f32_16x16x32_bf16 v[132:135], a[76:79], v[80:83], v[132:135]\n"
    "ds_read_b64_tr_b16 v[66:67], v17 offset:42048\n"
    "v_mfma_f32_16x16x32_bf16 v[136:139], a[72:75], v[84:87], v[136:139]\n"
    "ds_read_b128 a[88:91], v14 offset:0\n"
    "ds_read_b128 a[92:95], v14 offset:2048\n"
    "v_mfma_f32_16x16x32_bf16 v[140:143], a[76:79], v[84:87], v[140:143]\n"
    "ds_read_b64_tr_b16 v[68:69], v17 offset:45056\n"
    "ds_read_b64_tr_b16 v[70:71], v17 offset:46080\n"
    "s_waitcnt lgkmcnt(6)\n"
    "v_mfma_f32_16x16x32_bf16 v[120:123], a[80:83], v[88:91], v[120:123]\n"
    "ds_read_b64_tr_b16 v[72:73], v17 offset:45088\n"
    "buffer_load_dwordx4 v9, s[44:47], s76 offen offset:0 lds\n"
    "v_mfma_f32_16x16x32_bf16 v[124:127], a[84:87], v[88:91], v[124:127]\n"
    "ds_read_b64_tr_b16 v[74:75], v17 offset:46112\n"
    "v_mfma_f32_16x16x32_bf16 v[128:131], a[80:83], v[92:95], v[128:131]\n"
    "ds_read_b64_tr_b16 v[76:77], v17 offset:45120\n"
    "buffer_load_dwordx4 v10, s[44:47], s76 offen offset:1024 lds\n"
    "v_mfma_f32_16x16x32_bf16 v[132:135], a[84:87], v[92:95], v[132:135]\n"
    "ds_read_b64_tr_b16 v[78:79], v17 offset:46144\n"
    "s_waitcnt lgkmcnt(4)\n"
    "v_mfma_f32_16x16x32_bf16 v[136:139], a[80:83], v[64:67], v[136:139]\n"
    "s_add_u32 m0, s81, 0\n"
    "s_nop 0\n"
    "buffer_load_dwordx4 v11, s[48:51], s68 offen offset:0 lds\n"
    "buffer_load_dwordx4 v12, s[48:51], s68 offen offset:1024 lds\n"
    "s_add_u32 m0, s81, 8192\n"
    "s_nop 0\n"
    "buffer_load_dwordx4 v11, s[48:51], s69 offen offset:0 lds\n"
    "buffer_load_dwordx4 v12, s[48:51], s69 offen offset:1024 lds\n"
    "s_add_u32 m0, s81, 16384\n"
    "s_waitcnt vmcnt(10)\n"
    "s_barrier\n"
    "v_mfma_f32_16x16x32_bf16 v[140:143], a[84:87], v[64:67], v[140:143]\n"
    "ds_read_b128 a[96:99], v13 offset:4096\n"
    "ds_read_b128 a[100:103], v13 offset:6144\n"
    "ds_read_b64_tr_b16 v[80:81], v17 offset:49152\n"
    "ds_read_b64_tr_b16 v[82:83], v17 offset:50176\n"
    "v_mfma_f32_16x16x32_bf16 v[120:123], a[88:91], v[68:71], v[120:123]\n"
    "ds_read_b64_tr_b16 v[84:85], v17 offset:49184\n"
    "v_mfma_f32_16x16x32_bf16 v[124:127], a[92:95], v[68:71], v[124:127]\n"
    "ds_read_b64_tr_b16 v[86:87], v17 offset:50208\n"
    "s_waitcnt lgkmcnt(6)\n"
    "v_mfma_f32_16x16x32_bf16 v[128:131], a[88:91], v[72:75], v[128:131]\n"
    "ds_read_b64_tr_b16 v[88:89], v17 offset:49216\n"
    "v_mfma_f32_16x16x32_bf16 v[132:135], a[92:95], v[72:75], v[132:135]\n"
    "ds_read_b64_tr_b16 v[90:91], v17 offset:50240\n"
    "v_mfma_f32_16x16x32_bf16 v[136:139], a[88:91], v[76:79], v[136:139]\n"
    "ds_read_b128 a[104:107], v14 offset:4096\n"
    "ds_read_b128 a[108:111], v14 offset:6144\n"
    "v_mfma_f32_16x16x32_bf16 v[140:143], a[92:95], v[76:79], v[140:143]\n"
    "ds_read_b64_tr_b16 v[92:93], v17 offset:53248\n"
    "ds_read_b64_tr_b16 v[94:95], v17 offset:54272\n"
    "s_waitcnt lgkmcnt(6)\n"
    "v_mfma_f32_16x16x32_bf16 v[120:123], a[96:99], v[80:83], v[120:123]\n"
    "ds_read_b64_tr_b16 v[64:65], v17 offset:53280\n"
    "buffer_load_dwordx4 v11, s[48:51], s70 offen offset:0 lds\n"
    "v_mfma_f32_16x16x32_bf16 v[124:127], a[100:103], v[80:83], v[124:127]\n"
    "ds_read_b64_tr_b16 v[66:67], v17 offset:54304\n"
    "v_mfma_f32_16x16x32_bf16 v[128:131], a[96:99], v[84:87], v[128:131]\n"
    "ds_read_b64_tr_b16 v[68:69], v17 offset:53312\n"
    "buffer_load_dwordx4 v12, s[48:51], s70 offen offset:1024 lds\n"
    "v_mfma_f32_16x16x32_bf16 v[132:135], a[100:103], v[84:87], v[132:135]\n"
    "ds_read_b64_tr_b16 v[70:71], v17 offset:54336\n"
    "s_waitcnt lgkmcnt(4)\n"
    "v_mfma_f32_16x16x32_bf16 v[136:139], a[96:99], v[88:91], v[136:139]\n"
    "s_add_u32 m0, s81, 24576\n"
    "s_nop 0\n"
    "buffer_load_dwordx4 v11, s[48:51], s71 offen offset:0 lds\n"
    "buffer_load_dwordx4 v12, s[48:51], s71 offen offset:1024 lds\n"
    "s_add_u32 m0, s81, 32768\n"
    "s_nop 0\n"
    "buffer_load_dwordx4 v11, s[48:51], s72 offen offset:0 lds\n"
    "buffer_load_dwordx4 v12, s[48:51], s72 offen offset:1024 lds\n"
    "s_waitcnt vmcnt(10)\n"
    "s_barrier\n"
    "v_mfma_f32_16x16x32_bf16 v[140:143], a[100:103], v[88:91], v[140:143]\n"
    "ds_read_b128 a[112:115], v13 offset:8192\n"
    "ds_read_b128 a[116:119], v13 offset:10240\n"
    "ds_read_b64_tr_b16 v[72:73], v17 offset:57344\n"
    "ds_read_b64_tr_b16 v[74:75], v17 offset:58368\n"
    "v_mfma_f32_16x16x32_bf16 v[120:123], a[104:107], v[92:95], v[120:123]\n"
    "ds_read_b64_tr_b16 v[76:77], v17 offset:57376\n"
    "v_mfma_f32_16x16x32_bf16 v[124:127], a[108:111], v[92:95], v[124:127]\n"
    "ds_read_b64_tr_b16 v[78:79], v17 offset:58400\n"
    "s_waitcnt lgkmcnt(6)\n"
    "v_mfma_f32_16x16x32_bf16 v[128:131], a[104:107], v[64:67], v[128:131]\n"
    "ds_read_b64_tr_b16 v[80:81], v17 offset:57408\n"
    "v_mfma_f32_16x16x32_bf16 v[132:135], a[108:111], v[64:67], v[132:135]\n"
    "ds_read_b64_tr_b16 v[82:83], v17 offset:58432\n"
    "v_mfma_f32_16x16x32_bf16 v[136:139], a[104:107], v[68:71], v[136:139]\n"
    "ds_read_b128 a[120:123], v14 offset:8192\n"
    "ds_read_b128 a[124:127], v14 offset:10240\n"
    "v_mfma_f32_16x16x32_bf16 v[140:143], a[108:111], v[68:71], v[140:143]\n"
    "ds_read_b64_tr_b16 v[84:85], v17 offset:61440\n"
    "ds_read_b64_tr_b16 v[86:87], v17 offset:62464\n"
    "s_waitcnt lgkmcnt(6)\n"
    "v_mfma_f32_16x16x32_bf16 v[120:123], a[112:115], v[72:75], v[120:123]\n"
    "ds_read_b64_tr_b16 v[88:89], v17 offset:61472\n"
    "v_mfma_f32_16x16x32_bf16 v[124:127], a[116:119], v[72:75], v[124:127]\n"
    "ds_read_b64_tr_b16 v[90:91], v17 offset:62496\n"
    "v_mfma_f32_16x16x32_bf16 v[128:131], a[112:115], v[76:79], v[128:131]\n"
    "ds_read_b64_tr_b16 v[92:93], v17 offset:61504\n"
    "v_mfma_f32_16x16x32_bf16 v[132:135], a[116:119], v[76:79], v[132:135]\n"
    "ds_read_b64_tr_b16 v[94:95], v17 offset:62528\n"
    "s_waitcnt lgkmcnt(4)\n"
    "v_mfma_f32_16x16x32_bf16 v[136:139], a[112:115], v[80:83], v[136:139]\n"
    "v_mfma_f32_16x16x32_bf16 v[140:143], a[116:119], v[80:83], v[140:143]\n"
    "v_mfma_f32_16x16x32_bf16 v[120:123], a[120:123], v[84:87], v[120:123]\n"
    "v_mfma_f32_16x16x32_bf16 v[124:127], a[124:127], v[84:87], v[124:127]\n"
    "s_waitcnt lgkmcnt(0)\n"
    "v_mfma_f32_16x16x32_bf16 v[128:131], a[120:123], v[88:91], v[128:131]\n"
    "v_mfma_f32_16x16x32_bf16 v[132:135], a[124:127], v[88:91], v[132:135]\n"
    "v_mfma_f32_16x16x32_bf16 v[136:139], a[120:123], v[92:95], v[136:139]\n"
    "v_mfma_f32_16x16x32_bf16 v[140:143], a[124:127], v[92:95], v[140:143]\n"
    "s_nop 1\n"
    "v_pk_mul_f32 v[220:221], v[120:121], v[120:121]\n"
    "v_pk_mul_f32 v[222:223], v[122:123], v[122:123]\n"
    "v_pk_mul_f32 v[224:225], v[124:125], v[124:125]\n"
    "v_pk_mul_f32 v[226:227], v[126:127], v[126:127]\n"
    "v_pk_fma_f32 v[220:221], v[220:221], s[18:19], s[18:19] op_sel:[0,0,1] op_sel_hi:[1,0,1]\n"
    "v_pk_fma_f32 v[222:223], v[222:223], s[18:19], s[18:19] op_sel:[0,0,1] op_sel_hi:[1,0,1]\n"
    "v_pk_fma_f32 v[224:225], v[224:225], s[18:19], s[18:19] op_sel:[0,0,1] op_sel_hi:[1,0,1]\n"
    "v_pk_fma_f32 v[226:227], v[226:227], s[18:19], s[18:19] op_sel:[0,0,1] op_sel_hi:[1,0,1]\n"
    "v_pk_mul_f32 v[220:221], v[220:221], v[120:121]\n"
    "v_pk_mul_f32 v[222:223], v[222:223], v[122:123]\n"
    "v_pk_mul_f32 v[224:225], v[224:225], v[124:125]\n"
    "v_pk_mul_f32 v[226:227], v[226:227], v[126:127]\n"
    "v_exp_f32 v220, v220\n"
    "v_exp_f32 v221, v221\n"
    "v_exp_f32 v222, v222\n"
    "v_exp_f32 v223, v223\n"
    "v_exp_f32 v224, v224\n"
    "v_exp_f32 v225, v225\n"
    "v_exp_f32 v226, v226\n"
    "v_exp_f32 v227, v227\n"
    "v_pk_add_f32 v[220:221], v[220:221], 1.0 op_sel_hi:[1,0]\n"
    "v_pk_add_f32 v[222:223], v[222:223], 1.0 op_sel_hi:[1,0]\n"
    "v_pk_add_f32 v[224:225], v[224:225], 1.0 op_sel_hi:[1,0]\n"
    "v_pk_add_f32 v[226:227], v[226:227], 1.0 op_sel_hi:[1,0]\n"
    "v_rcp_f32 v220, v220\n"
    "v_rcp_f32 v221, v221\n"
    "v_rcp_f32 v222, v222\n"
    "v_rcp_f32 v223, v223\n"
    "v_rcp_f32 v224, v224\n"
    "v_rcp_f32 v225, v225\n"
    "v_rcp_f32 v226, v226\n"
    "v_rcp_f32 v227, v227\n"
    "v_pk_mul_f32 v[120:121], v[220:221], v[120:121]\n"
    "v_pk_mul_f32 v[122:123], v[222:223], v[122:123]\n"
    "v_pk_mul_f32 v[124:125], v[224:225], v[124:125]\n"
    "v_pk_mul_f32 v[126:127], v[226:227], v[126:127]\n"
    "v_pk_mul_f32 v[220:221], v[128:129], v[128:129]\n"
    "v_pk_mul_f32 v[222:223], v[130:131], v[130:131]\n"
    "v_pk_mul_f32 v[224:225], v[132:133], v[132:133]\n"
    "v_pk_mul_f32 v[226:227], v[134:135], v[134:135]\n"
    "v_pk_fma_f32 v[220:221], v[220:221], s[18:19], s[18:19] op_sel:[0,0,1] op_sel_hi:[1,0,1]\n"
    "v_pk_fma_f32 v[222:223], v[222:223], s[18:19], s[18:19] op_sel:[0,0,1] op_sel_hi:[1,0,1]\n"
    "v_pk_fma_f32 v[224:225], v[224:225], s[18:19], s[18:19] op_sel:[0,0,1] op_sel_hi:[1,0,1]\n"
    "v_pk_fma_f32 v[226:227], v[226:227], s[18:19], s[18:19] op_sel:[0,0,1] op_sel_hi:[1,0,1]\n"
    "v_pk_mul_f32 v[220:221], v[220:221], v[128:129]\n"
    "v_pk_mul_f32 v[222:223], v[222:223], v[130:131]\n"
    "v_pk_mul_f32 v[224:225], v[224:225], v[132:133]\n"
    "v_pk_mul_f32 v[226:227], v[226:227], v[134:135]\n"
    "v_exp_f32 v220, v220\n"
    "v_exp_f32 v221, v221\n"
    "v_exp_f32 v222, v222\n"
    "v_exp_f32 v223, v223\n"
    "v_exp_f32 v224, v224\n"
    "v_exp_f32 v225, v225\n"
    "v_exp_f32 v226, v226\n"
    "v_exp_f32 v227, v227\n"
    "v_pk_add_f32 v[220:221], v[220:221], 1.0 op_sel_hi:[1,0]\n"
    "v_pk_add_f32 v[222:223], v[222:223], 1.0 op_sel_hi:[1,0]\n"
    "v_pk_add_f32 v[224:225], v[224:225], 1.0 op_sel_hi:[1,0]\n"
    "v_pk_add_f32 v[226:227], v[226:227], 1.0 op_sel_hi:[1,0]\n"
    "v_rcp_f32 v220, v220\n"
    "v_rcp_f32 v221, v221\n"
    "v_rcp_f32 v222, v222\n"
    "v_rcp_f32 v223, v223\n"
    "v_rcp_f32 v224, v224\n"
    "v_rcp_f32 v225, v225\n"
    "v_rcp_f32 v226, v226\n"
    "v_rcp_f32 v227, v227\n"
    "v_pk_mul_f32 v[128:129], v[220:221], v[128:129]\n"
    "v_pk_mul_f32 v[130:131], v[222:223], v[130:131]\n"
    "v_pk_mul_f32 v[132:133], v[224:225], v[132:133]\n"
    "v_pk_mul_f32 v[134:135], v[226:227], v[134:135]\n"
    "v_pk_mul_f32 v[220:221], v[136:137], v[136:137]\n"
    "v_pk_mul_f32 v[222:223], v[138:139], v[138:139]\n"
    "v_pk_mul_f32 v[224:225], v[140:141], v[140:141]\n"
    "v_pk_mul_f32 v[226:227], v[142:143], v[142:143]\n"
    "v_pk_fma_f32 v[220:221], v[220:221], s[18:19], s[18:19] op_sel:[0,0,1] op_sel_hi:[1,0,1]\n"
    "v_pk_fma_f32 v[222:223], v[222:223], s[18:19], s[18:19] op_sel:[0,0,1] op_sel_hi:[1,0,1]\n"
    "v_pk_fma_f32 v[224:225], v[224:225], s[18:19], s[18:19] op_sel:[0,0,1] op_sel_hi:[1,0,1]\n"
    "v_pk_fma_f32 v[226:227], v[226:227], s[18:19], s[18:19] op_sel:[0,0,1] op_sel_hi:[1,0,1]\n"
    "v_pk_mul_f32 v[220:221], v[220:221], v[136:137]\n"
    "v_pk_mul_f32 v[222:223], v[222:223], v[138:139]\n"
    "v_pk_mul_f32 v[224:225], v[224:225], v[140:141]\n"
    "v_pk_mul_f32 v[226:227], v[226:227], v[142:143]\n"
    "v_exp_f32 v220, v220\n"
    "v_exp_f32 v221, v221\n"
    "v_exp_f32 v222, v222\n"
    "v_exp_f32 v223, v223\n"
    "v_exp_f32 v224, v224\n"
    "v_exp_f32 v225, v225\n"
    "v_exp_f32 v226, v226\n"
    "v_exp_f32 v227, v227\n"
    "v_pk_add_f32 v[220:221], v[220:221], 1.0 op_sel_hi:[1,0]\n"
    "v_pk_add_f32 v[222:223], v[222:223], 1.0 op_sel_hi:[1,0]\n"
    "v_pk_add_f32 v[224:225], v[224:225], 1.0 op_sel_hi:[1,0]\n"
    "v_pk_add_f32 v[226:227], v[226:227], 1.0 op_sel_hi:[1,0]\n"
    "v_rcp_f32 v220, v220\n"
    "v_rcp_f32 v221, v221\n"
    "v_rcp_f32 v222, v222\n"
    "v_rcp_f32 v223, v223\n"
    "v_rcp_f32 v224, v224\n"
    "v_rcp_f32 v225, v225\n"
    "v_rcp_f32 v226, v226\n"
    "v_rcp_f32 v227, v227\n"
    "v_pk_mul_f32 v[136:137], v[220:221], v[136:137]\n"
    "v_pk_mul_f32 v[138:139], v[222:223], v[138:139]\n"
    "v_pk_mul_f32 v[140:141], v[224:225], v[140:141]\n"
    "v_pk_mul_f32 v[142:143], v[226:227], v[142:143]\n"
    "v_pk_mul_f32 v[236:237], v[120:121], v[120:121]\n"
    "v_pk_add_f32 v[228:229], v[120:121], v[128:129]\n"
    "v_pk_mul_f32 v[238:239], v[122:123], v[122:123]\n"
    "v_pk_add_f32 v[230:231], v[122:123], v[130:131]\n"
    "v_pk_mul_f32 v[240:241], v[124:125], v[124:125]\n"
    "v_pk_add_f32 v[232:233], v[124:125], v[132:133]\n"
    "v_pk_mul_f32 v[242:243], v[126:127], v[126:127]\n"
    "v_pk_add_f32 v[234:235], v[126:127], v[134:135]\n"
    "v_pk_fma_f32 v[236:237], v[128:129], v[128:129], v[236:237]\n"
    "v_pk_add_f32 v[228:229], v[228:229], v[136:137]\n"
    "v_pk_fma_f32 v[238:239], v[130:131], v[130:131], v[238:239]\n"
    "v_pk_add_f32 v[230:231], v[230:231], v[138:139]\n"
    "s_add_u32 m0, s81, 40960\n"
    "v_pk_fma_f32 v[240:241], v[132:133], v[132:133], v[240:241]\n"
    "v_pk_add_f32 v[232:233], v[232:233], v[140:141]\n"
    "v_pk_fma_f32 v[242:243], v[134:135], v[134:135], v[242:243]\n"
    "buffer_load_dwordx4 v11, s[48:51], s73 offen offset:0 lds\n"
    "v_pk_add_f32 v[234:235], v[234:235], v[142:143]\n"
    "v_pk_fma_f32 v[236:237], v[136:137], v[136:137], v[236:237]\n"
    "v_pk_fma_f32 v[238:239], v[138:139], v[138:139], v[238:239]\n"
    "v_pk_fma_f32 v[240:241], v[140:141], v[140:141], v[240:241]\n"
    "v_pk_fma_f32 v[242:243], v[142:143], v[142:143], v[242:243]\n"
    "v_add_f32_dpp v32, v228, v228 row_mirror row_mask:0xf bank_mask:0x3\n"
    "v_add_f32_dpp v33, v229, v229 row_mirror row_mask:0xf bank_mask:0x3\n"
    "v_add_f32_dpp v34, v230, v230 row_mirror row_mask:0xf bank_mask:0x3\n"
    "v_add_f32_dpp v35, v231, v231 row_mirror row_mask:0xf bank_mask:0x3\n"
    "v_add_f32_dpp v36, v232, v232 row_mirror row_mask:0xf bank_mask:0x3\n"
    "v_add_f32_dpp v37, v233, v233 row_mirror row_mask:0xf bank_mask:0x3\n"
    "v_add_f32_dpp v38, v234, v234 row_mirror row_mask:0xf bank_mask:0x3\n"
    "v_add_f32_dpp v39, v235, v235 row_mirror row_mask:0xf bank_mask:0x3\n"
    "v_add_f32_dpp v32, v236, v236 row_mirror row_mask:0xf bank_mask:0xc\n"
    "v_add_f32_dpp v33, v237, v237 row_mirror row_mask:0xf bank_mask:0xc\n"
    "v_add_f32_dpp v34, v238, v238 row_mirror row_mask:0xf bank_mask:0xc\n"
    "v_add_f32_dpp v35, v239, v239 row_mirror row_mask:0xf bank_mask:0xc\n"
    "v_add_f32_dpp v36, v240, v240 row_mirror row_mask:0xf bank_mask:0xc\n"
    "buffer_load_dwordx4 v12, s[48:51], s73 offen offset:1024 lds\n"
    "v_add_f32_dpp v37, v241, v241 row_mirror row_mask:0xf bank_mask:0xc\n"
    "v_add_f32_dpp v38, v242, v242 row_mirror row_mask:0xf bank_mask:0xc\n"
    "v_add_f32_dpp v39, v243, v243 row_mirror row_mask:0xf bank_mask:0xc\n"
    "v_add_f32_dpp v40, v32, v32 row_half_mirror row_mask:0xf bank_mask:0x5\n"
    "v_add_f32_dpp v41, v33, v33 row_half_mirror row_mask:0xf bank_mask:0x5\n"
    "v_add_f32_dpp v42, v34, v34 row_half_mirror row_mask:0xf bank_mask:0x5\n"
    "v_add_f32_dpp v43, v35, v35 row_half_mirror row_mask:0xf bank_mask:0x5\n"
    "v_add_f32_dpp v40, v36, v36 row_half_mirror row_mask:0xf bank_mask:0xa\n"
    "v_add_f32_dpp v41, v37, v37 row_half_mirror row_mask:0xf bank_mask:0xa\n"
    "v_add_f32_dpp v42, v38, v38 row_half_mirror row_mask:0xf bank_mask:0xa\n"
    "v_add_f32_dpp v43, v39, v39 row_half_mirror row_mask:0xf bank_mask:0xa\n"
    "v_add_f32_dpp v32, v40, v40 quad_perm:[2,3,0,1] row_mask:0xf bank_mask:0xf\n"
    "v_add_f32_dpp v33, v41, v41 quad_perm:[2,3,0,1] row_mask:0xf bank_mask:0xf\n"
    "v_add_f32_dpp v34, v42, v42 quad_perm:[2,3,0,1] row_mask:0xf bank_mask:0xf\n"
    "v_add_f32_dpp v35, v43, v43 quad_perm:[2,3,0,1] row_mask:0xf bank_mask:0xf\n"
    "v_cndmask_b32 v40, v32, v34, s[20:21]\n"
    "v_cndmask_b32 v41, v33, v35, s[20:21]\n"
    "s_nop 0\n"
    "v_add_f32_dpp v32, v40, v40 quad_perm:[1,0,3,2] row_mask:0xf bank_mask:0xf\n"
    "v_add_f32_dpp v33, v41, v41 quad_perm:[1,0,3,2] row_mask:0xf bank_mask:0xf\n"
    "v_cndmask_b32 v40, v32, v33, s[22:23]\n"
    "v_mov_b32 v41, s64\n"
    "buffer_store_dwordx2 v[40:41], v24, s[56:59], s95 offen sc1\n"
    "s_waitcnt vmcnt(11)\n"
    "s_barrier\n"
    "ds_read_b128 v[64:67], v15 offset:0\n"
    "ds_read_b128 v[68:71], v15 offset:2048\n"
    "ds_read_b128 v[72:75], v15 offset:4096\n"
    "ds_read_b128 v[76:79], v15 offset:6144\n"
    "ds_read_b128 v[80:83], v16 offset:0\n"
    "ds_read_b128 v[84:87], v16 offset:2048\n"
    "s_waitcnt lgkmcnt(4)\n"
    "v_mfma_f32_16x16x32_bf16 v[152:155], a[0:3], v[64:67], 0\n"
    "ds_read_b128 v[88:91], v16 offset:4096\n"
    "s_add_u32 m0, s81, 49152\n"
    "v_mfma_f32_16x16x32_bf16 v[156:159], a[4:7], v[64:67], 0\n"
    "buffer_load_dwordx4 v11, s[48:51], s74 offen offset:0 lds\n"
    "v_mfma_f32_16x16x32_bf16 v[160:163], a[0:3], v[68:71], 0\n"
    "ds_read_b128 v[92:95], v16 offset:6144\n"
    "buffer_load_dwordx4 v12, s[48:51], s74 offen offset:1024 lds\n"
    "v_mfma_f32_16x16x32_bf16 v[164:167], a[4:7], v[68:71], 0\n"
    "s_add_u32 m0, s81, 57344\n"
    "s_nop 0\n"
    "buffer_load_dwordx4 v11, s[48:51], s75 offen offset:0 lds\n"
    "s_waitcnt lgkmcnt(4)\n"
    "v_mfma_f32_16x16x32_bf16 v[168:171], a[0:3], v[72:75], 0\n"
    "buffer_load_dwordx4 v12, s[48:51], s75 offen offset:1024 lds\n"
    "s_waitcnt vmcnt(13)\n"
    "s_barrier\n"
    "v_mfma_f32_16x16x32_bf16 v[172:175], a[4:7], v[72:75], 0\n"
    "ds_read_b128 v[64:67], v15 offset:8192\n"
    "v_lshl_add_u32 v5, v29, 12, v28\n"
    "v_mfma_f32_16x16x32_bf16 v[176:179], a[0:3], v[76:79], 0\n"
    "ds_read_b128 v[68:71], v15 offset:10240\n"
    "s_lshl_b32 s77, s63, 11\n"
    "v_mfma_f32_16x16x32_bf16 v[180:183], a[4:7], v[76:79], 0\n"
    "v_add_u32 v5, s77, v5\n"
    "v_xor_b32 v8, 64, v5\n"
    "s_waitcnt lgkmcnt(4)\n"
    "v_mfma_f32_16x16x32_bf16 v[152:155], a[8:11], v[80:83], v[152:155]\n"
    "ds_read_b128 v[72:75], v15 offset:12288\n"
    "v_add_u32 v6, 64512, v8\n"
    "v_mfma_f32_16x16x32_bf16 v[156:159], a[12:15], v[80:83], v[156:159]\n"
    "v_add_u32 v7, 14336, v5\n"
    "v_add_u32 v8, 78848, v8\n"
    "v_mfma_f32_16x16x32_bf16 v[160:163], a[8:11], v[84:87], v[160:163]\n"
    "ds_read_b128 v[76:79], v15 offset:14336\n"
    "s_add_u32 m0, s67, 12288\n"
    "v_mfma_f32_16x16x32_bf16 v[164:167], a[12:15], v[84:87], v[164:167]\n"
    "s_bfe_u32 s77, s24, 0x40000\n"
    "s_lshl_b32 s76, s77, 7\n"
    "s_waitcnt lgkmcnt(4)\n"
    "v_mfma_f32_16x16x32_bf16 v[168:171], a[8:11], v[88:91], v[168:171]\n"
    "ds_read_b128 v[80:83], v16 offset:8192\n"
    "buffer_load_dwordx4 v5, s[52:55], s76 offen offset:0 lds\n"
    "v_mfma_f32_16x16x32_bf16 v[172:175], a[12:15], v[88:91], v[172:175]\n"
    "buffer_load_dwordx4 v6, s[52:55], s76 offen offset:1024 lds\n"
    "v_mfma_f32_16x16x32_bf16 v[176:179], a[8:11], v[92:95], v[176:179]\n"
    "ds_read_b128 v[84:87], v16 offset:10240\n"
    "buffer_load_dwordx4 v7, s[52:55], s76 offen offset:2048 lds\n"
    "v_mfma_f32_16x16x32_bf16 v[180:183], a[12:15], v[92:95], v[180:183]\n"
    "buffer_load_dwordx4 v8, s[52:55], s76 offen offset:3072 lds\n"
    "s_waitcnt lgkmcnt(4)\n"
    "v_mfma_f32_16x16x32_bf16 v[152:155], a[16:19], v[64:67], v[152:155]\n"
    "ds_read_b128 v[88:91], v16 offset:12288\n"
    "s_add_u32 m0, s67, 16384\n"
    "v_mfma_f32_16x16x32_bf16 v[156:159], a[20:23], v[64:67], v[156:159]\n"
    "s_bfe_u32 s77, s24, 0x40004\n"
    "s_lshl_b32 s76, s77, 7\n"
    "v_mfma_f32_16x16x32_bf16 v[160:163], a[16:19], v[68:71], v[160:163]\n"
    "ds_read_b128 v[92:95], v16 offset:14336\n"
    "buffer_load_dwordx4 v5, s[52:55], s76 offen offset:0 lds\n"
    "v_mfma_f32_16x16x32_bf16 v[164:167], a[20:23], v[68:71], v[164:167]\n"
    "s_waitcnt lgkmcnt(4)\n"
    "v_mfma_f32_16x16x32_bf16 v[168:171], a[16:19], v[72:75], v[168:171]\n"
    "buffer_load_dwordx4 v6, s[52:55], s76 offen offset:1024 lds\n"
    "buffer_load_dwordx4 v7, s[52:55], s76 offen offset:2048 lds\n"
    "buffer_load_dwordx4 v8, s[52:55], s76 offen offset:3072 lds\n"
    "s_waitcnt vmcnt(19)\n"
    "s_barrier\n"
    "v_mfma_f32_16x16x32_bf16 v[172:175], a[20:23], v[72:75], v[172:175]\n"
    "ds_read_b128 v[64:67], v15 offset:16384\n"
    "v_mfma_f32_16x16x32_bf16 v[176:179], a[16:19], v[76:79], v[176:179]\n"
    "ds_read_b128 v[68:71], v15 offset:18432\n"
    "v_mfma_f32_16x16x32_bf16 v[180:183], a[20:23], v[76:79], v[180:183]\n"
    "s_waitcnt lgkmcnt(4)\n"
    "v_mfma_f32_16x16x32_bf16 v[152:155], a[24:27], v[80:83], v[152:155]\n"
    "ds_read_b128 v[72:75], v15 offset:20480\n"
    "v_mfma_f32_16x16x32_bf16 v[156:159], a[28:31], v[80:83], v[156:159]\n"
    "v_mfma_f32_16x16x32_bf16 v[160:163], a[24:27], v[84:87], v[160:163]\n"
    "ds_read_b128 v[76:79], v15 offset:22528\n"
    "v_mfma_f32_16x16x32_bf16 v[164:167], a[28:31], v[84:87], v[164:167]\n"
    "s_waitcnt lgkmcnt(4)\n"
    "v_mfma_f32_16x16x32_bf16 v[168:171], a[24:27], v[88:91], v[168:171]\n"
    "ds_read_b128 v[80:83], v16 offset:16384\n"
    "v_mfma_f32_16x16x32_bf16 v[172:175], a[28:31], v[88:91], v[172:175]\n"
    "v_mfma_f32_16x16x32_bf16 v[176:179], a[24:27], v[92:95], v[176:179]\n"
    "ds_read_b128 v[84:87], v16 offset:18432\n"
    "v_mfma_f32_16x16x32_bf16 v[180:183], a[28:31], v[92:95], v[180:183]\n"
    "s_waitcnt lgkmcnt(4)\n"
    "v_mfma_f32_16x16x32_bf16 v[152:155], a[32:35], v[64:67], v[152:155]\n"
    "ds_read_b128 v[88:91], v16 offset:20480\n"
    "v_mfma_f32_16x16x32_bf16 v[156:159], a[36:39], v[64:67], v[156:159]\n"
    "v_mfma_f32_16x16x32_bf16 v[160:163], a[32:35], v[68:71], v[160:163]\n"
    "ds_read_b128 v[92:95], v16 offset:22528\n"
    "v_mfma_f32_16x16x32_bf16 v[164:167], a[36:39], v[68:71], v[164:167]\n"
    "s_waitcnt lgkmcnt(4)\n"
    "v_mfma_f32_16x16x32_bf16 v[168:171], a[32:35], v[72:75], v[168:171]\n"
    "s_waitcnt vmcnt(17)\n"
    "s_barrier\n"
    "v_mfma_f32_16x16x32_bf16 v[172:175], a[36:39], v[72:75], v[172:175]\n"
    "ds_read_b128 v[64:67], v15 offset:24576\n"
    "v_mov_b32 v9, v119\n"
    "v_mfma_f32_16x16x32_bf16 v[176:179], a[32:35], v[76:79], v[176:179]\n"
    "ds_read_b128 v[68:71], v15 offset:26624\n"
    "v_add_u32 v10, 9984, v119\n"
    "v_mfma_f32_16x16x32_bf16 v[180:183], a[36:39], v[76:79], v[180:183]\n"
    "s_add_u32 m0, s81, 0\n"
    "s_mul_i32 s76, s68, 1376\n"
    "s_waitcnt lgkmcnt(4)\n"
    "v_mfma_f32_16x16x32_bf16 v[152:155], a[40:43], v[80:83], v[152:155]\n"
    "ds_read_b128 v[72:75], v15 offset:28672\n"
    "buffer_load_dwordx4 v9, s[44:47], s76 offen offset:0 lds\n"
    "v_mfma_f32_16x16x32_bf16 v[156:159], a[44:47], v[80:83], v[156:159]\n"
    "v_mfma_f32_16x16x32_bf16 v[160:163], a[40:43], v[84:87], v[160:163]\n"
    "ds_read_b128 v[76:79], v15 offset:30720\n"
    "v_mfma_f32_16x16x32_bf16 v[164:167], a[44:47], v[84:87], v[164:167]\n"
    "s_waitcnt lgkmcnt(4)\n"
    "v_mfma_f32_16x16x32_bf16 v[168:171], a[40:43], v[88:91], v[168:171]\n"
    "ds_read_b128 v[80:83], v16 offset:24576\n"
    "v_mfma_f32_16x16x32_bf16 v[172:175], a[44:47], v[88:91], v[172:175]\n"
    "v_mfma_f32_16x16x32_bf16 v[176:179], a[40:43], v[92:95], v[176:179]\n"
    "ds_read_b128 v[84:87], v16 offset:26624\n"
    "v_mfma_f32_16x16x32_bf16 v[180:183], a[44:47], v[92:95], v[180:183]\n"
    "s_waitcnt lgkmcnt(4)\n"
    "v_mfma_f32_16x16x32_bf16 v[152:155], a[48:51], v[64:67], v[152:155]\n"
    "ds_read_b128 v[88:91], v16 offset:28672\n"
    "v_mfma_f32_16x16x32_bf16 v[156:159], a[52:55], v[64:67], v[156:159]\n"
    "v_mfma_f32_16x16x32_bf16 v[160:163], a[48:51], v[68:71], v[160:163]\n"
    "ds_read_b128 v[92:95], v16 offset:30720\n"
    "v_mfma_f32_16x16x32_bf16 v[164:167], a[52:55], v[68:71], v[164:167]\n"
    "s_waitcnt lgkmcnt(4)\n"
    "v_mfma_f32_16x16x32_bf16 v[168:171], a[48:51], v[72:75], v[168:171]\n"
    "buffer_load_dwordx4 v10, s[44:47], s76 offen offset:1024 lds\n"
    "s_waitcnt vmcnt(17)\n"
    "s_barrier\n"
    "v_mfma_f32_16x16x32_bf16 v[172:175], a[52:55], v[72:75], v[172:175]\n"
    "ds_read_b128 v[64:67], v15 offset:32768\n"
    "v_mfma_f32_16x16x32_bf16 v[176:179], a[48:51], v[76:79], v[176:179]\n"
    "ds_read_b128 v[68:71], v15 offset:34816\n"
    "v_mfma_f32_16x16x32_bf16 v[180:183], a[52:55], v[76:79], v[180:183]\n"
    "s_waitcnt lgkmcnt(4)\n"
    "v_mfma_f32_16x16x32_bf16 v[152:155], a[56:59], v[80:83], v[152:155]\n"
    "ds_read_b128 v[72:75], v15 offset:36864\n"
    "v_mfma_f32_16x16x32_bf16 v[156:159], a[60:63], v[80:83], v[156:159]\n"
    "v_mfma_f32_16x16x32_bf16 v[160:163], a[56:59], v[84:87], v[160:163]\n"
    "ds_read_b128 v[76:79], v15 offset:38912\n"
    "v_mfma_f32_16x16x32_bf16 v[164:167], a[60:63], v[84:87], v[164:167]\n"
    "s_waitcnt lgkmcnt(4)\n"
    "v_mfma_f32_16x16x32_bf16 v[168:171], a[56:59], v[88:91], v[168:171]\n"
    "ds_read_b128 v[80:83], v16 offset:32768\n"
    "v_mfma_f32_16x16x32_bf16 v[172:175], a[60:63], v[88:91], v[172:175]\n"
    "v_mfma_f32_16x16x32_bf16 v[176:179], a[56:59], v[92:95], v[176:179]\n"
    "ds_read_b128 v[84:87], v16 offset:34816\n"
    "v_mfma_f32_16x16x32_bf16 v[180:183], a[60:63], v[92:95], v[180:183]\n"
    "s_waitcnt lgkmcnt(4)\n"
    "v_mfma_f32_16x16x32_bf16 v[152:155], a[64:67], v[64:67], v[152:155]\n"
    "ds_read_b128 v[88:91], v16 offset:36864\n"
    "v_mfma_f32_16x16x32_bf16 v[156:159], a[68:71], v[64:67], v[156:159]\n"
    "v_mfma_f32_16x16x32_bf16 v[160:163], a[64:67], v[68:71], v[160:163]\n"
    "ds_read_b128 v[92:95], v16 offset:38912\n"
    "v_mfma_f32_16x16x32_bf16 v[164:167], a[68:71], v[68:71], v[164:167]\n"
    "s_waitcnt lgkmcnt(4)\n"
    "v_mfma_f32_16x16x32_bf16 v[168:171], a[64:67], v[72:75], v[168:171]\n"
    "s_waitcnt vmcnt(15)\n"
    "s_barrier\n"
    "v_mfma_f32_16x16x32_bf16 v[172:175], a[68:71], v[72:75], v[172:175]\n"
    "ds_read_b128 v[64:67], v15 offset:40960\n"
    "v_mfma_f32_16x16x32_bf16 v[176:179], a[64:67], v[76:79], v[176:179]\n"
    "ds_read_b128 v[68:71], v15 offset:43008\n"
    "v_mfma_f32_16x16x32_bf16 v[180:183], a[68:71], v[76:79], v[180:183]\n"
    "s_waitcnt lgkmcnt(4)\n"
    "v_mfma_f32_16x16x32_bf16 v[152:155], a[72:75], v[80:83], v[152:155]\n"
    "ds_read_b128 v[72:75], v15 offset:45056\n"
    "v_mfma_f32_16x16x32_bf16 v[156:159], a[76:79], v[80:83], v[156:159]\n"
    "v_mfma_f32_16x16x32_bf16 v[160:163], a[72:75], v[84:87], v[160:163]\n"
    "ds_read_b128 v[76:79], v15 offset:47104\n"
    "v_mfma_f32_16x16x32_bf16 v[164:167], a[76:79], v[84:87], v[164:167]\n"
    "s_waitcnt lgkmcnt(4)\n"
    "v_mfma_f32_16x16x32_bf16 v[168:171], a[72:75], v[88:91], v[168:171]\n"
    "ds_read_b128 v[80:83], v16 offset:40960\n"
    "v_mfma_f32_16x16x32_bf16 v[172:175], a[76:79], v[88:91], v[172:175]\n"
    "v_mfma_f32_16x16x32_bf16 v[176:179], a[72:75], v[92:95], v[176:179]\n"
    "ds_read_b128 v[84:87], v16 offset:43008\n"
    "v_mfma_f32_16x16x32_bf16 v[180:183], a[76:79], v[92:95], v[180:183]\n"
    "s_waitcnt lgkmcnt(4)\n"
    "v_mfma_f32_16x16x32_bf16 v[152:155], a[80:83], v[64:67], v[152:155]\n"
    "ds_read_b128 v[88:91], v16 offset:45056\n"
    "v_mfma_f32_16x16x32_bf16 v[156:159], a[84:87], v[64:67], v[156:159]\n"
    "v_mfma_f32_16x16x32_bf16 v[160:163], a[80:83], v[68:71], v[160:163]\n"
    "ds_read_b128 v[92:95], v16 offset:47104\n"
    "v_mfma_f32_16x16x32_bf16 v[164:167], a[84:87], v[68:71], v[164:167]\n"
    "s_waitcnt lgkmcnt(4)\n"
    "v_mfma_f32_16x16x32_bf16 v[168:171], a[80:83], v[72:75], v[168:171]\n"
    "s_waitcnt vmcnt(12)\n"
    "s_barrier\n"
    "v_mfma_f32_16x16x32_bf16 v[172:175], a[84:87], v[72:75], v[172:175]\n"
    "ds_read_b128 v[64:67], v15 offset:49152\n"
    "s_add_u32 m0, s81, 8192\n"
    "v_mfma_f32_16x16x32_bf16 v[176:179], a[80:83], v[76:79], v[176:179]\n"
    "ds_read_b128 v[68:71], v15 offset:51200\n"
    "s_mul_i32 s76, s69, 1376\n"
    "v_mfma_f32_16x16x32_bf16 v[180:183], a[84:87], v[76:79], v[180:183]\n"
    "buffer_load_dwordx4 v9, s[44:47], s76 offen offset:0 lds\n"
    "s_waitcnt lgkmcnt(4)\n"
    "v_mfma_f32_16x16x32_bf16 v[152:155], a[88:91], v[80:83], v[152:155]\n"
    "ds_read_b128 v[72:75], v15 offset:53248\n"
    "v_mfma_f32_16x16x32_bf16 v[156:159], a[92:95], v[80:83], v[156:159]\n"
    "v_mfma_f32_16x16x32_bf16 v[160:163], a[88:91], v[84:87], v[160:163]\n"
    "ds_read_b128 v[76:79], v15 offset:55296\n"
    "v_mfma_f32_16x16x32_bf16 v[164:167], a[92:95], v[84:87], v[164:167]\n"
    "s_waitcnt lgkmcnt(4)\n"
    "v_mfma_f32_16x16x32_bf16 v[168:171], a[88:91], v[88:91], v[168:171]\n"
    "ds_read_b128 v[80:83], v16 offset:49152\n"
    "v_mfma_f32_16x16x32_bf16 v[172:175], a[92:95], v[88:91], v[172:175]\n"
    "v_mfma_f32_16x16x32_bf16 v[176:179], a[88:91], v[92:95], v[176:179]\n"
    "ds_read_b128 v[84:87], v16 offset:51200\n"
    "v_mfma_f32_16x16x32_bf16 v[180:183], a[92:95], v[92:95], v[180:183]\n"
    "s_waitcnt lgkmcnt(4)\n"
    "v_mfma_f32_16x16x32_bf16 v[152:155], a[96:99], v[64:67], v[152:155]\n"
    "ds_read_b128 v[88:91], v16 offset:53248\n"
    "v_mfma_f32_16x16x32_bf16 v[156:159], a[100:103], v[64:67], v[156:159]\n"
    "v_mfma_f32_16x16x32_bf16 v[160:163], a[96:99], v[68:71], v[160:163]\n"
    "ds_read_b128 v[92:95], v16 offset:55296\n"
    "v_mfma_f32_16x16x32_bf16 v[164:167], a[100:103], v[68:71], v[164:167]\n"
    "s_waitcnt lgkmcnt(4)\n"
    "v_mfma_f32_16x16x32_bf16 v[168:171], a[96:99], v[72:75], v[168:171]\n"
    "buffer_load_dwordx4 v10, s[44:47], s76 offen offset:1024 lds\n"
    "s_waitcnt vmcnt(12)\n"
    "s_barrier\n"
    "v_mfma_f32_16x16x32_bf16 v[172:175], a[100:103], v[72:75], v[172:175]\n"
    "ds_read_b128 v[64:67], v15 offset:57344\n"
    "v_mfma_f32_16x16x32_bf16 v[176:179], a[96:99], v[76:79], v[176:179]\n"
    "ds_read_b128 v[68:71], v15 offset:59392\n"
    "v_mfma_f32_16x16x32_bf16 v[180:183], a[100:103], v[76:79], v[180:183]\n"
    "s_waitcnt lgkmcnt(4)\n"
    "v_mfma_f32_16x16x32_bf16 v[152:155], a[104:107], v[80:83], v[152:155]\n"
    "ds_read_b128 v[72:75], v15 offset:61440\n"
    "v_mfma_f32_16x16x32_bf16 v[156:159], a[108:111], v[80:83], v[156:159]\n"
    "v_mfma_f32_16x16x32_bf16 v[160:163], a[104:107], v[84:87], v[160:163]\n"
    "ds_read_b128 v[76:79], v15 offset:63488\n"
    "v_mfma_f32_16x16x32_bf16 v[164:167], a[108:111], v[84:87], v[164:167]\n"
    "s_waitcnt lgkmcnt(4)\n"
    "v_mfma_f32_16x16x32_bf16 v[168:171], a[104:107], v[88:91], v[168:171]\n"
    "ds_read_b128 v[80:83], v16 offset:57344\n"
    "v_mfma_f32_16x16x32_bf16 v[172:175], a[108:111], v[88:91], v[172:175]\n"
    "v_mfma_f32_16x16x32_bf16 v[176:179], a[104:107], v[92:95], v[176:179]\n"
    "ds_read_b128 v[84:87], v16 offset:59392\n"
    "v_mfma_f32_16x16x32_bf16 v[180:183], a[108:111], v[92:95], v[180:183]\n"
    "s_waitcnt lgkmcnt(4)\n"
    "v_mfma_f32_16x16x32_bf16 v[152:155], a[112:115], v[64:67], v[152:155]\n"
    "ds_read_b128 v[88:91], v16 offset:61440\n"
    "v_mfma_f32_16x16x32_bf16 v[156:159], a[116:119], v[64:67], v[156:159]\n"
    "v_mfma_f32_16x16x32_bf16 v[160:163], a[112:115], v[68:71], v[160:163]\n"
    "ds_read_b128 v[92:95], v16 offset:63488\n"
    "v_mfma_f32_16x16x32_bf16 v[164:167], a[116:119], v[68:71], v[164:167]\n"
    "s_waitcnt lgkmcnt(4)\n"
    "v_mfma_f32_16x16x32_bf16 v[168:171], a[112:115], v[72:75], v[168:171]\n"
    "v_mfma_f32_16x16x32_bf16 v[172:175], a[116:119], v[72:75], v[172:175]\n"
    "v_mfma_f32_16x16x32_bf16 v[176:179], a[112:115], v[76:79], v[176:179]\n"
    "v_mfma_f32_16x16x32_bf16 v[180:183], a[116:119], v[76:79], v[180:183]\n"
    "s_waitcnt lgkmcnt(2)\n"
    "v_mfma_f32_16x16x32_bf16 v[152:155], a[120:123], v[80:83], v[152:155]\n"
    "v_mfma_f32_16x16x32_bf16 v[156:159], a[124:127], v[80:83], v[156:159]\n"
    "v_mfma_f32_16x16x32_bf16 v[160:163], a[120:123], v[84:87], v[160:163]\n"
    "v_mfma_f32_16x16x32_bf16 v[164:167], a[124:127], v[84:87], v[164:167]\n"
    "s_waitcnt lgkmcnt(0)\n"
    "v_mfma_f32_16x16x32_bf16 v[168:171], a[120:123], v[88:91], v[168:171]\n"
    "buffer_load_dwordx4 v[32:35], v22, s[56:59], s95 offen offset:0 sc1\n"
    "v_mfma_f32_16x16x32_bf16 v[172:175], a[124:127], v[88:91], v[172:175]\n"
    "v_mfma_f32_16x16x32_bf16 v[176:179], a[120:123], v[92:95], v[176:179]\n"
    "v_mfma_f32_16x16x32_bf16 v[180:183], a[124:127], v[92:95], v[180:183]\n"
    "v_fma_f32 v66, v184, v152, v153\n"
    "v_fma_f32 v67, v184, v160, v161\n"
    "v_fma_f32 v66, -v185, v160, v66\n"
    "v_fma_f32 v67, v185, v152, v67\n"
    "v_fma_f32 v70, v200, v168, v169\n"
    "s_nop 1\n"
    "v_fma_f32 v71, v200, v176, v177\n"
    "v_fma_f32 v70, -v201, v176, v70\n"
    "v_fma_f32 v71, v201, v168, v71\n"
    "v_fma_f32 v64, v184, v66, v154\n"
    "v_fma_f32 v65, v184, v67, v162\n"
    "v_fma_f32 v64, -v185, v67, v64\n"
    "v_fma_f32 v65, v185, v66, v65\n"
    "v_fma_f32 v68, v200, v70, v170\n"
    "v_fma_f32 v69, v200, v71, v178\n"
    "v_fma_f32 v68, -v201, v71, v68\n"
    "v_fma_f32 v69, v201, v70, v69\n"
    "v_fma_f32 v66, v184, v64, v155\n"
    "v_fma_f32 v67, v184, v65, v163\n"
    "v_fma_f32 v66, -v185, v65, v66\n"
    "v_fma_f32 v67, v185, v64, v67\n"
    "v_fma_f32 v70, v200, v68, v171\n"
    "v_fma_f32 v71, v200, v69, v179\n"
    "v_fma_f32 v70, -v201, v69, v70\n"
    "v_fma_f32 v71, v201, v68, v71\n"
    "v_fma_f32 v64, v184, v66, v156\n"
    "v_fma_f32 v65, v184, v67, v164\n"
    "v_fma_f32 v64, -v185, v67, v64\n"
    "v_fma_f32 v65, v185, v66, v65\n"
    "v_fma_f32 v68, v200, v70, v172\n"
    "v_fma_f32 v69, v200, v71, v180\n"
    "v_fma_f32 v68, -v201, v71, v68\n"
    "v_fma_f32 v69, v201, v70, v69\n"
    "v_fma_f32 v66, v184, v64, v157\n"
    "v_fma_f32 v67, v184, v65, v165\n"
    "v_fma_f32 v66, -v185, v65, v66\n"
    "v_fma_f32 v67, v185, v64, v67\n"
    "v_fma_f32 v70, v200, v68, v173\n"
    "v_fma_f32 v71, v200, v69, v181\n"
    "v_fma_f32 v70, -v201, v69, v70\n"
    "v_fma_f32 v71, v201, v68, v71\n"
    "v_fma_f32 v64, v184, v66, v158\n"
    "v_fma_f32 v65, v184, v67, v166\n"
    "v_fma_f32 v64, -v185, v67, v64\n"
    "v_fma_f32 v65, v185, v66, v65\n"
    "v_fma_f32 v68, v200, v70, v174\n"
    "v_fma_f32 v69, v200, v71, v182\n"
    "v_fma_f32 v68, -v201, v71, v68\n"
    "v_fma_f32 v69, v201, v70, v69\n"
    "v_fma_f32 v66, v184, v64, v159\n"
    "v_fma_f32 v67, v184, v65, v167\n"
    "v_fma_f32 v66, -v185, v65, v66\n"
    "v_fma_f32 v67, v185, v64, v67\n"
    "v_fma_f32 v70, v200, v68, v175\n"
    "v_fma_f32 v71, v200, v69, v183\n"
    "v_fma_f32 v70, -v201, v69, v70\n"
    "v_fma_f32 v71, v201, v68, v71\n"
    "buffer_load_dwordx4 v[36:39], v22, s[56:59], s95 offen offset:1024 sc1\n"
    "v_mov_b32 v72, v66\n"
    "v_mov_b32 v76, v66\n"
    "v_mov_b32 v73, v67\n"
    "v_mov_b32 v77, v67\n"
    "v_mov_b32 v74, v70\n"
    "v_mov_b32 v78, v70\n"
    "v_mov_b32 v75, v71\n"
    "v_mov_b32 v79, v71\n"
    "v_permlane16_swap_b32 v72, v76\n"
    "v_permlane16_swap_b32 v73, v77\n"
    "v_permlane16_swap_b32 v74, v78\n"
    "v_permlane16_swap_b32 v75, v79\n"
    "v_fma_f32 v80, v186, v72, v76\n"
    "v_fma_f32 v81, v186, v73, v77\n"
    "v_fma_f32 v80, -v187, v73, v80\n"
    "v_fma_f32 v81, v187, v72, v81\n"
    "v_fma_f32 v82, v202, v74, v78\n"
    "v_fma_f32 v83, v202, v75, v79\n"
    "v_fma_f32 v82, -v203, v75, v82\n"
    "v_fma_f32 v83, v203, v74, v83\n"
    "v_mov_b32 v84, v80\n"
    "v_mov_b32 v85, v81\n"
    "v_mov_b32 v86, v82\n"
    "v_mov_b32 v87, v83\n"
    "v_permlane32_swap_b32 v80, v84\n"
    "v_permlane32_swap_b32 v81, v85\n"
    "v_permlane32_swap_b32 v82, v86\n"
    "v_permlane32_swap_b32 v83, v87\n"
    "v_fma_f32 v88, v188, v80, v84\n"
    "v_fma_f32 v89, v188, v81, v85\n"
    "v_fma_f32 v88, -v189, v81, v88\n"
    "v_fma_f32 v89, v189, v80, v89\n"
    "v_fma_f32 v90, v204, v82, v86\n"
    "v_fma_f32 v91, v204, v83, v87\n"
    "v_fma_f32 v90, -v205, v83, v90\n"
    "v_fma_f32 v91, v205, v82, v91\n"
    "ds_write_b64 v108, v[88:89] offset:0\n"
    "ds_write_b64 v108, v[90:91] offset:128\n"
    "v_cndmask_b32 v72, 0, v72, s[16:17]\n"
    "v_cndmask_b32 v73, 0, v73, s[16:17]\n"
    "v_cndmask_b32 v74, 0, v74, s[16:17]\n"
    "v_cndmask_b32 v75, 0, v75, s[16:17]\n"
    "v_fma_f32 v220, v192, v80, v72\n"
    "v_fma_f32 v221, v192, v81, v73\n"
    "v_fma_f32 v220, -v193, v81, v220\n"
    "v_fma_f32 v221, v193, v80, v221\n"
    "v_fma_f32 v222, v208, v82, v74\n"
    "v_fma_f32 v223, v208, v83, v75\n"
    "v_fma_f32 v222, -v209, v83, v222\n"
    "v_fma_f32 v223, v209, v82, v223\n"
    "s_waitcnt lgkmcnt(0)\n"
    "s_barrier\n"
    "ds_read_b64 v[72:73], v109 offset:0\n"
    "ds_read_b64 v[74:75], v109 offset:128\n"
    "ds_read_b64 v[76:77], v109 offset:256\n"
    "ds_read_b64 v[78:79], v109 offset:384\n"
    "ds_read_b64 v[80:81], v109 offset:512\n"
    "ds_read_b64 v[82:83], v109 offset:640\n"
    "ds_read_b64 v[84:85], v109 offset:768\n"
    "ds_read_b64 v[86:87], v109 offset:896\n"
    "s_waitcnt lgkmcnt(5)\n"
    "v_fma_f32 v224, v190, v72, v76\n"
    "v_fma_f32 v225, v190, v73, v77\n"
    "v_fma_f32 v224, -v191, v73, v224\n"
    "buffer_load_dwordx4 v[40:43], v22, s[56:59], s95 offen offset:2048 sc1\n"
    "v_fma_f32 v225, v191, v72, v225\n"
    "s_waitcnt lgkmcnt(4)\n"
    "v_fma_f32 v226, v206, v74, v78\n"
    "v_fma_f32 v227, v206, v75, v79\n"
    "v_fma_f32 v226, -v207, v75, v226\n"
    "v_fma_f32 v227, v207, v74, v227\n"
    "s_waitcnt lgkmcnt(3)\n"
    "v_fma_f32 v228, v190, v224, v80\n"
    "v_fma_f32 v229, v190, v225, v81\n"
    "v_fma_f32 v228, -v191, v225, v228\n"
    "v_fma_f32 v229, v191, v224, v229\n"
    "s_waitcnt lgkmcnt(2)\n"
    "v_fma_f32 v230, v206, v226, v82\n"
    "v_fma_f32 v231, v206, v227, v83\n"
    "v_fma_f32 v230, -v207, v227, v230\n"
    "v_fma_f32 v231, v207, v226, v231\n"
    "s_waitcnt lgkmcnt(1)\n"
    "v_fma_f32 v232, v190, v228, v84\n"
    "v_fma_f32 v233, v190, v229, v85\n"
    "v_fma_f32 v232, -v191, v229, v232\n"
    "v_fma_f32 v233, v191, v228, v233\n"
    "s_waitcnt lgkmcnt(0)\n"
    "v_fma_f32 v234, v206, v230, v86\n"
    "v_fma_f32 v235, v206, v231, v87\n"
    "v_fma_f32 v234, -v207, v231, v234\n"
    "v_fma_f32 v235, v207, v230, v235\n"
    "v_cndmask_b32 v68, v232, v234, s[16:17]\n"
    "v_cndmask_b32 v70, v233, v235, s[16:17]\n"
    "v_mov_b32 v69, s64\n"
    "buffer_load_dwordx4 v[44:47], v22, s[56:59], s95 offen offset:3072 sc1\n"
    "v_mov_b32 v71, s64\n"
    "buffer_store_dwordx4 v[68:71], v114, s[56:59], 0 offen sc1\n"
    "v_mov_b32 v64, 0\n"
    "v_mov_b32 v65, 0\n"
    "v_mov_b32 v66, 0\n"
    "v_mov_b32 v67, 0\n"
    "buffer_load_dwordx4 v[48:51], v31, s[56:59], s95 offen offset:0 sc1\n"
    "buffer_load_dwordx4 v[52:55], v31, s[56:59], s95 offen offset:1024 sc1\n"
    "buffer_load_dwordx4 v[56:59], v31, s[56:59], s95 offen offset:2048 sc1\n"
    "buffer_load_dwordx4 v[60:63], v31, s[56:59], s95 offen offset:3072 sc1\n"
    "s_cmp_eq_u32 s60, 1\n"
    "s_cselect_b64 vcc, -1, 0\n"
    "v_cndmask_b32 v64, v64, v72, vcc\n"
    "v_cndmask_b32 v65, v65, v73, vcc\n"
    "v_cndmask_b32 v66, v66, v74, vcc\n"
    "v_cndmask_b32 v67, v67, v75, vcc\n"
    "s_cmp_eq_u32 s60, 2\n"
    "s_cselect_b64 vcc, -1, 0\n"
    "v_cndmask_b32 v64, v64, v224, vcc\n"
    "v_cndmask_b32 v65, v65, v225, vcc\n"
    "v_cndmask_b32 v66, v66, v226, vcc\n"
    "v_cndmask_b32 v67, v67, v227, vcc\n"
    "s_cmp_eq_u32 s60, 3\n"
    "s_cselect_b64 vcc, -1, 0\n"
    "v_cndmask_b32 v64, v64, v228, vcc\n"
    "v_cndmask_b32 v65, v65, v229, vcc\n"
    "v_cndmask_b32 v66, v66, v230, vcc\n"
    "v_cndmask_b32 v67, v67, v231, vcc\n"
    "v_fma_f32 v220, v194, v64, v220\n"
    "v_fma_f32 v221, v194, v65, v221\n"
    "s_waitcnt vmcnt(17)\n"
    "v_fma_f32 v220, -v195, v65, v220\n"
    "v_fma_f32 v221, v195, v64, v221\n"
    "v_fma_f32 v222, v210, v66, v222\n"
    "ds_read_b128 a[128:131], v18 offset:12288\n"
    "v_fma_f32 v223, v210, v67, v223\n"
    "v_fma_f32 v222, -v211, v67, v222\n"
    "v_fma_f32 v223, v211, v66, v223\n"
    "ds_read_b128 a[132:135], v19 offset:12288\n"
    "s_add_u32 m0, s81, 16384\n"
    "s_mul_i32 s76, s70, 1376\n"
    "buffer_load_dwordx4 v9, s[44:47], s76 offen offset:0 lds\n"
    "buffer_load_dwordx4 v10, s[44:47], s76 offen offset:1024 lds\n"
    "s_add_u32 m0, s81, 24576\n"
    "s_mul_i32 s76, s71, 1376\n"
    "buffer_load_dwordx4 v9, s[44:47], s76 offen offset:0 lds\n"
    "buffer_load_dwordx4 v10, s[44:47], s76 offen offset:1024 lds\n"
    "s_add_u32 m0, s81, 32768\n"
    "s_mul_i32 s76, s72, 1376\n"
    "buffer_load_dwordx4 v9, s[44:47], s76 offen offset:0 lds\n"
    "buffer_load_dwordx4 v10, s[44:47], s76 offen offset:1024 lds\n"
    "s_add_u32 m0, s81, 40960\n"
    "s_mul_i32 s76, s73, 1376\n"
    "buffer_load_dwordx4 v9, s[44:47], s76 offen offset:0 lds\n"
    "buffer_load_dwordx4 v10, s[44:47], s76 offen offset:1024 lds\n"
    "s_add_u32 m0, s81, 49152\n"
    "s_mul_i32 s76, s74, 1376\n"
    "buffer_load_dwordx4 v9, s[44:47], s76 offen offset:0 lds\n"
    "buffer_load_dwordx4 v10, s[44:47], s76 offen offset:1024 lds\n"
    "s_add_u32 m0, s81, 57344\n"
    "s_mul_i32 s76, s75, 1376\n"
    "buffer_load_dwordx4 v9, s[44:47], s76 offen offset:0 lds\n"
    "buffer_load_dwordx4 v10, s[44:47], s76 offen offset:1024 lds\n"
    "s_waitcnt vmcnt(20)\n"
    "v_xor_b32 v0, s64, v33\n"
    "v_bitop3_b32 v0, v0, v35, s64 bitop3:0xf6\n"
    "s_waitcnt vmcnt(19)\n"
    "v_bitop3_b32 v0, v0, v37, s64 bitop3:0xf6\n"
    "v_bitop3_b32 v0, v0, v39, s64 bitop3:0xf6\n"
    "s_waitcnt vmcnt(18)\n"
    "v_bitop3_b32 v0, v0, v41, s64 bitop3:0xf6\n"
    "v_bitop3_b32 v0, v0, v43, s64 bitop3:0xf6\n"
    "s_waitcnt vmcnt(17)\n"
    "v_bitop3_b32 v0, v0, v45, s64 bitop3:0xf6\n"
    "v_bitop3_b32 v0, v0, v47, s64 bitop3:0xf6\n"
    "s_waitcnt vmcnt(15)\n"
    "v_bitop3_b32 v0, v0, v49, s64 bitop3:0xf6\n"
    "v_bitop3_b32 v0, v0, v51, s64 bitop3:0xf6\n"
    "s_waitcnt vmcnt(14)\n"
    "v_bitop3_b32 v0, v0, v53, s64 bitop3:0xf6\n"
    "v_bitop3_b32 v0, v0, v55, s64 bitop3:0xf6\n"
    "s_waitcnt vmcnt(13)\n"
    "v_bitop3_b32 v0, v0, v57, s64 bitop3:0xf6\n"
    "v_bitop3_b32 v0, v0, v59, s64 bitop3:0xf6\n"
    "s_waitcnt vmcnt(12)\n"
    "v_bitop3_b32 v0, v0, v61, s64 bitop3:0xf6\n"
    "v_bitop3_b32 v0, v0, v63, s64 bitop3:0xf6\n"
    "v_cmp_eq_u32 vcc, 0, v0\n"
    "s_nop 0\n"
    "s_cmp_eq_u64 vcc, -1\n"
    "s_cbranch_scc1 L0ok1_%=\n"
    "s_mov_b32 s80, 0x4000\n"
    "L0spin2_%=:\n"
    "s_sleep 1\n"
    "buffer_load_dwordx4 v[32:35], v22, s[56:59], s95 offen offset:0 sc1\n"
    "buffer_load_dwordx4 v[36:39], v22, s[56:59], s95 offen offset:1024 sc1\n"
    "buffer_load_dwordx4 v[40:43], v22, s[56:59], s95 offen offset:2048 sc1\n"
    "buffer_load_dwordx4 v[44:47], v22, s[56:59], s95 offen offset:3072 sc1\n"
    "buffer_load_dwordx4 v[48:51], v31, s[56:59], s95 offen offset:0 sc1\n"
    "buffer_load_dwordx4 v[52:55], v31, s[56:59], s95 offen offset:1024 sc1\n"
    "buffer_load_dwordx4 v[56:59], v31, s[56:59], s95 offen offset:2048 sc1\n"
    "buffer_load_dwordx4 v[60:63], v31, s[56:59], s95 offen offset:3072 sc1\n"
    "s_waitcnt vmcnt(0)\n"
    "v_xor_b32 v0, s64, v33\n"
    "v_bitop3_b32 v0, v0, v35, s64 bitop3:0xf6\n"
    "v_bitop3_b32 v0, v0, v37, s64 bitop3:0xf6\n"
    "v_bitop3_b32 v0, v0, v39, s64 bitop3:0xf6\n"
    "v_bitop3_b32 v0, v0, v41, s64 bitop3:0xf6\n"
    "v_bitop3_b32 v0, v0, v43, s64 bitop3:0xf6\n"
    "v_bitop3_b32 v0, v0, v45, s64 bitop3:0xf6\n"
    "v_bitop3_b32 v0, v0, v47, s64 bitop3:0xf6\n"
    "v_bitop3_b32 v0, v0, v49, s64 bitop3:0xf6\n"
    "v_bitop3_b32 v0, v0, v51, s64 bitop3:0xf6\n"
    "v_bitop3_b32 v0, v0, v53, s64 bitop3:0xf6\n"
    "v_bitop3_b32 v0, v0, v55, s64 bitop3:0xf6\n"
    "v_bitop3_b32 v0, v0, v57, s64 bitop3:0xf6\n"
    "v_bitop3_b32 v0, v0, v59, s64 bitop3:0xf6\n"
    "v_bitop3_b32 v0, v0, v61, s64 bitop3:0xf6\n"
    "v_bitop3_b32 v0, v0, v63, s64 bitop3:0xf6\n"
    "v_cmp_eq_u32 vcc, 0, v0\n"
    "s_nop 0\n"
    "s_cmp_eq_u64 vcc, -1\n"
    "s_cbranch_scc1 L0ok1_%=\n"
    "s_sub_u32 s80, s80, 1\n"
    "s_cmp_lg_u32 s80, 0\n"
    "s_cbranch_scc1 L0spin2_%=\n"
    "L0ok1_%=:\n"
    "v_add_f32 v32, v32, v36\n"
    "v_add_f32 v34, v34, v38\n"
    "v_add_f32 v32, v32, v40\n"
    "v_add_f32 v34, v34, v42\n"
    "v_add_f32 v32, v32, v44\n"
    "v_add_f32 v34, v34, v46\n"
    "v_add_f32 v32, v32, v48\n"
    "v_add_f32 v34, v34, v50\n"
    "v_add_f32 v32, v32, v52\n"
    "v_add_f32 v34, v34, v54\n"
    "v_add_f32 v32, v32, v56\n"
    "v_add_f32 v34, v34, v58\n"
    "v_add_f32 v32, v32, v60\n"
    "v_add_f32 v34, v34, v62\n"
    "v_mov_b32 v33, v32\n"
    "v_mov_b32 v35, v34\n"
    "s_nop 0\n"
    "v_permlane32_swap_b32 v32, v33\n"
    "v_permlane32_swap_b32 v34, v35\n"
    "v_add_f32 v32, v32, v33\n"
    "v_add_f32 v34, v34, v35\n"
    "v_mul_f32 v32, 0x3abe82fa, v32\n"
    "v_mul_f32 v34, 0x3abe82fa, v34\n"
    "v_fma_f32 v34, -v32, v32, v34\n"
    "v_max_f32 v34, 0, v34\n"
    "v_add_f32 v34, 0x3727c5ac, v34\n"
    "v_rsq_f32 v34, v34\n"
    "v_cmp_gt_u32 vcc, 32, v4\n"
    "v_mul_f32 v32, -v32, v34\n"
    "s_nop 0\n"
    "v_cndmask_b32 v32, v32, v34, vcc\n"
    "ds_write_b32 v106, v32\n"
    "ds_read_b128 v[36:39], v107 offset:0\n"
    "ds_read_b128 v[40:43], v107 offset:16\n"
    "ds_read_b128 v[44:47], v107 offset:32\n"
    "ds_read_b128 v[48:51], v107 offset:48\n"
    "s_waitcnt lgkmcnt(1)\n"
    "v_pk_fma_f32 v[120:121], v[120:121], v[36:37], v[44:45]\n"
    "v_pk_fma_f32 v[122:123], v[122:123], v[38:39], v[46:47]\n"
    "s_waitcnt lgkmcnt(0)\n"
    "v_pk_fma_f32 v[124:125], v[124:125], v[40:41], v[48:49]\n"
    "v_pk_fma_f32 v[126:127], v[126:127], v[42:43], v[50:51]\n"
    "v_cvt_pk_bf16_f32 v52, v120, v121\n"
    "v_cvt_pk_bf16_f32 v53, v122, v123\n"
    "v_cvt_pk_bf16_f32 v54, v124, v125\n"
    "v_cvt_pk_bf16_f32 v55, v126, v127\n"
    "buffer_store_dwordx4 v[52:55], v23, s[56:59], s86 offen offset:0 sc1\n"
    "v_pk_fma_f32 v[128:129], v[128:129], v[36:37], v[44:45]\n"
    "v_pk_fma_f32 v[130:131], v[130:131], v[38:39], v[46:47]\n"
    "v_pk_fma_f32 v[132:133], v[132:133], v[40:41], v[48:49]\n"
    "v_pk_fma_f32 v[134:135], v[134:135], v[42:43], v[50:51]\n"
    "v_cvt_pk_bf16_f32 v56, v128, v129\n"
    "v_cvt_pk_bf16_f32 v57, v130, v131\n"
    "v_cvt_pk_bf16_f32 v58, v132, v133\n"
    "v_cvt_pk_bf16_f32 v59, v134, v135\n"
    "buffer_store_dwordx4 v[56:59], v23, s[56:59], s86 offen offset:512 sc1\n"
    "v_pk_fma_f32 v[136:137], v[136:137], v[36:37], v[44:45]\n"
    "v_pk_fma_f32 v[138:139], v[138:139], v[38:39], v[46:47]\n"
    "v_pk_fma_f32 v[140:141], v[140:141], v[40:41], v[48:49]\n"
    "v_pk_fma_f32 v[142:143], v[142:143], v[42:43], v[50:51]\n"
    "v_cvt_pk_bf16_f32 v60, v136, v137\n"
    "v_cvt_pk_bf16_f32 v61, v138, v139\n"
    "v_cvt_pk_bf16_f32 v62, v140, v141\n"
    "v_cvt_pk_bf16_f32 v63, v142, v143\n"
    "buffer_store_dwordx4 v[60:63], v23, s[56:59], s86 offen offset:1024 sc1\n"
    "v_mov_b32 v24, s64\n"
    "v_cmp_eq_u32 vcc, 0, v4\n"
    "v_mov_b32 v23, 0x40000000\n"
    "s_nop 0\n"
    "v_cndmask_b32 v23, v23, 0, vcc\n"
    "s_barrier\n"
    "ds_read_b64_tr_b16 v[64:65], v115 offset:0\n"
    "ds_read_b64_tr_b16 v[66:67], v115 offset:1024\n"
    "ds_read_b64_tr_b16 v[68:69], v116 offset:0\n"
    "ds_read_b64_tr_b16 v[70:71], v116 offset:1024\n"
    "ds_read_b64_tr_b16 v[72:73], v117 offset:0\n"
    "ds_read_b64_tr_b16 v[74:75], v117 offset:1024\n"
    "ds_read_b64_tr_b16 v[76:77], v118 offset:0\n"
    "ds_read_b64_tr_b16 v[78:79], v118 offset:1024\n"
    "ds_read_b64_tr_b16 v[80:81], v115 offset:4096\n"
    "ds_read_b64_tr_b16 v[82:83], v115 offset:5120\n"
    "ds_read_b64_tr_b16 v[84:85], v116 offset:4096\n"
    "ds_read_b64_tr_b16 v[86:87], v116 offset:5120\n"
    "s_waitcnt lgkmcnt(8)\n"
    "v_mfma_f32_16x16x32_bf16 v[120:123], v[64:67], a[0:3], 0\n"
    "ds_read_b64_tr_b16 v[88:89], v117 offset:4096\n"
    "ds_read_b128 a[136:139], v20 offset:12288\n"
    "v_mfma_f32_16x16x32_bf16 v[124:127], v[64:67], a[4:7], 0\n"
    "ds_read_b64_tr_b16 v[90:91], v117 offset:5120\n"
    "ds_read_b128 a[140:143], v21 offset:12288\n"
    "v_mfma_f32_16x16x32_bf16 v[128:131], v[68:71], a[0:3], 0\n"
    "ds_read_b64_tr_b16 v[92:93], v118 offset:4096\n"
    "v_mfma_f32_16x16x32_bf16 v[132:135], v[68:71], a[4:7], 0\n"
    "ds_read_b64_tr_b16 v[94:95], v118 offset:5120\n"
    "ds_read_b128 a[144:147], v18 offset:16384\n"
    "s_waitcnt lgkmcnt(11)\n"
    "v_mfma_f32_16x16x32_bf16 v[136:139], v[72:75], a[0:3], 0\n"
    "buffer_load_dwordx4 v[32:35], v2, s[56:59], 0 offen sc1\n"
    "buffer_load_dwordx4 v[36:39], v3, s[56:59], 0 offen sc1\n"
    "s_barrier\n"
    "v_mfma_f32_16x16x32_bf16 v[140:143], v[72:75], a[4:7], 0\n"
    "ds_read_b64_tr_b16 v[64:65], v115 offset:8192\n"
    "ds_read_b64_tr_b16 v[66:67], v115 offset:9216\n"
    "v_mfma_f32_16x16x32_bf16 v[144:147], v[76:79], a[0:3], 0\n"
    "ds_read_b64_tr_b16 v[68:69], v116 offset:8192\n"
    "ds_read_b128 a[148:151], v19 offset:16384\n"
    "v_mfma_f32_16x16x32_bf16 v[148:151], v[76:79], a[4:7], 0\n"
    "s_waitcnt lgkmcnt(11)\n"
    "ds_read_b64_tr_b16 v[70:71], v116 offset:9216\n"
    "ds_read_b128 a[152:155], v20 offset:16384\n"
    "v_mfma_f32_16x16x32_bf16 v[120:123], v[80:83], a[8:11], v[120:123]\n"
    "ds_read_b64_tr_b16 v[72:73], v117 offset:8192\n"
    "ds_read_b128 a[156:159], v21 offset:16384\n"
    "v_mfma_f32_16x16x32_bf16 v[124:127], v[80:83], a[12:15], v[124:127]\n"
    "s_waitcnt lgkmcnt(11)\n"
    "ds_read_b64_tr_b16 v[74:75], v117 offset:9216\n"
    "v_mfma_f32_16x16x32_bf16 v[128:131], v[84:87], a[8:11], v[128:131]\n"
    "ds_read_b64_tr_b16 v[76:77], v118 offset:8192\n"
    "v_mfma_f32_16x16x32_bf16 v[132:135], v[84:87], a[12:15], v[132:135]\n"
    "ds_read_b64_tr_b16 v[78:79], v118 offset:9216\n"
    "s_waitcnt lgkmcnt(12)\n"
    "v_mfma_f32_16x16x32_bf16 v[136:139], v[88:91], a[8:11], v[136:139]\n"
    "ds_read_b64_tr_b16 v[80:81], v115 offset:12288\n"
    "v_mfma_f32_16x16x32_bf16 v[140:143], v[88:91], a[12:15], v[140:143]\n"
    "ds_read_b64_tr_b16 v[82:83], v115 offset:13312\n"
    "v_mfma_f32_16x16x32_bf16 v[144:147], v[92:95], a[8:11], v[144:147]\n"
    "ds_read_b64_tr_b16 v[84:85], v116 offset:12288\n"
    "v_mfma_f32_16x16x32_bf16 v[148:151], v[92:95], a[12:15], v[148:151]\n"
    "s_waitcnt lgkmcnt(11)\n"
    "ds_read_b64_tr_b16 v[86:87], v116 offset:13312\n"
    "s_waitcnt lgkmcnt(10)\n"
    "v_mfma_f32_16x16x32_bf16 v[120:123], v[64:67], a[16:19], v[120:123]\n"
    "ds_read_b64_tr_b16 v[88:89], v117 offset:12288\n"
    "v_mfma_f32_16x16x32_bf16 v[124:127], v[64:67], a[20:23], v[124:127]\n"
    "ds_read_b64_tr_b16 v[90:91], v117 offset:13312\n"
    "s_add_u32 m0, s67, 0\n"
    "v_mfma_f32_16x16x32_bf16 v[128:131], v[68:71], a[16:19], v[128:131]\n"
    "ds_read_b64_tr_b16 v[92:93], v118 offset:12288\n"
    "s_bfe_u32 s77, s24, 0x40008\n"
    "v_mfma_f32_16x16x32_bf16 v[132:135], v[68:71], a[20:23], v[132:135]\n"
    "ds_read_b64_tr_b16 v[94:95], v118 offset:13312\n"
    "s_lshl_b32 s76, s77, 7\n"
    "s_waitcnt lgkmcnt(8)\n"
    "v_mfma_f32_16x16x32_bf16 v[136:139], v[72:75], a[16:19], v[136:139]\n"
    "buffer_load_dwordx4 v5, s[52:55], s76 offen offset:0 lds\n"
    "buffer_load_dwordx4 v6, s[52:55], s76 offen offset:1024 lds\n"
    "buffer_load_dwordx4 v7, s[52:55], s76 offen offset:2048 lds\n"
    "buffer_load_dwordx4 v8, s[52:55], s76 offen offset:3072 lds\n"
    "s_add_u32 m0, s67, 4096\n"
    "s_bfe_u32 s77, s24, 0x4000c\n"
    "s_lshl_b32 s76, s77, 7\n"
    "buffer_load_dwordx4 v5, s[52:55], s76 offen offset:0 lds\n"
    "buffer_load_dwordx4 v6, s[52:55], s76 offen offset:1024 lds\n"
    "buffer_load_dwordx4 v7, s[52:55], s76 offen offset:2048 lds\n"
    "buffer_load_dwordx4 v8, s[52:55], s76 offen offset:3072 lds\n"
    "s_add_u32 m0, s67, 8192\n"
    "s_bfe_u32 s77, s24, 0x40010\n"
    "s_lshl_b32 s76, s77, 7\n"
    "s_waitcnt vmcnt(10)\n"
    "buffer_load_dwordx4 v5, s[52:55], s76 offen offset:0 lds\n"
    "buffer_load_dwordx4 v6, s[52:55], s76 offen offset:1024 lds\n"
    "buffer_load_dwordx4 v7, s[52:55], s76 offen offset:2048 lds\n"
    "buffer_load_dwordx4 v8, s[52:55], s76 offen offset:3072 lds\n"
    "s_barrier\n"
    "s_add_u32 s80, s14, 0x1000\n"
    "buffer_store_dwordx2 v[24:25], v23, s[56:59], s80 offen sc1\n"
    "v_mfma_f32_16x16x32_bf16 v[140:143], v[72:75], a[20:23], v[140:143]\n"
    "ds_read_b64_tr_b16 v[64:65], v115 offset:16384\n"
    "ds_read_b64_tr_b16 v[66:67], v115 offset:17408\n"
    "v_mfma_f32_16x16x32_bf16 v[144:147], v[76:79], a[16:19], v[144:147]\n"
    "ds_read_b64_tr_b16 v[68:69], v116 offset:16384\n"
    "v_mfma_f32_16x16x32_bf16 v[148:151], v[76:79], a[20:23], v[148:151]\n"
    "ds_read_b64_tr_b16 v[70:71], v116 offset:17408\n"
    "s_waitcnt lgkmcnt(8)\n"
    "v_mfma_f32_16x16x32_bf16 v[120:123], v[80:83], a[24:27], v[120:123]\n"
    "ds_read_b64_tr_b16 v[72:73], v117 offset:16384\n"
    "v_mfma_f32_16x16x32_bf16 v[124:127], v[80:83], a[28:31], v[124:127]\n"
    "ds_read_b64_tr_b16 v[74:75], v117 offset:17408\n"
    "v_mfma_f32_16x16x32_bf16 v[128:131], v[84:87], a[24:27], v[128:131]\n"
    "ds_read_b64_tr_b16 v[76:77], v118 offset:16384\n"
    "v_mfma_f32_16x16x32_bf16 v[132:135], v[84:87], a[28:31], v[132:135]\n"
    "ds_read_b64_tr_b16 v[78:79], v118 offset:17408\n"
    "s_waitcnt lgkmcnt(8)\n"
    "v_mfma_f32_16x16x32_bf16 v[136:139], v[88:91], a[24:27], v[136:139]\n"
    "ds_read_b64_tr_b16 v[80:81], v115 offset:20480\n"
    "v_mfma_f32_16x16x32_bf16 v[140:143], v[88:91], a[28:31], v[140:143]\n"
    "ds_read_b64_tr_b16 v[82:83], v115 offset:21504\n"
    "v_mfma_f32_16x16x32_bf16 v[144:147], v[92:95], a[24:27], v[144:147]\n"
    "ds_read_b64_tr_b16 v[84:85], v116 offset:20480\n"
    "s_add_u32 m0, s67, 12288\n"
    "v_mfma_f32_16x16x32_bf16 v[148:151], v[92:95], a[28:31], v[148:151]\n"
    "ds_read_b64_tr_b16 v[86:87], v116 offset:21504\n"
    "s_bfe_u32 s77, s24, 0x40014\n"
    "s_waitcnt lgkmcnt(8)\n"
    "v_mfma_f32_16x16x32_bf16 v[120:123], v[64:67], a[32:35], v[120:123]\n"
    "ds_read_b64_tr_b16 v[88:89], v117 offset:20480\n"
    "s_lshl_b32 s76, s77, 7\n"
    "v_mfma_f32_16x16x32_bf16 v[124:127], v[64:67], a[36:39], v[124:127]\n"
    "ds_read_b64_tr_b16 v[90:91], v117 offset:21504\n"
    "buffer_load_dwordx4 v5, s[52:55], s76 offen offset:0 lds\n"
    "v_mfma_f32_16x16x32_bf16 v[128:131], v[68:71], a[32:35], v[128:131]\n"
    "ds_read_b64_tr_b16 v[92:93], v118 offset:20480\n"
    "buffer_load_dwordx4 v6, s[52:55], s76 offen offset:1024 lds\n"
    "v_mfma_f32_16x16x32_bf16 v[132:135], v[68:71], a[36:39], v[132:135]\n"
    "ds_read_b64_tr_b16 v[94:95], v118 offset:21504\n"
    "s_waitcnt lgkmcnt(8)\n"
    "v_mfma_f32_16x16x32_bf16 v[136:139], v[72:75], a[32:35], v[136:139]\n"
    "buffer_load_dwordx4 v7, s[52:55], s76 offen offset:2048 lds\n"
    "buffer_load_dwordx4 v8, s[52:55], s76 offen offset:3072 lds\n"
    "s_barrier\n"
    "v_mfma_f32_16x16x32_bf16 v[140:143], v[72:75], a[36:39], v[140:143]\n"
    "ds_read_b64_tr_b16 v[64:65], v115 offset:24576\n"
    "ds_read_b64_tr_b16 v[66:67], v115 offset:25600\n"
    "v_mfma_f32_16x16x32_bf16 v[144:147], v[76:79], a[32:35], v[144:147]\n"
    "ds_read_b64_tr_b16 v[68:69], v116 offset:24576\n"
    "s_waitcnt vmcnt(13)\n"
    "v_mfma_f32_16x16x32_bf16 v[148:151], v[76:79], a[36:39], v[148:151]\n"
    "ds_read_b64_tr_b16 v[70:71], v116 offset:25600\n"
    "ds_read_b128 a[160:163], v18 offset:0\n"
    "v_cmp_gt_u32 vcc, 0x40000000, v2\n"
    "s_nop 1\n"
    "v_cndmask_b32 v40, 0, v25, vcc\n"
    "v_cmp_gt_u32 vcc, 0x40000000, v3\n"
    "s_nop 1\n"
    "v_cndmask_b32 v41, 0, v25, vcc\n"
    "v_xor_b32 v0, v40, v33\n"
    "v_bitop3_b32 v0, v0, v35, v40 bitop3:0xf6\n"
    "v_bitop3_b32 v0, v0, v37, v41 bitop3:0xf6\n"
    "v_bitop3_b32 v0, v0, v39, v41 bitop3:0xf6\n"
    "v_cmp_eq_u32 vcc, 0, v0\n"
    "s_nop 0\n"
    "s_cmp_eq_u64 vcc, -1\n"
    "s_cbranch_scc1 L0ok3_%=\n"
    "s_mov_b32 s80, 0x4000\n"
    "L0spin4_%=:\n"
    "s_sleep 1\n"
    "buffer_load_dwordx4 v[32:35], v2, s[56:59], 0 offen sc1\n"
    "buffer_load_dwordx4 v[36:39], v3, s[56:59], 0 offen sc1\n"
    "s_waitcnt vmcnt(0)\n"
    "v_xor_b32 v0, v40, v33\n"
    "v_bitop3_b32 v0, v0, v35, v40 bitop3:0xf6\n"
    "v_bitop3_b32 v0, v0, v37, v41 bitop3:0xf6\n"
    "v_bitop3_b32 v0, v0, v39, v41 bitop3:0xf6\n"
    "v_cmp_eq_u32 vcc, 0, v0\n"
    "s_nop 0\n"
    "s_cmp_eq_u64 vcc, -1\n"
    "s_cbranch_scc1 L0ok3_%=\n"
    "s_sub_u32 s80, s80, 1\n"
    "s_cmp_lg_u32 s80, 0\n"
    "s_cbranch_scc1 L0spin4_%=\n"
    "L0ok3_%=:\n"
    "v_mul_f32 v42, v216, v32\n"
    "v_mul_f32 v43, v216, v34\n"
    "ds_read_b128 a[164:167], v19 offset:0\n"
    "v_fma_f32 v42, -v217, v34, v42\n"
    "v_fma_f32 v43, v217, v32, v43\n"
    "v_fma_f32 v42, v218, v36, v42\n"
    "ds_read_b128 a[168:171], v20 offset:0\n"
    "v_fma_f32 v43, v218, v38, v43\n"
    "v_fma_f32 v42, -v219, v38, v42\n"
    "v_fma_f32 v43, v219, v36, v43\n"
    "s_waitcnt lgkmcnt(11)\n"
    "ds_read_b128 a[172:175], v21 offset:0\n"
    "v_mov_b32 v44, v42\n"
    "v_mov_b32 v45, v43\n"
    "s_nop 0\n"
    "v_permlane32_swap_b32 v42, v44\n"
    "v_permlane32_swap_b32 v43, v45\n"
    "v_add_f32 v42, v42, v44\n"
    "v_add_f32 v43, v43, v45\n"
    "ds_write_b64 v110, v[42:43]\n"
    "s_lshl_b32 s79, s94, 16\n"
    "s_lshl_b32 s78, s91, 11\n"
    "s_add_u32 s79, s79, s78\n"
    "s_add_u32 s97, s79, 0x700000\n"
    "s_add_u32 s93, s97, 0x8000\n"
    "s_waitcnt lgkmcnt(0)\n"
    "s_barrier\n"
    "ds_read_b64 v[48:49], v111 offset:0\n"
    "ds_read_b64 v[50:51], v111 offset:128\n"
    "ds_read_b64 v[52:53], v111 offset:256\n"
    "ds_read_b64 v[54:55], v111 offset:384\n"
    "ds_read_b64 v[56:57], v111 offset:512\n"
    "ds_read_b64 v[58:59], v111 offset:640\n"
    "ds_read_b64 v[60:61], v111 offset:768\n"
    "ds_read_b64 v[62:63], v111 offset:896\n"
    "s_waitcnt lgkmcnt(5)\n"
    "v_pk_add_f32 v[48:49], v[48:49], v[52:53]\n"
    "s_waitcnt lgkmcnt(1)\n"
    "v_pk_add_f32 v[56:57], v[56:57], v[60:61]\n"
    "s_waitcnt vmcnt(9)\n"
    "v_pk_add_f32 v[50:51], v[50:51], v[54:55]\n"
    "s_waitcnt lgkmcnt(0)\n"
    "v_pk_add_f32 v[58:59], v[58:59], v[62:63]\n"
    "v_pk_add_f32 v[48:49], v[48:49], v[56:57]\n"
    "ds_read_b128 a[176:179], v18 offset:4096\n"
    "v_pk_add_f32 v[50:51], v[50:51], v[58:59]\n"
    "v_fma_f32 v220, v196, v48, v220\n"
    "v_fma_f32 v221, v196, v49, v221\n"
    "ds_read_b128 a[180:183], v19 offset:4096\n"
    "v_fma_f32 v220, -v197, v49, v220\n"
    "v_fma_f32 v221, v197, v48, v221\n"
    "v_fma_f32 v222, v212, v50, v222\n"
    "ds_read_b128 a[184:187], v20 offset:4096\n"
    "v_fma_f32 v223, v212, v51, v223\n"
    "v_fma_f32 v222, -v213, v51, v222\n"
    "v_fma_f32 v223, v213, v50, v223\n"
    "ds_read_b128 a[188:191], v21 offset:4096\n"
    "v_fma_f32 v152, v184, v220, v152\n"
    "v_fma_f32 v160, v184, v221, v160\n"
    "v_fma_f32 v152, -v185, v221, v152\n"
    "v_fma_f32 v160, v185, v220, v160\n"
    "v_fma_f32 v168, v200, v222, v168\n"
    "v_fma_f32 v176, v200, v223, v176\n"
    "v_fma_f32 v168, -v201, v223, v168\n"
    "v_fma_f32 v176, v201, v222, v176\n"
    "v_fma_f32 v153, v184, v152, v153\n"
    "v_fma_f32 v161, v184, v160, v161\n"
    "v_fma_f32 v153, -v185, v160, v153\n"
    "v_fma_f32 v161, v185, v152, v161\n"
    "v_fma_f32 v169, v200, v168, v169\n"
    "v_fma_f32 v177, v200, v176, v177\n"
    "v_fma_f32 v169, -v201, v176, v169\n"
    "v_fma_f32 v177, v201, v168, v177\n"
    "v_fma_f32 v154, v184, v153, v154\n"
    "v_fma_f32 v162, v184, v161, v162\n"
    "v_fma_f32 v154, -v185, v161, v154\n"
    "v_fma_f32 v162, v185, v153, v162\n"
    "v_fma_f32 v170, v200, v169, v170\n"
    "v_fma_f32 v178, v200, v177, v178\n"
    "v_fma_f32 v170, -v201, v177, v170\n"
    "v_fma_f32 v178, v201, v169, v178\n"
    "v_fma_f32 v155, v184, v154, v155\n"
    "v_fma_f32 v163, v184, v162, v163\n"
    "v_fma_f32 v155, -v185, v162, v155\n"
    "v_fma_f32 v163, v185, v154, v163\n"
    "v_fma_f32 v171, v200, v170, v171\n"
    "v_fma_f32 v179, v200, v178, v179\n"
    "s_waitcnt vmcnt(5)\n"
    "v_fma_f32 v171, -v201, v178, v171\n"
    "v_fma_f32 v179, v201, v170, v179\n"
    "v_fma_f32 v156, v184, v155, v156\n"
    "ds_read_b128 a[192:195], v18 offset:8192\n"
    "v_fma_f32 v164, v184, v163, v164\n"
    "v_fma_f32 v156, -v185, v163, v156\n"
    "v_fma_f32 v164, v185, v155, v164\n"
    "ds_read_b128 a[196:199], v19 offset:8192\n"
    "v_fma_f32 v172, v200, v171, v172\n"
    "v_fma_f32 v180, v200, v179, v180\n"
    "v_fma_f32 v172, -v201, v179, v172\n"
    "ds_read_b128 a[200:203], v20 offset:8192\n"
    "v_fma_f32 v180, v201, v171, v180\n"
    "v_fma_f32 v157, v184, v156, v157\n"
    "v_fma_f32 v165, v184, v164, v165\n"
    "ds_read_b128 a[204:207], v21 offset:8192\n"
    "v_fma_f32 v157, -v185, v164, v157\n"
    "v_fma_f32 v165, v185, v156, v165\n"
    "v_fma_f32 v173, v200, v172, v173\n"
    "v_fma_f32 v181, v200, v180, v181\n"
    "v_fma_f32 v173, -v201, v180, v173\n"
    "v_fma_f32 v181, v201, v172, v181\n"
    "v_fma_f32 v158, v184, v157, v158\n"
    "v_fma_f32 v166, v184, v165, v166\n"
    "v_fma_f32 v158, -v185, v165, v158\n"
    "v_fma_f32 v166, v185, v157, v166\n"
    "v_fma_f32 v174, v200, v173, v174\n"
    "v_fma_f32 v182, v200, v181, v182\n"
    "v_fma_f32 v174, -v201, v181, v174\n"
    "v_fma_f32 v182, v201, v173, v182\n"
    "v_fma_f32 v159, v184, v158, v159\n"
    "v_fma_f32 v167, v184, v166, v167\n"
    "v_fma_f32 v159, -v185, v166, v159\n"
    "v_fma_f32 v167, v185, v158, v167\n"
    "v_fma_f32 v175, v200, v174, v175\n"
    "v_fma_f32 v183, v200, v182, v183\n"
    "v_fma_f32 v175, -v201, v182, v175\n"
    "v_fma_f32 v183, v201, v174, v183\n"
    "v_pk_mul_f32 v[32:33], v[152:153], v[198:199] op_sel_hi:[1,0]\n"
    "v_pk_mul_f32 v[40:41], v[160:161], v[198:199] op_sel_hi:[1,0] neg_lo:[0,1] neg_hi:[0,1]\n"
    "v_pk_mul_f32 v[34:35], v[154:155], v[198:199] op_sel_hi:[1,0]\n"
    "v_pk_mul_f32 v[42:43], v[162:163], v[198:199] op_sel_hi:[1,0] neg_lo:[0,1] neg_hi:[0,1]\n"
    "v_pk_mul_f32 v[36:37], v[156:157], v[198:199] op_sel_hi:[1,0]\n"
    "v_pk_mul_f32 v[44:45], v[164:165], v[198:199] op_sel_hi:[1,0] neg_lo:[0,1] neg_hi:[0,1]\n"
    "v_pk_mul_f32 v[38:39], v[158:159], v[198:199] op_sel_hi:[1,0]\n"
    "v_pk_mul_f32 v[46:47], v[166:167], v[198:199] op_sel_hi:[1,0] neg_lo:[0,1] neg_hi:[0,1]\n"
    "v_pk_fma_f32 v[32:33], v[160:161], v[198:199], v[32:33] op_sel:[0,1,0] op_sel_hi:[1,1,1] neg_lo:[0,1,0] neg_hi:[0,1,0]\n"
    "v_pk_fma_f32 v[40:41], v[152:153], v[198:199], v[40:41] op_sel:[0,1,0] op_sel_hi:[1,1,1] neg_lo:[0,1,0] neg_hi:[0,1,0]\n"
    "v_pk_fma_f32 v[34:35], v[162:163], v[198:199], v[34:35] op_sel:[0,1,0] op_sel_hi:[1,1,1] neg_lo:[0,1,0] neg_hi:[0,1,0]\n"
    "v_pk_fma_f32 v[42:43], v[154:155], v[198:199], v[42:43] op_sel:[0,1,0] op_sel_hi:[1,1,1] neg_lo:[0,1,0] neg_hi:[0,1,0]\n"
    "v_pk_fma_f32 v[36:37], v[164:165], v[198:199], v[36:37] op_sel:[0,1,0] op_sel_hi:[1,1,1] neg_lo:[0,1,0] neg_hi:[0,1,0]\n"
    "v_pk_fma_f32 v[44:45], v[156:157], v[198:199], v[44:45] op_sel:[0,1,0] op_sel_hi:[1,1,1] neg_lo:[0,1,0] neg_hi:[0,1,0]\n"
    "v_pk_fma_f32 v[38:39], v[166:167], v[198:199], v[38:39] op_sel:[0,1,0] op_sel_hi:[1,1,1] neg_lo:[0,1,0] neg_hi:[0,1,0]\n"
    "v_pk_fma_f32 v[46:47], v[158:159], v[198:199], v[46:47] op_sel:[0,1,0] op_sel_hi:[1,1,1] neg_lo:[0,1,0] neg_hi:[0,1,0]\n"
    "v_cvt_pk_bf16_f32 v48, v32, v33\n"
    "v_cvt_pk_bf16_f32 v49, v34, v35\n"
    "v_cvt_pk_bf16_f32 v50, v36, v37\n"
    "v_cvt_pk_bf16_f32 v51, v38, v39\n"
    "ds_write_b128 v112, v[48:51]\n"
    "ds_read_b64_tr_b16 v[232:233], v113 offset:0\n"
    "ds_read_b64_tr_b16 v[234:235], v113 offset:256\n"
    "v_cvt_pk_bf16_f32 v52, v40, v41\n"
    "v_cvt_pk_bf16_f32 v53, v42, v43\n"
    "v_cvt_pk_bf16_f32 v54, v44, v45\n"
    "v_cvt_pk_bf16_f32 v55, v46, v47\n"
    "ds_write_b128 v112, v[52:55]\n"
    "ds_read_b64_tr_b16 v[236:237], v113 offset:0\n"
    "ds_read_b64_tr_b16 v[238:239], v113 offset:256\n"
    "v_pk_mul_f32 v[32:33], v[168:169], v[214:215] op_sel_hi:[1,0]\n"
    "v_pk_mul_f32 v[40:41], v[176:177], v[214:215] op_sel_hi:[1,0] neg_lo:[0,1] neg_hi:[0,1]\n"
    "v_pk_mul_f32 v[34:35], v[170:171], v[214:215] op_sel_hi:[1,0]\n"
    "v_pk_mul_f32 v[42:43], v[178:179], v[214:215] op_sel_hi:[1,0] neg_lo:[0,1] neg_hi:[0,1]\n"
    "v_pk_mul_f32 v[36:37], v[172:173], v[214:215] op_sel_hi:[1,0]\n"
    "v_pk_mul_f32 v[44:45], v[180:181], v[214:215] op_sel_hi:[1,0] neg_lo:[0,1] neg_hi:[0,1]\n"
    "v_pk_mul_f32 v[38:39], v[174:175], v[214:215] op_sel_hi:[1,0]\n"
    "v_pk_mul_f32 v[46:47], v[182:183], v[214:215] op_sel_hi:[1,0] neg_lo:[0,1] neg_hi:[0,1]\n"
    "v_pk_fma_f32 v[32:33], v[176:177], v[214:215], v[32:33] op_sel:[0,1,0] op_sel_hi:[1,1,1] neg_lo:[0,1,0] neg_hi:[0,1,0]\n"
    "v_pk_fma_f32 v[40:41], v[168:169], v[214:215], v[40:41] op_sel:[0,1,0] op_sel_hi:[1,1,1] neg_lo:[0,1,0] neg_hi:[0,1,0]\n"
    "v_pk_fma_f32 v[34:35], v[178:179], v[214:215], v[34:35] op_sel:[0,1,0] op_sel_hi:[1,1,1] neg_lo:[0,1,0] neg_hi:[0,1,0]\n"
    "v_pk_fma_f32 v[42:43], v[170:171], v[214:215], v[42:43] op_sel:[0,1,0] op_sel_hi:[1,1,1] neg_lo:[0,1,0] neg_hi:[0,1,0]\n"
    "v_pk_fma_f32 v[36:37], v[180:181], v[214:215], v[36:37] op_sel:[0,1,0] op_sel_hi:[1,1,1] neg_lo:[0,1,0] neg_hi:[0,1,0]\n"
    "v_pk_fma_f32 v[44:45], v[172:173], v[214:215], v[44:45] op_sel:[0,1,0] op_sel_hi:[1,1,1] neg_lo:[0,1,0] neg_hi:[0,1,0]\n"
    "v_pk_fma_f32 v[38:39], v[182:183], v[214:215], v[38:39] op_sel:[0,1,0] op_sel_hi:[1,1,1] neg_lo:[0,1,0] neg_hi:[0,1,0]\n"
    "v_pk_fma_f32 v[46:47], v[174:175], v[214:215], v[46:47] op_sel:[0,1,0] op_sel_hi:[1,1,1] neg_lo:[0,1,0] neg_hi:[0,1,0]\n"
    "v_cvt_pk_bf16_f32 v48, v32, v33\n"
    "v_cvt_pk_bf16_f32 v49, v34, v35\n"
    "v_cvt_pk_bf16_f32 v50, v36, v37\n"
    "v_cvt_pk_bf16_f32 v51, v38, v39\n"
    "ds_write_b128 v112, v[48:51]\n"
    "s_waitcnt lgkmcnt(11)\n"
    "ds_read_b64_tr_b16 v[240:241], v113 offset:0\n"
    "ds_read_b64_tr_b16 v[242:243], v113 offset:256\n"
    "v_cvt_pk_bf16_f32 v52, v40, v41\n"
    "v_cvt_pk_bf16_f32 v53, v42, v43\n"
    "v_cvt_pk_bf16_f32 v54, v44, v45\n"
    "s_waitcnt vmcnt(0)\n"
    "v_cvt_pk_bf16_f32 v55, v46, v47\n"
    "ds_write_b128 v112, v[52:55]\n"
    "ds_read_b64_tr_b16 v[244:245], v113 offset:0\n"
    "s_waitcnt lgkmcnt(11)\n"
    "ds_read_b64_tr_b16 v[246:247], v113 offset:256\n"
    "s_waitcnt lgkmcnt(9)\n"
    "buffer_store_dwordx4 v[232:235], v22, s[56:59], s97 offen offset:0 sc1\n"
    "s_waitcnt lgkmcnt(6)\n"
    "buffer_store_dwordx4 v[236:239], v22, s[56:59], s93 offen offset:0 sc1\n"
    "s_waitcnt lgkmcnt(3)\n"
    "buffer_store_dwordx4 v[240:243], v22, s[56:59], s97 offen offset:1024 sc1\n"
    "s_waitcnt lgkmcnt(0)\n"
    "buffer_store_dwordx4 v[244:247], v22, s[56:59], s93 offen offset:1024 sc1\n"
    "v_mfma_f32_16x16x32_bf16 v[120:123], v[80:83], a[40:43], v[120:123]\n"
    "ds_read_b64_tr_b16 v[72:73], v117 offset:24576\n"
    "ds_read_b128 a[208:211], v18 offset:12288\n"
    "v_mfma_f32_16x16x32_bf16 v[124:127], v[80:83], a[44:47], v[124:127]\n"
    "ds_read_b64_tr_b16 v[74:75], v117 offset:25600\n"
    "ds_read_b128 a[212:215], v19 offset:12288\n"
    "v_mfma_f32_16x16x32_bf16 v[128:131], v[84:87], a[40:43], v[128:131]\n"
    "ds_read_b64_tr_b16 v[76:77], v118 offset:24576\n"
    "ds_read_b128 a[216:219], v20 offset:12288\n"
    "v_mfma_f32_16x16x32_bf16 v[132:135], v[84:87], a[44:47], v[132:135]\n"
    "ds_read_b64_tr_b16 v[78:79], v118 offset:25600\n"
    "ds_read_b128 a[220:223], v21 offset:12288\n"
    "v_mfma_f32_16x16x32_bf16 v[136:139], v[88:91], a[40:43], v[136:139]\n"
    "ds_read_b64_tr_b16 v[80:81], v115 offset:28672\n"
    "v_mfma_f32_16x16x32_bf16 v[140:143], v[88:91], a[44:47], v[140:143]\n"
    "ds_read_b64_tr_b16 v[82:83], v115 offset:29696\n"
    "v_mfma_f32_16x16x32_bf16 v[144:147], v[92:95], a[40:43], v[144:147]\n"
    "ds_read_b64_tr_b16 v[84:85], v116 offset:28672\n"
    "v_mfma_f32_16x16x32_bf16 v[148:151], v[92:95], a[44:47], v[148:151]\n"
    "ds_read_b64_tr_b16 v[86:87], v116 offset:29696\n"
    "v_mfma_f32_16x16x32_bf16 v[120:123], v[64:67], a[48:51], v[120:123]\n"
    "ds_read_b64_tr_b16 v[88:89], v117 offset:28672\n"
    "v_mfma_f32_16x16x32_bf16 v[124:127], v[64:67], a[52:55], v[124:127]\n"
    "ds_read_b64_tr_b16 v[90:91], v117 offset:29696\n"
    "v_mfma_f32_16x16x32_bf16 v[128:131], v[68:71], a[48:51], v[128:131]\n"
    "ds_read_b64_tr_b16 v[92:93], v118 offset:28672\n"
    "v_mfma_f32_16x16x32_bf16 v[132:135], v[68:71], a[52:55], v[132:135]\n"
    "s_waitcnt lgkmcnt(11)\n"
    "ds_read_b64_tr_b16 v[94:95], v118 offset:29696\n"
    "s_waitcnt lgkmcnt(9)\n"
    "v_mfma_f32_16x16x32_bf16 v[136:139], v[72:75], a[48:51], v[136:139]\n"
    "s_barrier\n"
    "v_mfma_f32_16x16x32_bf16 v[140:143], v[72:75], a[52:55], v[140:143]\n"
    "ds_read_b64_tr_b16 v[64:65], v115 offset:32768\n"
    "ds_read_b64_tr_b16 v[66:67], v115 offset:33792\n"
    "v_mfma_f32_16x16x32_bf16 v[144:147], v[76:79], a[48:51], v[144:147]\n"
    "ds_read_b64_tr_b16 v[68:69], v116 offset:32768\n"
    "v_mfma_f32_16x16x32_bf16 v[148:151], v[76:79], a[52:55], v[148:151]\n"
    "ds_read_b64_tr_b16 v[70:71], v116 offset:33792\n"
    "s_waitcnt lgkmcnt(8)\n"
    "v_mfma_f32_16x16x32_bf16 v[120:123], v[80:83], a[56:59], v[120:123]\n"
    "ds_read_b64_tr_b16 v[72:73], v117 offset:32768\n"
    "v_mfma_f32_16x16x32_bf16 v[124:127], v[80:83], a[60:63], v[124:127]\n"
    "ds_read_b64_tr_b16 v[74:75], v117 offset:33792\n"
    "v_mfma_f32_16x16x32_bf16 v[128:131], v[84:87], a[56:59], v[128:131]\n"
    "ds_read_b64_tr_b16 v[76:77], v118 offset:32768\n"
    "v_mfma_f32_16x16x32_bf16 v[132:135], v[84:87], a[60:63], v[132:135]\n"
    "ds_read_b64_tr_b16 v[78:79], v118 offset:33792\n"
    "s_waitcnt lgkmcnt(8)\n"
    "v_mfma_f32_16x16x32_bf16 v[136:139], v[88:91], a[56:59], v[136:139]\n"
    "ds_read_b64_tr_b16 v[80:81], v115 offset:36864\n"
    "v_mfma_f32_16x16x32_bf16 v[140:143], v[88:91], a[60:63], v[140:143]\n"
    "ds_read_b64_tr_b16 v[82:83], v115 offset:37888\n"
    "v_mfma_f32_16x16x32_bf16 v[144:147], v[92:95], a[56:59], v[144:147]\n"
    "ds_read_b64_tr_b16 v[84:85], v116 offset:36864\n"
    "v_mfma_f32_16x16x32_bf16 v[148:151], v[92:95], a[60:63], v[148:151]\n"
    "ds_read_b64_tr_b16 v[86:87], v116 offset:37888\n"
    "s_waitcnt lgkmcnt(8)\n"
    "v_mfma_f32_16x16x32_bf16 v[120:123], v[64:67], a[64:67], v[120:123]\n"
    "ds_read_b64_tr_b16 v[88:89], v117 offset:36864\n"
    "v_mfma_f32_16x16x32_bf16 v[124:127], v[64:67], a[68:71], v[124:127]\n"
    "ds_read_b64_tr_b16 v[90:91], v117 offset:37888\n"
    "v_mfma_f32_16x16x32_bf16 v[128:131], v[68:71], a[64:67], v[128:131]\n"
    "ds_read_b64_tr_b16 v[92:93], v118 offset:36864\n"
    "v_mfma_f32_16x16x32_bf16 v[132:135], v[68:71], a[68:71], v[132:135]\n"
    "ds_read_b64_tr_b16 v[94:95], v118 offset:37888\n"
    "s_waitcnt lgkmcnt(8)\n"
    "v_mfma_f32_16x16x32_bf16 v[136:139], v[72:75], a[64:67], v[136:139]\n"
    "s_barrier\n"
    "v_mfma_f32_16x16x32_bf16 v[140:143], v[72:75], a[68:71], v[140:143]\n"
    "ds_read_b64_tr_b16 v[64:65], v115 offset:40960\n"
    "ds_read_b64_tr_b16 v[66:67], v115 offset:41984\n"
    "v_mfma_f32_16x16x32_bf16 v[144:147], v[76:79], a[64:67], v[144:147]\n"
    "ds_read_b64_tr_b16 v[68:69], v116 offset:40960\n"
    "v_mfma_f32_16x16x32_bf16 v[148:151], v[76:79], a[68:71], v[148:151]\n"
    "ds_read_b64_tr_b16 v[70:71], v116 offset:41984\n"
    "s_waitcnt lgkmcnt(8)\n"
    "v_mfma_f32_16x16x32_bf16 v[120:123], v[80:83], a[72:75], v[120:123]\n"
    "ds_read_b64_tr_b16 v[72:73], v117 offset:40960\n"
    "v_mfma_f32_16x16x32_bf16 v[124:127], v[80:83], a[76:79], v[124:127]\n"
    "ds_read_b64_tr_b16 v[74:75], v117 offset:41984\n"
    "v_mfma_f32_16x16x32_bf16 v[128:131], v[84:87], a[72:75], v[128:131]\n"
    "ds_read_b64_tr_b16 v[76:77], v118 offset:40960\n"
    "v_mfma_f32_16x16x32_bf16 v[132:135], v[84:87], a[76:79], v[132:135]\n"
    "ds_read_b64_tr_b16 v[78:79], v118 offset:41984\n"
    "s_waitcnt lgkmcnt(8)\n"
    "v_mfma_f32_16x16x32_bf16 v[136:139], v[88:91], a[72:75], v[136:139]\n"
    "ds_read_b64_tr_b16 v[80:81], v115 offset:45056\n"
    "v_mfma_f32_16x16x32_bf16 v[140:143], v[88:91], a[76:79], v[140:143]\n"
    "ds_read_b64_tr_b16 v[82:83], v115 offset:46080\n"
    "v_mfma_f32_16x16x32_bf16 v[144:147], v[92:95], a[72:75], v[144:147]\n"
    "ds_read_b64_tr_b16 v[84:85], v116 offset:45056\n"
    "v_mfma_f32_16x16x32_bf16 v[148:151], v[92:95], a[76:79], v[148:151]\n"
    "ds_read_b64_tr_b16 v[86:87], v116 offset:46080\n"
    "s_waitcnt lgkmcnt(8)\n"
    "v_mfma_f32_16x16x32_bf16 v[120:123], v[64:67], a[80:83], v[120:123]\n"
    "ds_read_b64_tr_b16 v[88:89], v117 offset:45056\n"
    "v_mfma_f32_16x16x32_bf16 v[124:127], v[64:67], a[84:87], v[124:127]\n"
    "ds_read_b64_tr_b16 v[90:91], v117 offset:46080\n"
    "v_mfma_f32_16x16x32_bf16 v[128:131], v[68:71], a[80:83], v[128:131]\n"
    "ds_read_b64_tr_b16 v[92:93], v118 offset:45056\n"
    "v_mfma_f32_16x16x32_bf16 v[132:135], v[68:71], a[84:87], v[132:135]\n"
    "ds_read_b64_tr_b16 v[94:95], v118 offset:46080\n"
    "s_waitcnt lgkmcnt(8)\n"
    "v_mfma_f32_16x16x32_bf16 v[136:139], v[72:75], a[80:83], v[136:139]\n"
    "s_barrier\n"
    "v_mfma_f32_16x16x32_bf16 v[140:143], v[72:75], a[84:87], v[140:143]\n"
    "ds_read_b64_tr_b16 v[64:65], v115 offset:49152\n"
    "ds_read_b64_tr_b16 v[66:67], v115 offset:50176\n"
    "v_mfma_f32_16x16x32_bf16 v[144:147], v[76:79], a[80:83], v[144:147]\n"
    "ds_read_b64_tr_b16 v[68:69], v116 offset:49152\n"
    "v_mfma_f32_16x16x32_bf16 v[148:151], v[76:79], a[84:87], v[148:151]\n"
    "ds_read_b64_tr_b16 v[70:71], v116 offset:50176\n"
    "s_waitcnt lgkmcnt(8)\n"
    "v_mfma_f32_16x16x32_bf16 v[120:123], v[80:83], a[88:91], v[120:123]\n"
    "ds_read_b64_tr_b16 v[72:73], v117 offset:49152\n"
    "v_mfma_f32_16x16x32_bf16 v[124:127], v[80:83], a[92:95], v[124:127]\n"
    "ds_read_b64_tr_b16 v[74:75], v117 offset:50176\n"
    "v_mfma_f32_16x16x32_bf16 v[128:131], v[84:87], a[88:91], v[128:131]\n"
    "ds_read_b64_tr_b16 v[76:77], v118 offset:49152\n"
    "v_mfma_f32_16x16x32_bf16 v[132:135], v[84:87], a[92:95], v[132:135]\n"
    "ds_read_b64_tr_b16 v[78:79], v118 offset:50176\n"
    "s_waitcnt lgkmcnt(8)\n"
    "v_mfma_f32_16x16x32_bf16 v[136:139], v[88:91], a[88:91], v[136:139]\n"
    "ds_read_b64_tr_b16 v[80:81], v115 offset:53248\n"
    "v_mfma_f32_16x16x32_bf16 v[140:143], v[88:91], a[92:95], v[140:143]\n"
    "ds_read_b64_tr_b16 v[82:83], v115 offset:54272\n"
    "v_mfma_f32_16x16x32_bf16 v[144:147], v[92:95], a[88:91], v[144:147]\n"
    "ds_read_b64_tr_b16 v[84:85], v116 offset:53248\n"
    "v_mfma_f32_16x16x32_bf16 v[148:151], v[92:95], a[92:95], v[148:151]\n"
    "ds_read_b64_tr_b16 v[86:87], v116 offset:54272\n"
    "s_waitcnt lgkmcnt(8)\n"
    "v_mfma_f32_16x16x32_bf16 v[120:123], v[64:67], a[96:99], v[120:123]\n"
    "ds_read_b64_tr_b16 v[88:89], v117 offset:53248\n"
    "v_mfma_f32_16x16x32_bf16 v[124:127], v[64:67], a[100:103], v[124:127]\n"
    "ds_read_b64_tr_b16 v[90:91], v117 offset:54272\n"
    "v_mfma_f32_16x16x32_bf16 v[128:131], v[68:71], a[96:99], v[128:131]\n"
    "ds_read_b64_tr_b16 v[92:93], v118 offset:53248\n"
    "v_mfma_f32_16x16x32_bf16 v[132:135], v[68:71], a[100:103], v[132:135]\n"
    "ds_read_b64_tr_b16 v[94:95], v118 offset:54272\n"
    "s_waitcnt lgkmcnt(8)\n"
    "v_mfma_f32_16x16x32_bf16 v[136:139], v[72:75], a[96:99], v[136:139]\n"
    "s_waitcnt vmcnt(0)\n"
    "s_barrier\n"
    "s_add_u32 s80, s14, 0x9000\n"
    "buffer_store_dwordx2 v[24:25], v23, s[56:59], s80 offen sc1\n"
    "v_mfma_f32_16x16x32_bf16 v[140:143], v[72:75], a[100:103], v[140:143]\n"
    "ds_read_b64_tr_b16 v[64:65], v115 offset:57344\n"
    "ds_read_b64_tr_b16 v[66:67], v115 offset:58368\n"
    "v_mfma_f32_16x16x32_bf16 v[144:147], v[76:79], a[96:99], v[144:147]\n"
    "ds_read_b64_tr_b16 v[68:69], v116 offset:57344\n"
    "v_mfma_f32_16x16x32_bf16 v[148:151], v[76:79], a[100:103], v[148:151]\n"
    "ds_read_b64_tr_b16 v[70:71], v116 offset:58368\n"
    "buffer_load_dwordx2 v[2:3], v254, s[56:59], 0 offen sc1\n"
    "s_waitcnt lgkmcnt(8)\n"
    "v_mfma_f32_16x16x32_bf16 v[120:123], v[80:83], a[104:107], v[120:123]\n"
    "ds_read_b64_tr_b16 v[72:73], v117 offset:57344\n"
    "v_mfma_f32_16x16x32_bf16 v[124:127], v[80:83], a[108:111], v[124:127]\n"
    "ds_read_b64_tr_b16 v[74:75], v117 offset:58368\n"
    "v_mfma_f32_16x16x32_bf16 v[128:131], v[84:87], a[104:107], v[128:131]\n"
    "ds_read_b64_tr_b16 v[76:77], v118 offset:57344\n"
    "v_mfma_f32_16x16x32_bf16 v[132:135], v[84:87], a[108:111], v[132:135]\n"
    "ds_read_b64_tr_b16 v[78:79], v118 offset:58368\n"
    "s_waitcnt lgkmcnt(8)\n"
    "v_mfma_f32_16x16x32_bf16 v[136:139], v[88:91], a[104:107], v[136:139]\n"
    "ds_read_b64_tr_b16 v[80:81], v115 offset:61440\n"
    "v_mfma_f32_16x16x32_bf16 v[140:143], v[88:91], a[108:111], v[140:143]\n"
    "ds_read_b64_tr_b16 v[82:83], v115 offset:62464\n"
    "v_mfma_f32_16x16x32_bf16 v[144:147], v[92:95], a[104:107], v[144:147]\n"
    "ds_read_b64_tr_b16 v[84:85], v116 offset:61440\n"
    "v_mfma_f32_16x16x32_bf16 v[148:151], v[92:95], a[108:111], v[148:151]\n"
    "ds_read_b64_tr_b16 v[86:87], v116 offset:62464\n"
    "s_waitcnt lgkmcnt(8)\n"
    "v_mfma_f32_16x16x32_bf16 v[120:123], v[64:67], a[112:115], v[120:123]\n"
    "ds_read_b64_tr_b16 v[88:89], v117 offset:61440\n"
    "v_mfma_f32_16x16x32_bf16 v[124:127], v[64:67], a[116:119], v[124:127]\n"
    "ds_read_b64_tr_b16 v[90:91], v117 offset:62464\n"
    "v_mfma_f32_16x16x32_bf16 v[128:131], v[68:71], a[112:115], v[128:131]\n"
    "ds_read_b64_tr_b16 v[92:93], v118 offset:61440\n"
    "v_mfma_f32_16x16x32_bf16 v[132:135], v[68:71], a[116:119], v[132:135]\n"
    "ds_read_b64_tr_b16 v[94:95], v118 offset:62464\n"
    "s_waitcnt lgkmcnt(8)\n"
    "v_mfma_f32_16x16x32_bf16 v[136:139], v[72:75], a[112:115], v[136:139]\n"
    "v_mfma_f32_16x16x32_bf16 v[140:143], v[72:75], a[116:119], v[140:143]\n"
    "v_mfma_f32_16x16x32_bf16 v[144:147], v[76:79], a[112:115], v[144:147]\n"
    "v_mfma_f32_16x16x32_bf16 v[148:151], v[76:79], a[116:119], v[148:151]\n"
    "s_waitcnt lgkmcnt(4)\n"
    "v_mfma_f32_16x16x32_bf16 v[120:123], v[80:83], a[120:123], v[120:123]\n"
    "v_mfma_f32_16x16x32_bf16 v[124:127], v[80:83], a[124:127], v[124:127]\n"
    "v_mfma_f32_16x16x32_bf16 v[128:131], v[84:87], a[120:123], v[128:131]\n"
    "v_mfma_f32_16x16x32_bf16 v[132:135], v[84:87], a[124:127], v[132:135]\n"
    "s_waitcnt lgkmcnt(0)\n"
    "v_mfma_f32_16x16x32_bf16 v[136:139], v[88:91], a[120:123], v[136:139]\n"
    "v_mfma_f32_16x16x32_bf16 v[140:143], v[88:91], a[124:127], v[140:143]\n"
    "v_mfma_f32_16x16x32_bf16 v[144:147], v[92:95], a[120:123], v[144:147]\n"
    "v_mfma_f32_16x16x32_bf16 v[148:151], v[92:95], a[124:127], v[148:151]\n"
    "s_barrier\n"
    "s_waitcnt vmcnt(0)\n"
    "v_xor_b32 v0, s64, v2\n"
    "v_bitop3_b32 v0, v0, v3, s64 bitop3:0xf6\n"
    "v_cmp_eq_u32 vcc, 0, v0\n"
    "s_nop 0\n"
    "s_cmp_eq_u64 vcc, -1\n"
    "s_cbranch_scc1 L0ok5_%=\n"
    "s_mov_b32 s80, 0x4000\n"
    "buffer_load_dwordx2 v[26:27], v254, s[56:59], 0 offen sc1\n"
    "L0spin6_%=:\n"
    "s_sleep 4\n"
    "buffer_load_dwordx2 v[2:3], v254, s[56:59], 0 offen sc1\n"
    "s_waitcnt vmcnt(1)\n"
    "v_xor_b32 v0, s64, v26\n"
    "v_bitop3_b32 v0, v0, v27, s64 bitop3:0xf6\n"
    "v_cmp_eq_u32 vcc, 0, v0\n"
    "s_nop 0\n"
    "s_cmp_eq_u64 vcc, -1\n"
    "s_cbranch_scc1 L0ok7_%=\n"
    "s_sleep 4\n"
    "buffer_load_dwordx2 v[26:27], v254, s[56:59], 0 offen sc1\n"
    "s_waitcnt vmcnt(1)\n"
    "v_xor_b32 v0, s64, v2\n"
    "v_bitop3_b32 v0, v0, v3, s64 bitop3:0xf6\n"
    "v_cmp_eq_u32 vcc, 0, v0\n"
    "s_nop 0\n"
    "s_cmp_eq_u64 vcc, -1\n"
    "s_cbranch_scc1 L0ok7_%=\n"
    "s_sub_u32 s80, s80, 1\n"
    "s_cmp_lg_u32 s80, 0\n"
    "s_cbranch_scc1 L0spin6_%=\n"
    "L0ok7_%=:\n"
    "L0ok5_%=:\n"
    "s_add_u32 m0, s81, 0\n"
    "s_bfe_u32 s77, s24, 0x40000\n"
    "s_mul_i32 s76, s77, 90112\n"
    "buffer_load_dwordx4 v248, s[56:59], s76 offen offset:0 sc1 lds\n"
    "buffer_load_dwordx4 v249, s[56:59], s76 offen offset:1024 sc1 lds\n"
    "s_add_u32 m0, s81, 8192\n"
    "s_bfe_u32 s77, s24, 0x40004\n"
    "s_mul_i32 s76, s77, 90112\n"
    "buffer_load_dwordx4 v248, s[56:59], s76 offen offset:0 sc1 lds\n"
    "buffer_load_dwordx4 v249, s[56:59], s76 offen offset:1024 sc1 lds\n"
    "s_add_u32 m0, s81, 16384\n"
    "s_bfe_u32 s77, s24, 0x40008\n"
    "s_mul_i32 s76, s77, 90112\n"
    "buffer_load_dwordx4 v248, s[56:59], s76 offen offset:0 sc1 lds\n"
    "buffer_load_dwordx4 v249, s[56:59], s76 offen offset:1024 sc1 lds\n"
    "s_add_u32 m0, s81, 24576\n"
    "s_bfe_u32 s77, s24, 0x4000c\n"
    "s_mul_i32 s76, s77, 90112\n"
    "buffer_load_dwordx4 v248, s[56:59], s76 offen offset:0 sc1 lds\n"
    "buffer_load_dwordx4 v249, s[56:59], s76 offen offset:1024 sc1 lds\n"
    "s_add_u32 m0, s81, 32768\n"
    "s_bfe_u32 s77, s24, 0x40010\n"
    "s_mul_i32 s76, s77, 90112\n"
    "buffer_load_dwordx4 v248, s[56:59], s76 offen offset:0 sc1 lds\n"
    "buffer_load_dwordx4 v249, s[56:59], s76 offen offset:1024 sc1 lds\n"
    "s_add_u32 m0, s81, 40960\n"
    "s_bfe_u32 s77, s24, 0x40014\n"
    "s_mul_i32 s76, s77, 90112\n"
    "buffer_load_dwordx4 v248, s[56:59], s76 offen offset:0 sc1 lds\n"
    "buffer_load_dwordx4 v249, s[56:59], s76 offen offset:1024 sc1 lds\n"
    "v_pk_mul_f32 v[220:221], v[120:121], v[120:121]\n"
    "v_pk_mul_f32 v[222:223], v[122:123], v[122:123]\n"
    "v_pk_mul_f32 v[224:225], v[124:125], v[124:125]\n"
    "v_pk_mul_f32 v[226:227], v[126:127], v[126:127]\n"
    "v_pk_fma_f32 v[220:221], v[220:221], s[18:19], s[18:19] op_sel:[0,0,1] op_sel_hi:[1,0,1]\n"
    "v_pk_fma_f32 v[222:223], v[222:223], s[18:19], s[18:19] op_sel:[0,0,1] op_sel_hi:[1,0,1]\n"
    "v_pk_fma_f32 v[224:225], v[224:225], s[18:19], s[18:19] op_sel:[0,0,1] op_sel_hi:[1,0,1]\n"
    "v_pk_fma_f32 v[226:227], v[226:227], s[18:19], s[18:19] op_sel:[0,0,1] op_sel_hi:[1,0,1]\n"
    "v_pk_mul_f32 v[220:221], v[220:221], v[120:121]\n"
    "v_pk_mul_f32 v[222:223], v[222:223], v[122:123]\n"
    "v_pk_mul_f32 v[224:225], v[224:225], v[124:125]\n"
    "v_pk_mul_f32 v[226:227], v[226:227], v[126:127]\n"
    "v_exp_f32 v220, v220\n"
    "v_exp_f32 v221, v221\n"
    "v_exp_f32 v222, v222\n"
    "v_exp_f32 v223, v223\n"
    "v_exp_f32 v224, v224\n"
    "v_exp_f32 v225, v225\n"
    "v_exp_f32 v226, v226\n"
    "v_exp_f32 v227, v227\n"
    "v_pk_add_f32 v[220:221], v[220:221], 1.0 op_sel_hi:[1,0]\n"
    "v_pk_add_f32 v[222:223], v[222:223], 1.0 op_sel_hi:[1,0]\n"
    "v_pk_add_f32 v[224:225], v[224:225], 1.0 op_sel_hi:[1,0]\n"
    "v_pk_add_f32 v[226:227], v[226:227], 1.0 op_sel_hi:[1,0]\n"
    "v_rcp_f32 v220, v220\n"
    "v_rcp_f32 v221, v221\n"
    "v_rcp_f32 v222, v222\n"
    "v_rcp_f32 v223, v223\n"
    "v_rcp_f32 v224, v224\n"
    "v_rcp_f32 v225, v225\n"
    "v_rcp_f32 v226, v226\n"
    "v_rcp_f32 v227, v227\n"
    "v_pk_mul_f32 v[120:121], v[220:221], v[120:121]\n"
    "v_pk_mul_f32 v[122:123], v[222:223], v[122:123]\n"
    "v_pk_mul_f32 v[124:125], v[224:225], v[124:125]\n"
    "v_pk_mul_f32 v[126:127], v[226:227], v[126:127]\n"
    "v_pk_mul_f32 v[220:221], v[128:129], v[128:129]\n"
    "v_pk_mul_f32 v[222:223], v[130:131], v[130:131]\n"
    "v_pk_mul_f32 v[224:225], v[132:133], v[132:133]\n"
    "v_pk_mul_f32 v[226:227], v[134:135], v[134:135]\n"
    "v_pk_fma_f32 v[220:221], v[220:221], s[18:19], s[18:19] op_sel:[0,0,1] op_sel_hi:[1,0,1]\n"
    "v_pk_fma_f32 v[222:223], v[222:223], s[18:19], s[18:19] op_sel:[0,0,1] op_sel_hi:[1,0,1]\n"
    "v_pk_fma_f32 v[224:225], v[224:225], s[18:19], s[18:19] op_sel:[0,0,1] op_sel_hi:[1,0,1]\n"
    "v_pk_fma_f32 v[226:227], v[226:227], s[18:19], s[18:19] op_sel:[0,0,1] op_sel_hi:[1,0,1]\n"
    "v_pk_mul_f32 v[220:221], v[220:221], v[128:129]\n"
    "v_pk_mul_f32 v[222:223], v[222:223], v[130:131]\n"
    "v_pk_mul_f32 v[224:225], v[224:225], v[132:133]\n"
    "v_pk_mul_f32 v[226:227], v[226:227], v[134:135]\n"
    "v_exp_f32 v220, v220\n"
    "v_exp_f32 v221, v221\n"
    "v_exp_f32 v222, v222\n"
    "v_exp_f32 v223, v223\n"
    "v_exp_f32 v224, v224\n"
    "v_exp_f32 v225, v225\n"
    "v_exp_f32 v226, v226\n"
    "v_exp_f32 v227, v227\n"
    "v_pk_add_f32 v[220:221], v[220:221], 1.0 op_sel_hi:[1,0]\n"
    "v_pk_add_f32 v[222:223], v[222:223], 1.0 op_sel_hi:[1,0]\n"
    "v_pk_add_f32 v[224:225], v[224:225], 1.0 op_sel_hi:[1,0]\n"
    "v_pk_add_f32 v[226:227], v[226:227], 1.0 op_sel_hi:[1,0]\n"
    "v_rcp_f32 v220, v220\n"
    "v_rcp_f32 v221, v221\n"
    "v_rcp_f32 v222, v222\n"
    "v_rcp_f32 v223, v223\n"
    "v_rcp_f32 v224, v224\n"
    "v_rcp_f32 v225, v225\n"
    "v_rcp_f32 v226, v226\n"
    "v_rcp_f32 v227, v227\n"
    "v_pk_mul_f32 v[128:129], v[220:221], v[128:129]\n"
    "v_pk_mul_f32 v[130:131], v[222:223], v[130:131]\n"
    "v_pk_mul_f32 v[132:133], v[224:225], v[132:133]\n"
    "v_pk_mul_f32 v[134:135], v[226:227], v[134:135]\n"
    "v_pk_mul_f32 v[220:221], v[136:137], v[136:137]\n"
    "v_pk_mul_f32 v[222:223], v[138:139], v[138:139]\n"
    "v_pk_mul_f32 v[224:225], v[140:141], v[140:141]\n"
    "v_pk_mul_f32 v[226:227], v[142:143], v[142:143]\n"
    "v_pk_fma_f32 v[220:221], v[220:221], s[18:19], s[18:19] op_sel:[0,0,1] op_sel_hi:[1,0,1]\n"
    "v_pk_fma_f32 v[222:223], v[222:223], s[18:19], s[18:19] op_sel:[0,0,1] op_sel_hi:[1,0,1]\n"
    "v_pk_fma_f32 v[224:225], v[224:225], s[18:19], s[18:19] op_sel:[0,0,1] op_sel_hi:[1,0,1]\n"
    "v_pk_fma_f32 v[226:227], v[226:227], s[18:19], s[18:19] op_sel:[0,0,1] op_sel_hi:[1,0,1]\n"
    "v_pk_mul_f32 v[220:221], v[220:221], v[136:137]\n"
    "v_pk_mul_f32 v[222:223], v[222:223], v[138:139]\n"
    "v_pk_mul_f32 v[224:225], v[224:225], v[140:141]\n"
    "v_pk_mul_f32 v[226:227], v[226:227], v[142:143]\n"
    "v_exp_f32 v220, v220\n"
    "v_exp_f32 v221, v221\n"
    "v_exp_f32 v222, v222\n"
    "v_exp_f32 v223, v223\n"
    "v_exp_f32 v224, v224\n"
    "v_exp_f32 v225, v225\n"
    "v_exp_f32 v226, v226\n"
    "v_exp_f32 v227, v227\n"
    "v_pk_add_f32 v[220:221], v[220:221], 1.0 op_sel_hi:[1,0]\n"
    "v_pk_add_f32 v[222:223], v[222:223], 1.0 op_sel_hi:[1,0]\n"
    "v_pk_add_f32 v[224:225], v[224:225], 1.0 op_sel_hi:[1,0]\n"
    "v_pk_add_f32 v[226:227], v[226:227], 1.0 op_sel_hi:[1,0]\n"
    "v_rcp_f32 v220, v220\n"
    "v_rcp_f32 v221, v221\n"
    "s_add_u32 m0, s67, 16384\n"
    "v_rcp_f32 v222, v222\n"
    "v_rcp_f32 v223, v223\n"
    "v_rcp_f32 v224, v224\n"
    "s_bfe_u32 s77, s24, 0x40018\n"
    "v_rcp_f32 v225, v225\n"
    "v_rcp_f32 v226, v226\n"
    "v_rcp_f32 v227, v227\n"
    "s_lshl_b32 s76, s77, 7\n"
    "v_pk_mul_f32 v[136:137], v[220:221], v[136:137]\n"
    "v_pk_mul_f32 v[138:139], v[222:223], v[138:139]\n"
    "v_pk_mul_f32 v[140:141], v[224:225], v[140:141]\n"
    "buffer_load_dwordx4 v5, s[52:55], s76 offen offset:0 lds\n"
    "v_pk_mul_f32 v[142:143], v[226:227], v[142:143]\n"
    "v_pk_mul_f32 v[220:221], v[144:145], v[144:145]\n"
    "v_pk_mul_f32 v[222:223], v[146:147], v[146:147]\n"
    "buffer_load_dwordx4 v6, s[52:55], s76 offen offset:1024 lds\n"
    "v_pk_mul_f32 v[224:225], v[148:149], v[148:149]\n"
    "v_pk_mul_f32 v[226:227], v[150:151], v[150:151]\n"
    "v_pk_fma_f32 v[220:221], v[220:221], s[18:19], s[18:19] op_sel:[0,0,1] op_sel_hi:[1,0,1]\n"
    "v_pk_fma_f32 v[222:223], v[222:223], s[18:19], s[18:19] op_sel:[0,0,1] op_sel_hi:[1,0,1]\n"
    "v_pk_fma_f32 v[224:225], v[224:225], s[18:19], s[18:19] op_sel:[0,0,1] op_sel_hi:[1,0,1]\n"
    "v_pk_fma_f32 v[226:227], v[226:227], s[18:19], s[18:19] op_sel:[0,0,1] op_sel_hi:[1,0,1]\n"
    "v_pk_mul_f32 v[220:221], v[220:221], v[144:145]\n"
    "v_pk_mul_f32 v[222:223], v[222:223], v[146:147]\n"
    "v_pk_mul_f32 v[224:225], v[224:225], v[148:149]\n"
    "v_pk_mul_f32 v[226:227], v[226:227], v[150:151]\n"
    "v_exp_f32 v220, v220\n"
    "v_exp_f32 v221, v221\n"
    "v_exp_f32 v222, v222\n"
    "v_exp_f32 v223, v223\n"
    "v_exp_f32 v224, v224\n"
    "buffer_load_dwordx4 v7, s[52:55], s76 offen offset:2048 lds\n"
    "v_exp_f32 v225, v225\n"
    "v_exp_f32 v226, v226\n"
    "v_exp_f32 v227, v227\n"
    "v_pk_add_f32 v[220:221], v[220:221], 1.0 op_sel_hi:[1,0]\n"
    "v_pk_add_f32 v[222:223], v[222:223], 1.0 op_sel_hi:[1,0]\n"
    "v_pk_add_f32 v[224:225], v[224:225], 1.0 op_sel_hi:[1,0]\n"
    "v_pk_add_f32 v[226:227], v[226:227], 1.0 op_sel_hi:[1,0]\n"
    "v_rcp_f32 v220, v220\n"
    "v_rcp_f32 v221, v221\n"
    "v_rcp_f32 v222, v222\n"
    "v_rcp_f32 v223, v223\n"
    "v_rcp_f32 v224, v224\n"
    "buffer_load_dwordx4 v8, s[52:55], s76 offen offset:3072 lds\n"
    "v_rcp_f32 v225, v225\n"
    "v_rcp_f32 v226, v226\n"
    "v_rcp_f32 v227, v227\n"
    "v_pk_mul_f32 v[144:145], v[220:221], v[144:145]\n"
    "v_pk_mul_f32 v[146:147], v[222:223], v[146:147]\n"
    "v_pk_mul_f32 v[148:149], v[224:225], v[148:149]\n"
    "v_pk_mul_f32 v[150:151], v[226:227], v[150:151]\n"
    "v_permlane16_swap_b32 v120, v124\n"
    "v_permlane16_swap_b32 v121, v125\n"
    "v_permlane16_swap_b32 v122, v126\n"
    "v_permlane16_swap_b32 v123, v127\n"
    "v_permlane16_swap_b32 v128, v132\n"
    "v_permlane16_swap_b32 v129, v133\n"
    "v_permlane16_swap_b32 v130, v134\n"
    "v_permlane16_swap_b32 v131, v135\n"
    "s_add_u32 m0, s67, 0\n"
    "v_permlane16_swap_b32 v136, v140\n"
    "v_permlane16_swap_b32 v137, v141\n"
    "v_permlane16_swap_b32 v138, v142\n"
    "s_bfe_u32 s77, s24, 0x4001c\n"
    "v_permlane16_swap_b32 v139, v143\n"
    "v_permlane16_swap_b32 v144, v148\n"
    "v_permlane16_swap_b32 v145, v149\n"
    "s_lshl_b32 s76, s77, 7\n"
    "v_permlane16_swap_b32 v146, v150\n"
    "v_permlane16_swap_b32 v147, v151\n"
    "buffer_load_dwordx4 v5, s[52:55], s76 offen offset:0 lds\n"
    "buffer_load_dwordx4 v6, s[52:55], s76 offen offset:1024 lds\n"
    "buffer_load_dwordx4 v7, s[52:55], s76 offen offset:2048 lds\n"
    "buffer_load_dwordx4 v8, s[52:55], s76 offen offset:3072 lds\n"
    "s_waitcnt vmcnt(16)\n"
    "s_barrier\n"
    "ds_read_b128 v[96:99], v252 offset:0\n"
    "ds_read_b128 v[100:103], v252 offset:1024\n"
    "ds_read_b128 v[104:107], v252 offset:2048\n"
    "ds_read_b128 v[108:111], v252 offset:3072\n"
    "ds_read_b128 v[112:115], v252 offset:4096\n"
    "s_waitcnt lgkmcnt(3)\n"
    "v_mfma_f32_32x32x16_bf16 v[32:47], v[96:99], a[128:131], 0\n"
    "ds_read_b128 v[116:119], v252 offset:5120\n"
    "s_add_u32 m0, s81, 49152\n"
    "s_bfe_u32 s77, s24, 0x40018\n"
    "s_mul_i32 s76, s77, 90112\n"
    "v_mov_b32 v228, 0xc0135761\n"
    "v_mfma_f32_32x32x16_bf16 v[48:63], v[100:103], a[128:131], 0\n"
    "ds_read_b128 v[96:99], v252 offset:6144\n"
    "buffer_load_dwordx4 v248, s[56:59], s76 offen offset:0 sc1 lds\n"
    "buffer_load_dwordx4 v249, s[56:59], s76 offen offset:1024 sc1 lds\n"
    "s_waitcnt lgkmcnt(3)\n"
    "v_mfma_f32_32x32x16_bf16 v[32:47], v[104:107], a[132:135], v[32:47]\n"
    "ds_read_b128 v[100:103], v252 offset:7168\n"
    "v_lshlrev_b32 v64, 16, v255\n"
    "v_mov_b32 v65, v64\n"
    "v_mov_b32 v66, v64\n"
    "v_mov_b32 v67, v64\n"
    "v_mfma_f32_32x32x16_bf16 v[48:63], v[108:111], a[132:135], v[48:63]\n"
    "ds_read_b128 v[104:107], v252 offset:8192\n"
    "s_add_u32 m0, s67, 4096\n"
    "s_bfe_u32 s77, s25, 0x40000\n"
    "s_lshl_b32 s76, s77, 7\n"
    "v_mov_b32 v68, v64\n"
    "s_waitcnt lgkmcnt(3)\n"
    "v_mfma_f32_32x32x16_bf16 v[32:47], v[112:115], a[136:139], v[32:47]\n"
    "ds_read_b128 v[108:111], v252 offset:9216\n"
    "buffer_load_dwordx4 v5, s[52:55], s76 offen offset:0 lds\n"
    "v_mov_b32 v69, v64\n"
    "v_mov_b32 v70, v64\n"
    "v_mfma_f32_32x32x16_bf16 v[48:63], v[116:119], a[136:139], v[48:63]\n"
    "ds_read_b128 v[112:115], v252 offset:10240\n"
    "v_mov_b32 v71, v64\n"
    "v_mov_b32 v72, v64\n"
    "v_mov_b32 v73, v64\n"
    "v_mov_b32 v74, v64\n"
    "s_waitcnt lgkmcnt(3)\n"
    "v_mfma_f32_32x32x16_bf16 v[32:47], v[96:99], a[140:143], v[32:47]\n"
    "ds_read_b128 v[116:119], v252 offset:11264\n"
    "buffer_load_dwordx4 v6, s[52:55], s76 offen offset:1024 lds\n"
    "v_mov_b32 v75, v64\n"
    "v_mov_b32 v76, v64\n"
    "v_mfma_f32_32x32x16_bf16 v[48:63], v[100:103], a[140:143], v[48:63]\n"
    "ds_read_b128 v[96:99], v252 offset:12288\n"
    "v_mov_b32 v77, v64\n"
    "v_mov_b32 v78, v64\n"
    "v_mov_b32 v79, v64\n"
    "v_mov_b32 v80, v64\n"
    "buffer_load_dwordx4 v7, s[52:55], s76 offen offset:2048 lds\n"
    "buffer_load_dwordx4 v8, s[52:55], s76 offen offset:3072 lds\n"
    "s_waitcnt lgkmcnt(3)\n"
    "v_mfma_f32_32x32x16_bf16 v[32:47], v[104:107], a[144:147], v[32:47]\n"
    "ds_read_b128 v[100:103], v252 offset:13312\n"
    "v_mov_b32 v81, v64\n"
    "v_mov_b32 v82, v64\n"
    "v_mov_b32 v83, v64\n"
    "v_mov_b32 v84, v64\n"
    "v_mfma_f32_32x32x16_bf16 v[48:63], v[108:111], a[144:147], v[48:63]\n"
    "ds_read_b128 v[104:107], v252 offset:14336\n"
    "v_mov_b32 v85, v64\n"
    "v_mov_b32 v86, v64\n"
    "v_mov_b32 v87, v64\n"
    "v_mov_b32 v88, v64\n"
    "s_waitcnt lgkmcnt(3)\n"
    "v_mfma_f32_32x32x16_bf16 v[32:47], v[112:115], a[148:151], v[32:47]\n"
    "ds_read_b128 v[108:111], v252 offset:15360\n"
    "v_mov_b32 v89, v64\n"
    "v_mov_b32 v90, v64\n"
    "v_mov_b32 v91, v64\n"
    "v_mov_b32 v92, v64\n"
    "v_mfma_f32_32x32x16_bf16 v[48:63], v[116:119], a[148:151], v[48:63]\n"
    "s_waitcnt vmcnt(18)\n"
    "s_barrier\n"
    "ds_read_b128 v[112:115], v252 offset:16384\n"
    "s_waitcnt lgkmcnt(3)\n"
    "v_mfma_f32_32x32x16_bf16 v[32:47], v[96:99], a[152:155], v[32:47]\n"
    "ds_read_b128 v[116:119], v252 offset:17408\n"
    "s_add_u32 m0, s81, 57344\n"
    "s_bfe_u32 s77, s24, 0x4001c\n"
    "s_mul_i32 s76, s77, 90112\n"
    "v_mov_b32 v93, v64\n"
    "v_mfma_f32_32x32x16_bf16 v[48:63], v[100:103], a[152:155], v[48:63]\n"
    "ds_read_b128 v[96:99], v252 offset:18432\n"
    "buffer_load_dwordx4 v248, s[56:59], s76 offen offset:0 sc1 lds\n"
    "v_mov_b32 v94, v64\n"
    "v_mov_b32 v95, v64\n"
    "s_waitcnt lgkmcnt(3)\n"
    "v_mfma_f32_32x32x16_bf16 v[32:47], v[104:107], a[156:159], v[32:47]\n"
    "ds_read_b128 v[100:103], v252 offset:19456\n"
    "buffer_load_dwordx4 v249, s[56:59], s76 offen offset:1024 sc1 lds\n"
    "s_waitcnt vmcnt(12)\n"
    "ds_read_b128 a[224:227], v18 offset:16384\n"
    "v_mfma_f32_32x32x16_bf16 v[48:63], v[108:111], a[156:159], v[48:63]\n"
    "ds_read_b128 v[104:107], v252 offset:20480\n"
    "ds_read_b128 a[228:231], v19 offset:16384\n"
    "ds_read_b128 a[232:235], v20 offset:16384\n"
    "ds_read_b128 a[236:239], v21 offset:16384\n"
    "s_waitcnt lgkmcnt(7)\n"
    "v_mfma_f32_32x32x16_bf16 v[32:47], v[112:115], a[160:163], v[32:47]\n"
    "ds_read_b128 v[108:111], v252 offset:21504\n"
    "v_mfma_f32_32x32x16_bf16 v[48:63], v[116:119], a[160:163], v[48:63]\n"
    "ds_read_b128 v[112:115], v252 offset:22528\n"
    "s_add_u32 m0, s67, 8192\n"
    "s_bfe_u32 s77, s25, 0x40004\n"
    "s_lshl_b32 s76, s77, 7\n"
    "s_waitcnt lgkmcnt(7)\n"
    "v_mfma_f32_32x32x16_bf16 v[32:47], v[96:99], a[164:167], v[32:47]\n"
    "ds_read_b128 v[116:119], v252 offset:23552\n"
    "buffer_load_dwordx4 v5, s[52:55], s76 offen offset:0 lds\n"
    "v_mfma_f32_32x32x16_bf16 v[48:63], v[100:103], a[164:167], v[48:63]\n"
    "ds_read_b128 v[96:99], v252 offset:24576\n"
    "s_waitcnt lgkmcnt(3)\n"
    "v_mfma_f32_32x32x16_bf16 v[32:47], v[104:107], a[168:171], v[32:47]\n"
    "ds_read_b128 v[100:103], v252 offset:25600\n"
    "v_mfma_f32_32x32x16_bf16 v[48:63], v[108:111], a[168:171], v[48:63]\n"
    "ds_read_b128 v[104:107], v252 offset:26624\n"
    "s_waitcnt lgkmcnt(3)\n"
    "v_mfma_f32_32x32x16_bf16 v[32:47], v[112:115], a[172:175], v[32:47]\n"
    "ds_read_b128 v[108:111], v252 offset:27648\n"
    "v_mfma_f32_32x32x16_bf16 v[48:63], v[116:119], a[172:175], v[48:63]\n"
    "ds_read_b128 v[112:115], v252 offset:28672\n"
    "buffer_load_dwordx4 v6, s[52:55], s76 offen offset:1024 lds\n"
    "buffer_load_dwordx4 v7, s[52:55], s76 offen offset:2048 lds\n"
    "buffer_load_dwordx4 v8, s[52:55], s76 offen offset:3072 lds\n"
    "s_waitcnt lgkmcnt(3)\n"
    "v_mfma_f32_32x32x16_bf16 v[32:47], v[96:99], a[176:179], v[32:47]\n"
    "ds_read_b128 v[116:119], v252 offset:29696\n"
    "s_waitcnt vmcnt(12)\n"
    "ds_read_b128 a[240:243], v18 offset:0\n"
    "ds_read_b128 a[244:247], v19 offset:0\n"
    "ds_read_b128 a[248:251], v20 offset:0\n"
    "v_mfma_f32_32x32x16_bf16 v[48:63], v[100:103], a[176:179], v[48:63]\n"
    "ds_read_b128 v[96:99], v252 offset:30720\n"
    "ds_read_b128 a[252:255], v21 offset:0\n"
    "s_waitcnt lgkmcnt(7)\n"
    "v_mfma_f32_32x32x16_bf16 v[32:47], v[104:107], a[180:183], v[32:47]\n"
    "ds_read_b128 v[100:103], v252 offset:31744\n"
    "v_mfma_f32_32x32x16_bf16 v[48:63], v[108:111], a[180:183], v[48:63]\n"
    "s_barrier\n"
    "ds_read_b128 v[104:107], v252 offset:32768\n"
    "s_waitcnt lgkmcnt(7)\n"
    "v_mfma_f32_32x32x16_bf16 v[32:47], v[112:115], a[184:187], v[32:47]\n"
    "ds_read_b128 v[108:111], v252 offset:33792\n"
    "v_mfma_f32_32x32x16_bf16 v[48:63], v[116:119], a[184:187], v[48:63]\n"
    "ds_read_b128 v[112:115], v252 offset:34816\n"
    "s_waitcnt lgkmcnt(3)\n"
    "v_mfma_f32_32x32x16_bf16 v[32:47], v[96:99], a[188:191], v[32:47]\n"
    "ds_read_b128 v[116:119], v252 offset:35840\n"
    "s_add_u32 m0, s81, 0\n"
    "s_bfe_u32 s77, s25, 0x40000\n"
    "s_mul_i32 s76, s77, 90112\n"
    "v_mfma_f32_32x32x16_bf16 v[48:63], v[100:103], a[188:191], v[48:63]\n"
    "ds_read_b128 v[96:99], v252 offset:36864\n"
    "buffer_load_dwordx4 v248, s[56:59], s76 offen offset:0 sc1 lds\n"
    "buffer_load_dwordx4 v249, s[56:59], s76 offen offset:1024 sc1 lds\n"
    "s_waitcnt lgkmcnt(3)\n"
    "v_mfma_f32_32x32x16_bf16 v[32:47], v[104:107], a[192:195], v[32:47]\n"
    "ds_read_b128 v[100:103], v252 offset:37888\n"
    "v_mfma_f32_32x32x16_bf16 v[48:63], v[108:111], a[192:195], v[48:63]\n"
    "ds_read_b128 v[104:107], v252 offset:38912\n"
    "s_waitcnt lgkmcnt(3)\n"
    "v_mfma_f32_32x32x16_bf16 v[32:47], v[112:115], a[196:199], v[32:47]\n"
    "ds_read_b128 v[108:111], v252 offset:39936\n"
    "v_mfma_f32_32x32x16_bf16 v[48:63], v[116:119], a[196:199], v[48:63]\n"
    "ds_read_b128 v[112:115], v252 offset:40960\n"
    "s_add_u32 m0, s67, 12288\n"
    "s_bfe_u32 s77, s25, 0x40008\n"
    "s_lshl_b32 s76, s77, 7\n"
    "s_waitcnt lgkmcnt(3)\n"
    "v_mfma_f32_32x32x16_bf16 v[32:47], v[96:99], a[200:203], v[32:47]\n"
    "ds_read_b128 v[116:119], v252 offset:41984\n"
    "buffer_load_dwordx4 v5, s[52:55], s76 offen offset:0 lds\n"
    "v_mfma_f32_32x32x16_bf16 v[48:63], v[100:103], a[200:203], v[48:63]\n"
    "ds_read_b128 v[96:99], v252 offset:43008\n"
    "s_waitcnt lgkmcnt(3)\n"
    "v_mfma_f32_32x32x16_bf16 v[32:47], v[104:107], a[204:207], v[32:47]\n"
    "ds_read_b128 v[100:103], v252 offset:44032\n"
    "buffer_load_dwordx4 v6, s[52:55], s76 offen offset:1024 lds\n"
    "v_mfma_f32_32x32x16_bf16 v[48:63], v[108:111], a[204:207], v[48:63]\n"
    "ds_read_b128 v[104:107], v252 offset:45056\n"
    "buffer_load_dwordx4 v7, s[52:55], s76 offen offset:2048 lds\n"
    "buffer_load_dwordx4 v8, s[52:55], s76 offen offset:3072 lds\n"
    "s_waitcnt lgkmcnt(3)\n"
    "v_mfma_f32_32x32x16_bf16 v[32:47], v[112:115], a[208:211], v[32:47]\n"
    "ds_read_b128 v[108:111], v252 offset:46080\n"
    "v_mfma_f32_32x32x16_bf16 v[48:63], v[116:119], a[208:211], v[48:63]\n"
    "ds_read_b128 v[112:115], v252 offset:47104\n"
    "s_waitcnt vmcnt(12)\n"
    "ds_read_b128 a[0:3], v18 offset:4096\n"
    "ds_read_b128 a[4:7], v19 offset:4096\n"
    "ds_read_b128 a[8:11], v20 offset:4096\n"
    "s_waitcnt lgkmcnt(6)\n"
    "v_mfma_f32_32x32x16_bf16 v[32:47], v[96:99], a[212:215], v[32:47]\n"
    "ds_read_b128 v[116:119], v252 offset:48128\n"
    "ds_read_b128 a[12:15], v21 offset:4096\n"
    "v_mfma_f32_32x32x16_bf16 v[48:63], v[100:103], a[212:215], v[48:63]\n"
    "s_waitcnt vmcnt(10)\n"
    "s_barrier\n"
    "ds_read_b128 v[96:99], v252 offset:49152\n"
    "s_waitcnt lgkmcnt(7)\n"
    "v_mfma_f32_32x32x16_bf16 v[32:47], v[104:107], a[216:219], v[32:47]\n"
    "ds_read_b128 v[100:103], v252 offset:50176\n"
    "v_mfma_f32_32x32x16_bf16 v[48:63], v[108:111], a[216:219], v[48:63]\n"
    "ds_read_b128 v[104:107], v252 offset:51200\n"
    "s_add_u32 m0, s81, 8192\n"
    "s_bfe_u32 s77, s25, 0x40004\n"
    "s_mul_i32 s76, s77, 90112\n"
    "s_waitcnt lgkmcnt(4)\n"
    "v_mfma_f32_32x32x16_bf16 v[32:47], v[112:115], a[220:223], v[32:47]\n"
    "ds_read_b128 v[108:111], v252 offset:52224\n"
    "buffer_load_dwordx4 v248, s[56:59], s76 offen offset:0 sc1 lds\n"
    "v_mfma_f32_32x32x16_bf16 v[48:63], v[116:119], a[220:223], v[48:63]\n"
    "ds_read_b128 v[112:115], v252 offset:53248\n"
    "buffer_load_dwordx4 v249, s[56:59], s76 offen offset:1024 sc1 lds\n"
    "s_waitcnt lgkmcnt(3)\n"
    "v_mfma_f32_32x32x16_bf16 v[32:47], v[96:99], a[224:227], v[32:47]\n"
    "ds_read_b128 v[116:119], v252 offset:54272\n"
    "v_mfma_f32_32x32x16_bf16 v[48:63], v[100:103], a[224:227], v[48:63]\n"
    "ds_read_b128 v[96:99], v252 offset:55296\n"
    "s_waitcnt lgkmcnt(3)\n"
    "v_mfma_f32_32x32x16_bf16 v[32:47], v[104:107], a[228:231], v[32:47]\n"
    "ds_read_b128 v[100:103], v252 offset:56320\n"
    "s_add_u32 m0, s67, 16384\n"
    "s_bfe_u32 s77, s25, 0x4000c\n"
    "s_lshl_b32 s76, s77, 7\n"
    "v_mfma_f32_32x32x16_bf16 v[48:63], v[108:111], a[228:231], v[48:63]\n"
    "ds_read_b128 v[104:107], v252 offset:57344\n"
    "buffer_load_dwordx4 v5, s[52:55], s76 offen offset:0 lds\n"
    "s_waitcnt lgkmcnt(3)\n"
    "v_mfma_f32_32x32x16_bf16 v[32:47], v[112:115], a[232:235], v[32:47]\n"
    "ds_read_b128 v[108:111], v252 offset:58368\n"
    "v_mfma_f32_32x32x16_bf16 v[48:63], v[116:119], a[232:235], v[48:63]\n"
    "ds_read_b128 v[112:115], v252 offset:59392\n"
    "s_waitcnt lgkmcnt(3)\n"
    "v_mfma_f32_32x32x16_bf16 v[32:47], v[96:99], a[236:239], v[32:47]\n"
    "ds_read_b128 v[116:119], v252 offset:60416\n"
    "buffer_load_dwordx4 v6, s[52:55], s76 offen offset:1024 lds\n"
    "v_mfma_f32_32x32x16_bf16 v[48:63], v[100:103], a[236:239], v[48:63]\n"
    "ds_read_b128 v[96:99], v252 offset:61440\n"
    "buffer_load_dwordx4 v7, s[52:55], s76 offen offset:2048 lds\n"
    "buffer_load_dwordx4 v8, s[52:55], s76 offen offset:3072 lds\n"
    "s_waitcnt lgkmcnt(3)\n"
    "v_mfma_f32_32x32x16_bf16 v[32:47], v[104:107], a[240:243], v[32:47]\n"
    "ds_read_b128 v[100:103], v252 offset:62464\n"
    "s_waitcnt vmcnt(12)\n"
    "ds_read_b128 a[16:19], v18 offset:8192\n"
    "ds_read_b128 a[20:23], v19 offset:8192\n"
    "ds_read_b128 a[24:27], v20 offset:8192\n"
    "v_mfma_f32_32x32x16_bf16 v[48:63], v[108:111], a[240:243], v[48:63]\n"
    "ds_read_b128 v[104:107], v252 offset:63488\n"
    "ds_read_b128 a[28:31], v21 offset:8192\n"
    "s_waitcnt lgkmcnt(7)\n"
    "v_mfma_f32_32x32x16_bf16 v[32:47], v[112:115], a[244:247], v[32:47]\n"
    "ds_read_b128 v[108:111], v252 offset:64512\n"
    "v_mfma_f32_32x32x16_bf16 v[48:63], v[116:119], a[244:247], v[48:63]\n"
    "s_add_u32 m0, s81, 16384\n"
    "s_bfe_u32 s77, s25, 0x40008\n"
    "s_mul_i32 s76, s77, 90112\n"
    "buffer_load_dwordx4 v248, s[56:59], s76 offen offset:0 sc1 lds\n"
    "buffer_load_dwordx4 v249, s[56:59], s76 offen offset:1024 sc1 lds\n"
    "s_add_u32 m0, s67, 0\n"
    "s_bfe_u32 s77, s25, 0x40010\n"
    "s_lshl_b32 s76, s77, 7\n"
    "buffer_load_dwordx4 v5, s[52:55], s76 offen offset:0 lds\n"
    "buffer_load_dwordx4 v6, s[52:55], s76 offen offset:1024 lds\n"
    "buffer_load_dwordx4 v7, s[52:55], s76 offen offset:2048 lds\n"
    "buffer_load_dwordx4 v8, s[52:55], s76 offen offset:3072 lds\n"
    "s_add_u32 m0, s81, 24576\n"
    "s_bfe_u32 s77, s25, 0x4000c\n"
    "s_mul_i32 s76, s77, 90112\n"
    "s_waitcnt vmcnt(10)\n"
    "s_barrier\n"
    "ds_read_b128 v[112:115], v252 offset:0\n"
    "s_waitcnt lgkmcnt(7)\n"
    "v_mfma_f32_32x32x16_bf16 v[32:47], v[96:99], a[248:251], v[32:47]\n"
    "ds_read_b128 v[116:119], v252 offset:1024\n"
    "v_mfma_f32_32x32x16_bf16 v[48:63], v[100:103], a[248:251], v[48:63]\n"
    "ds_read_b128 v[96:99], v252 offset:2048\n"
    "s_waitcnt lgkmcnt(3)\n"
    "v_mfma_f32_32x32x16_bf16 v[32:47], v[104:107], a[252:255], v[32:47]\n"
    "ds_read_b128 v[100:103], v252 offset:3072\n"
    "v_mfma_f32_32x32x16_bf16 v[48:63], v[108:111], a[252:255], v[48:63]\n"
    "ds_read_b128 v[104:107], v252 offset:4096\n"
    "buffer_load_dwordx4 v248, s[56:59], s76 offen offset:0 sc1 lds\n"
    "buffer_load_dwordx4 v249, s[56:59], s76 offen offset:1024 sc1 lds\n"
    "s_waitcnt lgkmcnt(3)\n"
    "v_mfma_f32_32x32x16_bf16 v[32:47], v[112:115], a[0:3], v[32:47]\n"
    "ds_read_b128 v[108:111], v252 offset:5120\n"
    "ds_read_b128 a[32:35], v18 offset:12288\n"
    "ds_read_b128 a[36:39], v19 offset:12288\n"
    "ds_read_b128 a[40:43], v20 offset:12288\n"
    "v_mfma_f32_32x32x16_bf16 v[48:63], v[116:119], a[0:3], v[48:63]\n"
    "ds_read_b128 v[112:115], v252 offset:6144\n"
    "ds_read_b128 a[44:47], v21 offset:12288\n"
    "s_waitcnt lgkmcnt(7)\n"
    "v_mfma_f32_32x32x16_bf16 v[32:47], v[96:99], a[4:7], v[32:47]\n"
    "ds_read_b128 v[116:119], v252 offset:7168\n"
    "v_mfma_f32_32x32x16_bf16 v[48:63], v[100:103], a[4:7], v[48:63]\n"
    "ds_read_b128 v[96:99], v252 offset:8192\n"
    "s_waitcnt lgkmcnt(7)\n"
    "v_mfma_f32_32x32x16_bf16 v[32:47], v[104:107], a[8:11], v[32:47]\n"
    "ds_read_b128 v[100:103], v252 offset:9216\n"
    "v_mfma_f32_32x32x16_bf16 v[48:63], v[108:111], a[8:11], v[48:63]\n"
    "ds_read_b128 v[104:107], v252 offset:10240\n"
    "s_add_u32 m0, s67, 4096\n"
    "s_bfe_u32 s77, s25, 0x40014\n"
    "s_lshl_b32 s76, s77, 7\n"
    "s_waitcnt lgkmcnt(3)\n"
    "v_mfma_f32_32x32x16_bf16 v[32:47], v[112:115], a[12:15], v[32:47]\n"
    "ds_read_b128 v[108:111], v252 offset:11264\n"
    "buffer_load_dwordx4 v5, s[52:55], s76 offen offset:0 lds\n"
    "v_mfma_f32_32x32x16_bf16 v[48:63], v[116:119], a[12:15], v[48:63]\n"
    "ds_read_b128 v[112:115], v252 offset:12288\n"
    "buffer_load_dwordx4 v6, s[52:55], s76 offen offset:1024 lds\n"
    "buffer_load_dwordx4 v7, s[52:55], s76 offen offset:2048 lds\n"
    "buffer_load_dwordx4 v8, s[52:55], s76 offen offset:3072 lds\n"
    "s_waitcnt lgkmcnt(3)\n"
    "v_mfma_f32_32x32x16_bf16 v[32:47], v[96:99], a[16:19], v[32:47]\n"
    "ds_read_b128 v[116:119], v252 offset:13312\n"
    "v_mfma_f32_32x32x16_bf16 v[48:63], v[100:103], a[16:19], v[48:63]\n"
    "ds_read_b128 v[96:99], v252 offset:14336\n"
    "s_waitcnt lgkmcnt(3)\n"
    "v_mfma_f32_32x32x16_bf16 v[32:47], v[104:107], a[20:23], v[32:47]\n"
    "ds_read_b128 v[100:103], v252 offset:15360\n"
    "s_waitcnt vmcnt(12)\n"
    "ds_read_b128 a[48:51], v18 offset:16384\n"
    "ds_read_b128 a[52:55], v19 offset:16384\n"
    "ds_read_b128 a[56:59], v20 offset:16384\n"
    "v_mfma_f32_32x32x16_bf16 v[48:63], v[108:111], a[20:23], v[48:63]\n"
    "ds_read_b128 a[60:63], v21 offset:16384\n"
    "s_waitcnt lgkmcnt(0)\n"
    "s_add_u32 m0, s81, 32768\n"
    "s_bfe_u32 s77, s25, 0x40010\n"
    "s_mul_i32 s76, s77, 90112\n"
    "buffer_load_dwordx4 v248, s[56:59], s76 offen offset:0 sc1 lds\n"
    "buffer_load_dwordx4 v249, s[56:59], s76 offen offset:1024 sc1 lds\n"
    "s_add_u32 m0, s67, 8192\n"
    "s_bfe_u32 s77, s25, 0x40018\n"
    "s_lshl_b32 s76, s77, 7\n"
    "buffer_load_dwordx4 v5, s[52:55], s76 offen offset:0 lds\n"
    "buffer_load_dwordx4 v6, s[52:55], s76 offen offset:1024 lds\n"
    "buffer_load_dwordx4 v7, s[52:55], s76 offen offset:2048 lds\n"
    "buffer_load_dwordx4 v8, s[52:55], s76 offen offset:3072 lds\n"
    "s_add_u32 m0, s81, 40960\n"
    "s_bfe_u32 s77, s25, 0x40014\n"
    "s_mul_i32 s76, s77, 90112\n"
    "s_waitcnt vmcnt(10)\n"
    "s_barrier\n"
    "ds_read_b128 v[104:107], v252 offset:16384\n"
    "v_mfma_f32_32x32x16_bf16 v[32:47], v[112:115], a[24:27], v[32:47]\n"
    "ds_read_b128 v[108:111], v252 offset:17408\n"
    "v_mfma_f32_32x32x16_bf16 v[48:63], v[116:119], a[24:27], v[48:63]\n"
    "ds_read_b128 v[112:115], v252 offset:18432\n"
    "v_mfma_f32_32x32x16_bf16 v[32:47], v[96:99], a[28:31], v[32:47]\n"
    "ds_read_b128 v[116:119], v252 offset:19456\n"
    "v_mfma_f32_32x32x16_bf16 v[48:63], v[100:103], a[28:31], v[48:63]\n"
    "ds_read_b128 v[96:99], v252 offset:20480\n"
    "buffer_load_dwordx4 v248, s[56:59], s76 offen offset:0 sc1 lds\n"
    "buffer_load_dwordx4 v249, s[56:59], s76 offen offset:1024 sc1 lds\n"
    "s_waitcnt lgkmcnt(3)\n"
    "v_mfma_f32_32x32x16_bf16 v[32:47], v[104:107], a[32:35], v[32:47]\n"
    "ds_read_b128 v[100:103], v252 offset:21504\n"
    "ds_read_b128 a[64:67], v18 offset:0\n"
    "ds_read_b128 a[68:71], v19 offset:0\n"
    "ds_read_b128 a[72:75], v20 offset:0\n"
    "v_mfma_f32_32x32x16_bf16 v[48:63], v[108:111], a[32:35], v[48:63]\n"
    "ds_read_b128 v[104:107], v252 offset:22528\n"
    "ds_read_b128 a[76:79], v21 offset:0\n"
    "s_waitcnt lgkmcnt(7)\n"
    "v_mfma_f32_32x32x16_bf16 v[32:47], v[112:115], a[36:39], v[32:47]\n"
    "ds_read_b128 v[108:111], v252 offset:23552\n"
    "v_mfma_f32_32x32x16_bf16 v[48:63], v[116:119], a[36:39], v[48:63]\n"
    "ds_read_b128 v[112:115], v252 offset:24576\n"
    "s_waitcnt lgkmcnt(7)\n"
    "v_mfma_f32_32x32x16_bf16 v[32:47], v[96:99], a[40:43], v[32:47]\n"
    "ds_read_b128 v[116:119], v252 offset:25600\n"
    "v_mfma_f32_32x32x16_bf16 v[48:63], v[100:103], a[40:43], v[48:63]\n"
    "ds_read_b128 v[96:99], v252 offset:26624\n"
    "s_add_u32 m0, s67, 12288\n"
    "s_bfe_u32 s77, s25, 0x4001c\n"
    "s_lshl_b32 s76, s77, 7\n"
    "s_waitcnt lgkmcnt(3)\n"
    "v_mfma_f32_32x32x16_bf16 v[32:47], v[104:107], a[44:47], v[32:47]\n"
    "ds_read_b128 v[100:103], v252 offset:27648\n"
    "buffer_load_dwordx4 v5, s[52:55], s76 offen offset:0 lds\n"
    "v_mfma_f32_32x32x16_bf16 v[48:63], v[108:111], a[44:47], v[48:63]\n"
    "ds_read_b128 v[104:107], v252 offset:28672\n"
    "buffer_load_dwordx4 v6, s[52:55], s76 offen offset:1024 lds\n"
    "buffer_load_dwordx4 v7, s[52:55], s76 offen offset:2048 lds\n"
    "buffer_load_dwordx4 v8, s[52:55], s76 offen offset:3072 lds\n"
    "s_waitcnt lgkmcnt(3)\n"
    "v_mfma_f32_32x32x16_bf16 v[32:47], v[112:115], a[48:51], v[32:47]\n"
    "ds_read_b128 v[108:111], v252 offset:29696\n"
    "v_mfma_f32_32x32x16_bf16 v[48:63], v[116:119], a[48:51], v[48:63]\n"
    "ds_read_b128 v[112:115], v252 offset:30720\n"
    "s_waitcnt lgkmcnt(3)\n"
    "v_mfma_f32_32x32x16_bf16 v[32:47], v[96:99], a[52:55], v[32:47]\n"
    "ds_read_b128 v[116:119], v252 offset:31744\n"
    "s_waitcnt vmcnt(12)\n"
    "ds_read_b128 a[80:83], v18 offset:4096\n"
    "ds_read_b128 a[84:87], v19 offset:4096\n"
    "ds_read_b128 a[88:91], v20 offset:4096\n"
    "v_mfma_f32_32x32x16_bf16 v[48:63], v[100:103], a[52:55], v[48:63]\n"
    "ds_read_b128 a[92:95], v21 offset:4096\n"
    "s_waitcnt lgkmcnt(0)\n"
    "s_and_b32 s77, s61, 12\n"
    "s_lshl1_add_u32 s77, s77, s90\n"
    "s_lshl2_add_u32 s95, s63, s77\n"
    "s_lshr_b32 s65, s60, 1\n"
    "s_and_b32 s82, s60, 1\n"
    "s_lshl1_add_u32 s66, s95, s65\n"
    "s_mov_b32 s44, s30\n"
    "s_and_b32 s45, s31, 0xffff\n"
    "s_mov_b32 s46, 0xac000\n"
    "s_mov_b32 s52, s32\n"
    "s_and_b32 s53, s33, 0xffff\n"
    "s_mov_b32 s54, 0x400000\n"
    "s_mov_b32 s10, 0x76543210\n"
    "s_cmp_eq_u32 s90, 1\n"
    "s_cselect_b32 s10, 0x98765432, s10\n"
    "s_cmp_eq_u32 s90, 2\n"
    "s_cselect_b32 s10, 0x10a98765, s10\n"
    "s_cmp_eq_u32 s90, 3\n"
    "s_cselect_b32 s10, 0x43210a98, s10\n"
    "s_mov_b32 s11, 0xa98\n"
    "s_cmp_eq_u32 s90, 1\n"
    "s_cselect_b32 s11, 0x10a, s11\n"
    "s_cmp_eq_u32 s90, 2\n"
    "s_cselect_b32 s11, 0x432, s11\n"
    "s_cmp_eq_u32 s90, 3\n"
    "s_cselect_b32 s11, 0x765, s11\n"
    "v_lshrrev_b32 v26, 3, v4\n"
    "v_bfe_u32 v27, v4, 4, 1\n"
    "v_lshlrev_b32 v27, 2, v27\n"
    "v_and_b32 v31, 7, v4\n"
    "v_xor_b32 v27, v27, v31\n"
    "v_lshlrev_b32 v27, 4, v27\n"
    "v_lshl_add_u32 v15, v26, 10, v27\n"
    "s_lshl_b32 s77, s60, 7\n"
    "s_add_u32 s77, s77, s62\n"
    "s_lshl_b32 s77, s77, 7\n"
    "v_add_u32 v15, s77, v15\n"
    "v_add_u32 v16, 7168, v15\n"
    "s_lshl_b32 s87, s60, 11\n"
    "s_lshr_b32 s77, s95, 1\n"
    "s_lshl_b32 s77, s77, 4\n"
    "v_and_b32 v13, 15, v4\n"
    "v_min_u32 v13, 11, v13\n"
    "v_add_u32 v13, s77, v13\n"
    "v_lshlrev_b32 v13, 7, v13\n"
    "v_add_u32 v13, 0x11000, v13\n"
    "s_add_u32 s77, s77, 15\n"
    "v_mov_b32 v27, s77\n"
    "s_and_b32 s76, s62, 2\n"
    "s_add_u32 s77, s77, s76\n"
    "s_lshr_b32 s76, s62, 2\n"
    "s_add_u32 s77, s77, s76\n"
    "s_sub_u32 s77, s77, 3\n"
    "v_mov_b32 v14, s77\n"
    "v_cndmask_b32 v14, v14, v27, s[16:17]\n"
    "v_lshlrev_b32 v14, 7, v14\n"
    "v_add_u32 v14, 0x11000, v14\n"
    "s_lshl4_add_u32 s77, s94, s91\n"
    "s_lshl_b32 s86, s77, 7\n"
    "s_add_u32 s86, s86, 0xe00000\n"
    "s_xor_b32 s88, s86, 128\n"
    "s_mul_i32 s77, s94, 12\n"
    "s_add_u32 s77, s77, s91\n"
    "s_lshl_b32 s77, s77, 12\n"
    "s_add_u32 s89, s77, 0x400000\n"
    "s_xor_b32 s85, s89, 4096\n"
    "v_mov_b32 v29, 0\n"
    "s_mul_i32 s77, s66, 22\n"
    "s_add_u32 s77, s77, s82\n"
    "s_lshl_b32 s77, s77, 11\n"
    "s_add_u32 s77, s77, 0xb00000\n"
    "v_add_u32 v28, s77, v22\n"
    "s_add_u32 m0, s81, 49152\n"
    "s_bfe_u32 s77, s25, 0x40018\n"
    "s_mul_i32 s76, s77, 90112\n"
    "buffer_load_dwordx4 v248, s[56:59], s76 offen offset:0 sc1 lds\n"
    "buffer_load_dwordx4 v249, s[56:59], s76 offen offset:1024 sc1 lds\n"
    "s_waitcnt vmcnt(6)\n"
    "s_barrier\n"
    "ds_read_b128 v[96:99], v252 offset:32768\n"
    "v_mfma_f32_32x32x16_bf16 v[32:47], v[104:107], a[56:59], v[32:47]\n"
    "ds_read_b128 v[100:103], v252 offset:33792\n"
    "s_add_u32 m0, s81, 57344\n"
    "s_bfe_u32 s77, s25, 0x4001c\n"
    "s_mul_i32 s76, s77, 90112\n"
    "v_mfma_f32_32x32x16_bf16 v[48:63], v[108:111], a[56:59], v[48:63]\n"
    "ds_read_b128 v[104:107], v252 offset:34816\n"
    "buffer_load_dwordx4 v248, s[56:59], s76 offen offset:0 sc1 lds\n"
    "buffer_load_dwordx4 v249, s[56:59], s76 offen offset:1024 sc1 lds\n"
    "v_mfma_f32_32x32x16_bf16 v[32:47], v[112:115], a[60:63], v[32:47]\n"
    "ds_read_b128 v[108:111], v252 offset:35840\n"
    "ds_read_b128 a[96:99], v18 offset:8192\n"
    "ds_read_b128 a[100:103], v19 offset:8192\n"
    "ds_read_b128 a[104:107], v20 offset:8192\n"
    "v_mfma_f32_32x32x16_bf16 v[48:63], v[116:119], a[60:63], v[48:63]\n"
    "ds_read_b128 v[112:115], v252 offset:36864\n"
    "ds_read_b128 a[108:111], v21 offset:8192\n"
    "s_add_u32 m0, s81, 0\n"
    "s_bfe_u32 s77, s24, 0x40000\n"
    "s_mul_i32 s76, s77, 90112\n"
    "buffer_load_dwordx4 v250, s[56:59], s76 offen offset:0 sc1 lds\n"
    "buffer_load_dwordx4 v251, s[56:59], s76 offen offset:1024 sc1 lds\n"
    "s_waitcnt lgkmcnt(7)\n"
    "v_mfma_f32_32x32x16_bf16 v[32:47], v[96:99], a[64:67], v[32:47]\n"
    "ds_read_b128 v[116:119], v252 offset:37888\n"
    "s_add_u32 m0, s81, 8192\n"
    "s_bfe_u32 s77, s24, 0x40004\n"
    "s_mul_i32 s76, s77, 90112\n"
    "v_mfma_f32_32x32x16_bf16 v[48:63], v[100:103], a[64:67], v[48:63]\n"
    "ds_read_b128 v[96:99], v252 offset:38912\n"
    "buffer_load_dwordx4 v250, s[56:59], s76 offen offset:0 sc1 lds\n"
    "buffer_load_dwordx4 v251, s[56:59], s76 offen offset:1024 sc1 lds\n"
    "s_waitcnt lgkmcnt(7)\n"
    "v_mfma_f32_32x32x16_bf16 v[32:47], v[104:107], a[68:71], v[32:47]\n"
    "ds_read_b128 v[100:103], v252 offset:39936\n"
    "v_mfma_f32_32x32x16_bf16 v[48:63], v[108:111], a[68:71], v[48:63]\n"
    "ds_read_b128 v[104:107], v252 offset:40960\n"
    "s_waitcnt lgkmcnt(3)\n"
    "v_mfma_f32_32x32x16_bf16 v[32:47], v[112:115], a[72:75], v[32:47]\n"
    "ds_read_b128 v[108:111], v252 offset:41984\n"
    "s_add_u32 m0, s81, 16384\n"
    "s_bfe_u32 s77, s24, 0x40008\n"
    "s_mul_i32 s76, s77, 90112\n"
    "v_mfma_f32_32x32x16_bf16 v[48:63], v[116:119], a[72:75], v[48:63]\n"
    "ds_read_b128 v[112:115], v252 offset:43008\n"
    "buffer_load_dwordx4 v250, s[56:59], s76 offen offset:0 sc1 lds\n"
    "s_waitcnt lgkmcnt(3)\n"
    "v_mfma_f32_32x32x16_bf16 v[32:47], v[96:99], a[76:79], v[32:47]\n"
    "ds_read_b128 v[116:119], v252 offset:44032\n"
    "v_mfma_f32_32x32x16_bf16 v[48:63], v[100:103], a[76:79], v[48:63]\n"
    "ds_read_b128 v[96:99], v252 offset:45056\n"
    "buffer_load_dwordx4 v251, s[56:59], s76 offen offset:1024 sc1 lds\n"
    "s_waitcnt lgkmcnt(3)\n"
    "v_mfma_f32_32x32x16_bf16 v[32:47], v[104:107], a[80:83], v[32:47]\n"
    "ds_read_b128 v[100:103], v252 offset:46080\n"
    "v_mfma_f32_32x32x16_bf16 v[48:63], v[108:111], a[80:83], v[48:63]\n"
    "ds_read_b128 v[104:107], v252 offset:47104\n"
    "s_waitcnt lgkmcnt(3)\n"
    "v_mfma_f32_32x32x16_bf16 v[32:47], v[112:115], a[84:87], v[32:47]\n"
    "ds_read_b128 v[108:111], v252 offset:48128\n"
    "s_waitcnt vmcnt(10)\n"
    "ds_read_b128 a[112:115], v18 offset:12288\n"
    "ds_read_b128 a[116:119], v19 offset:12288\n"
    "ds_read_b128 a[120:123], v20 offset:12288\n"
    "v_mfma_f32_32x32x16_bf16 v[48:63], v[116:119], a[84:87], v[48:63]\n"
    "ds_read_b128 a[124:127], v21 offset:12288\n"
    "s_waitcnt lgkmcnt(0)\n"
    "s_add_u32 m0, s81, 24576\n"
    "s_bfe_u32 s77, s24, 0x4000c\n"
    "s_mul_i32 s76, s77, 90112\n"
    "buffer_load_dwordx4 v250, s[56:59], s76 offen offset:0 sc1 lds\n"
    "buffer_load_dwordx4 v251, s[56:59], s76 offen offset:1024 sc1 lds\n"
    "s_add_u32 m0, s87, 0\n"
    "s_bfe_u32 s77, s10, 0x40000\n"
    "s_add_u32 s77, s77, s62\n"
    "s_sub_u32 s76, s77, 11\n"
    "s_min_u32 s77, s77, s76\n"
    "s_lshl_b32 s76, s77, 16\n"
    "buffer_load_dwordx4 v15, s[44:47], s76 offen offset:0 lds\n"
    "buffer_load_dwordx4 v16, s[44:47], s76 offen offset:1024 lds\n"
    "v_lshrrev_b32 v26, 4, v4\n"
    "s_lshl_b32 s77, s66, 5\n"
    "s_lshl2_add_u32 s77, s82, s77\n"
    "v_add_u32 v26, s77, v26\n"
    "v_and_b32 v27, 15, v4\n"
    "s_lshl_b32 s77, s62, 7\n"
    "v_lshl_add_u32 v27, v27, 3, s77\n"
    "s_waitcnt vmcnt(10)\n"
    "s_barrier\n"
    "ds_read_b128 v[112:115], v252 offset:49152\n"
    "v_mfma_f32_32x32x16_bf16 v[32:47], v[96:99], a[88:91], v[32:47]\n"
    "ds_read_b128 v[116:119], v252 offset:50176\n"
    "v_mfma_f32_32x32x16_bf16 v[48:63], v[100:103], a[88:91], v[48:63]\n"
    "ds_read_b128 v[96:99], v252 offset:51200\n"
    "v_mfma_f32_32x32x16_bf16 v[32:47], v[104:107], a[92:95], v[32:47]\n"
    "ds_read_b128 v[100:103], v252 offset:52224\n"
    "v_mfma_f32_32x32x16_bf16 v[48:63], v[108:111], a[92:95], v[48:63]\n"
    "ds_read_b128 v[104:107], v252 offset:53248\n"
    "global_load_dwordx2 v[184:185], v27, s[34:35]\n"
    "v_lshl_add_u32 v27, v26, 10, v27\n"
    "v_lshlrev_b32 v9, 1, v27\n"
    "s_mov_b32 s76, 0\n"
    "buffer_load_dwordx2 v[176:177], v27, s[40:43], s76 offen\n"
    "s_mov_b32 s76, 8192\n"
    "buffer_load_dwordx2 v[178:179], v27, s[40:43], s76 offen\n"
    "s_mov_b32 s76, 16384\n"
    "buffer_load_dwordx2 v[180:181], v27, s[40:43], s76 offen\n"
    "s_mov_b32 s76, 24576\n"
    "buffer_load_dwordx2 v[182:183], v27, s[40:43], s76 offen\n"
    "s_waitcnt lgkmcnt(3)\n"
    "v_mfma_f32_32x32x16_bf16 v[32:47], v[112:115], a[96:99], v[32:47]\n"
    "ds_read_b128 v[108:111], v252 offset:54272\n"
    "v_mfma_f32_32x32x16_bf16 v[48:63], v[116:119], a[96:99], v[48:63]\n"
    "ds_read_b128 v[112:115], v252 offset:55296\n"
    "s_waitcnt lgkmcnt(3)\n"
    "v_mfma_f32_32x32x16_bf16 v[32:47], v[96:99], a[100:103], v[32:47]\n"
    "ds_read_b128 v[116:119], v252 offset:56320\n"
    "s_add_u32 m0, s81, 32768\n"
    "s_bfe_u32 s77, s24, 0x40010\n"
    "s_mul_i32 s76, s77, 90112\n"
    "v_mfma_f32_32x32x16_bf16 v[48:63], v[100:103], a[100:103], v[48:63]\n"
    "ds_read_b128 v[96:99], v252 offset:57344\n"
    "buffer_load_dwordx4 v250, s[56:59], s76 offen offset:0 sc1 lds\n"
    "s_waitcnt lgkmcnt(3)\n"
    "v_mfma_f32_32x32x16_bf16 v[32:47], v[104:107], a[104:107], v[32:47]\n"
    "ds_read_b128 v[100:103], v252 offset:58368\n"
    "buffer_load_dwordx4 v251, s[56:59], s76 offen offset:1024 sc1 lds\n"
    "v_mfma_f32_32x32x16_bf16 v[48:63], v[108:111], a[104:107], v[48:63]\n"
    "ds_read_b128 v[104:107], v252 offset:59392\n"
    "s_waitcnt lgkmcnt(3)\n"
    "v_mfma_f32_32x32x16_bf16 v[32:47], v[112:115], a[108:111], v[32:47]\n"
    "ds_read_b128 v[108:111], v252 offset:60416\n"
    "v_mfma_f32_32x32x16_bf16 v[48:63], v[116:119], a[108:111], v[48:63]\n"
    "ds_read_b128 v[112:115], v252 offset:61440\n"
    "s_add_u32 m0, s81, 40960\n"
    "s_bfe_u32 s77, s24, 0x40014\n"
    "s_mul_i32 s76, s77, 90112\n"
    "s_waitcnt lgkmcnt(3)\n"
    "v_mfma_f32_32x32x16_bf16 v[32:47], v[96:99], a[112:115], v[32:47]\n"
    "ds_read_b128 v[116:119], v252 offset:62464\n"
    "buffer_load_dwordx4 v250, s[56:59], s76 offen offset:0 sc1 lds\n"
    "v_mfma_f32_32x32x16_bf16 v[48:63], v[100:103], a[112:115], v[48:63]\n"
    "ds_read_b128 v[96:99], v252 offset:63488\n"
    "s_waitcnt lgkmcnt(3)\n"
    "v_mfma_f32_32x32x16_bf16 v[32:47], v[104:107], a[116:119], v[32:47]\n"
    "ds_read_b128 v[100:103], v252 offset:64512\n"
    "buffer_load_dwordx4 v251, s[56:59], s76 offen offset:1024 sc1 lds\n"
    "v_mfma_f32_32x32x16_bf16 v[48:63], v[108:111], a[116:119], v[48:63]\n"
    "s_waitcnt vmcnt(15)\n"
    "s_barrier\n"
    "ds_read_b128 v[104:107], v252 offset:0\n"
    "s_waitcnt lgkmcnt(3)\n"
    "v_mfma_f32_32x32x16_bf16 v[32:47], v[112:115], a[120:123], v[32:47]\n"
    "ds_read_b128 v[108:111], v252 offset:1024\n"
    "v_mfma_f32_32x32x16_bf16 v[48:63], v[116:119], a[120:123], v[48:63]\n"
    "ds_read_b128 v[112:115], v252 offset:2048\n"
    "s_add_u32 m0, s81, 49152\n"
    "s_bfe_u32 s77, s24, 0x40018\n"
    "s_mul_i32 s76, s77, 90112\n"
    "s_waitcnt lgkmcnt(3)\n"
    "v_mfma_f32_32x32x16_bf16 v[32:47], v[96:99], a[124:127], v[32:47]\n"
    "ds_read_b128 v[116:119], v252 offset:3072\n"
    "buffer_load_dwordx4 v250, s[56:59], s76 offen offset:0 sc1 lds\n"
    "v_mfma_f32_32x32x16_bf16 v[48:63], v[100:103], a[124:127], v[48:63]\n"
    "ds_read_b128 v[96:99], v252 offset:4096\n"
    "buffer_load_dwordx4 v251, s[56:59], s76 offen offset:1024 sc1 lds\n"
    "s_waitcnt lgkmcnt(3)\n"
    "v_mfma_f32_32x32x16_bf16 v[64:79], v[104:107], a[128:131], v[64:79]\n"
    "ds_read_b128 v[100:103], v252 offset:5120\n"
    "v_mfma_f32_32x32x16_bf16 v[80:95], v[108:111], a[128:131], v[80:95]\n"
    "ds_read_b128 v[104:107], v252 offset:6144\n"
    "s_waitcnt lgkmcnt(3)\n"
    "v_mfma_f32_32x32x16_bf16 v[64:79], v[112:115], a[132:135], v[64:79]\n"
    "ds_read_b128 v[108:111], v252 offset:7168\n"
    "v_mfma_f32_32x32x16_bf16 v[80:95], v[116:119], a[132:135], v[80:95]\n"
    "ds_read_b128 v[112:115], v252 offset:8192\n"
    "v_cvt_pk_bf16_f32 v32, v32, v33\n"
    "v_cvt_pk_bf16_f32 v33, v34, v35\n"
    "v_cvt_pk_bf16_f32 v34, v36, v37\n"
    "v_cvt_pk_bf16_f32 v35, v38, v39\n"
    "s_waitcnt lgkmcnt(3)\n"
    "v_mfma_f32_32x32x16_bf16 v[64:79], v[96:99], a[136:139], v[64:79]\n"
    "ds_read_b128 v[116:119], v252 offset:9216\n"
    "buffer_store_dwordx4 v[32:35], v22, s[56:59], s85 offen offset:0 sc1\n"
    "v_cvt_pk_bf16_f32 v40, v40, v41\n"
    "v_cvt_pk_bf16_f32 v41, v42, v43\n"
    "v_cvt_pk_bf16_f32 v42, v44, v45\n"
    "v_mfma_f32_32x32x16_bf16 v[80:95], v[100:103], a[136:139], v[80:95]\n"
    "ds_read_b128 v[96:99], v252 offset:10240\n"
    "v_cvt_pk_bf16_f32 v43, v46, v47\n"
    "buffer_store_dwordx4 v[40:43], v22, s[56:59], s85 offen offset:1024 sc1\n"
    "v_cvt_pk_bf16_f32 v48, v48, v49\n"
    "v_cvt_pk_bf16_f32 v49, v50, v51\n"
    "s_waitcnt lgkmcnt(3)\n"
    "v_mfma_f32_32x32x16_bf16 v[64:79], v[104:107], a[140:143], v[64:79]\n"
    "ds_read_b128 v[100:103], v252 offset:11264\n"
    "v_cvt_pk_bf16_f32 v50, v52, v53\n"
    "v_cvt_pk_bf16_f32 v51, v54, v55\n"
    "buffer_store_dwordx4 v[48:51], v22, s[56:59], s85 offen offset:2048 sc1\n"
    "v_cvt_pk_bf16_f32 v56, v56, v57\n"
    "v_mfma_f32_32x32x16_bf16 v[80:95], v[108:111], a[140:143], v[80:95]\n"
    "ds_read_b128 v[104:107], v252 offset:12288\n"
    "v_cvt_pk_bf16_f32 v57, v58, v59\n"
    "v_cvt_pk_bf16_f32 v58, v60, v61\n"
    "v_cvt_pk_bf16_f32 v59, v62, v63\n"
    "buffer_store_dwordx4 v[56:59], v22, s[56:59], s85 offen offset:3072 sc1\n"
    "s_waitcnt lgkmcnt(3)\n"
    "v_mfma_f32_32x32x16_bf16 v[64:79], v[112:115], a[144:147], v[64:79]\n"
    "ds_read_b128 v[108:111], v252 offset:13312\n"
    "v_mfma_f32_32x32x16_bf16 v[80:95], v[116:119], a[144:147], v[80:95]\n"
    "ds_read_b128 v[112:115], v252 offset:14336\n"
    "s_waitcnt lgkmcnt(3)\n"
    "v_mfma_f32_32x32x16_bf16 v[64:79], v[96:99], a[148:151], v[64:79]\n"
    "ds_read_b128 v[116:119], v252 offset:15360\n"
    "v_mfma_f32_32x32x16_bf16 v[80:95], v[100:103], a[148:151], v[80:95]\n"
    "s_waitcnt vmcnt(17)\n"
    "s_barrier\n"
    "ds_read_b128 v[96:99], v252 offset:16384\n"
    "s_waitcnt lgkmcnt(3)\n"
    "v_mfma_f32_32x32x16_bf16 v[64:79], v[104:107], a[152:155], v[64:79]\n"
    "ds_read_b128 v[100:103], v252 offset:17408\n"
    "v_mfma_f32_32x32x16_bf16 v[80:95], v[108:111], a[152:155], v[80:95]\n"
    "ds_read_b128 v[104:107], v252 offset:18432\n"
    "s_waitcnt lgkmcnt(3)\n"
    "v_mfma_f32_32x32x16_bf16 v[64:79], v[112:115], a[156:159], v[64:79]\n"
    "ds_read_b128 v[108:111], v252 offset:19456\n"
    "v_mfma_f32_32x32x16_bf16 v[80:95], v[116:119], a[156:159], v[80:95]\n"
    "ds_read_b128 v[112:115], v252 offset:20480\n"
    "s_waitcnt lgkmcnt(3)\n"
    "v_mfma_f32_32x32x16_bf16 v[64:79], v[96:99], a[160:163], v[64:79]\n"
    "ds_read_b128 v[116:119], v252 offset:21504\n"
    "v_mfma_f32_32x32x16_bf16 v[80:95], v[100:103], a[160:163], v[80:95]\n"
    "ds_read_b128 v[96:99], v252 offset:22528\n"
    "s_waitcnt lgkmcnt(3)\n"
    "v_mfma_f32_32x32x16_bf16 v[64:79], v[104:107], a[164:167], v[64:79]\n"
    "ds_read_b128 v[100:103], v252 offset:23552\n"
    "v_mfma_f32_32x32x16_bf16 v[80:95], v[108:111], a[164:167], v[80:95]\n"
    "ds_read_b128 v[104:107], v252 offset:24576\n"
    "s_waitcnt lgkmcnt(3)\n"
    "v_mfma_f32_32x32x16_bf16 v[64:79], v[112:115], a[168:171], v[64:79]\n"
    "ds_read_b128 v[108:111], v252 offset:25600\n"
    "v_mfma_f32_32x32x16_bf16 v[80:95], v[116:119], a[168:171], v[80:95]\n"
    "ds_read_b128 v[112:115], v252 offset:26624\n"
    "s_waitcnt lgkmcnt(3)\n"
    "v_mfma_f32_32x32x16_bf16 v[64:79], v[96:99], a[172:175], v[64:79]\n"
    "ds_read_b128 v[116:119], v252 offset:27648\n"
    "v_mfma_f32_32x32x16_bf16 v[80:95], v[100:103], a[172:175], v[80:95]\n"
    "ds_read_b128 v[96:99], v252 offset:28672\n"
    "s_add_u32 m0, s81, 57344\n"
    "s_bfe_u32 s77, s24, 0x4001c\n"
    "s_mul_i32 s76, s77, 90112\n"
    "s_waitcnt lgkmcnt(3)\n"
    "v_mfma_f32_32x32x16_bf16 v[64:79], v[104:107], a[176:179], v[64:79]\n"
    "ds_read_b128 v[100:103], v252 offset:29696\n"
    "buffer_load_dwordx4 v250, s[56:59], s76 offen offset:0 sc1 lds\n"
    "v_mfma_f32_32x32x16_bf16 v[80:95], v[108:111], a[176:179], v[80:95]\n"
    "ds_read_b128 v[104:107], v252 offset:30720\n"
    "s_waitcnt lgkmcnt(3)\n"
    "v_mfma_f32_32x32x16_bf16 v[64:79], v[112:115], a[180:183], v[64:79]\n"
    "ds_read_b128 v[108:111], v252 offset:31744\n"
    "buffer_load_dwordx4 v251, s[56:59], s76 offen offset:1024 sc1 lds\n"
    "v_mfma_f32_32x32x16_bf16 v[80:95], v[116:119], a[180:183], v[80:95]\n"
    "s_add_u32 m0, s81, 0\n"
    "s_bfe_u32 s77, s25, 0x40000\n"
    "s_mul_i32 s76, s77, 90112\n"
    "buffer_load_dwordx4 v250, s[56:59], s76 offen offset:0 sc1 lds\n"
    "buffer_load_dwordx4 v251, s[56:59], s76 offen offset:1024 sc1 lds\n"
    "s_add_u32 m0, s87, 8192\n"
    "s_bfe_u32 s77, s10, 0x40004\n"
    "s_add_u32 s77, s77, s62\n"
    "s_sub_u32 s76, s77, 11\n"
    "s_min_u32 s77, s77, s76\n"
    "s_lshl_b32 s76, s77, 16\n"
    "buffer_load_dwordx4 v15, s[44:47], s76 offen offset:0 lds\n"
    "buffer_load_dwordx4 v16, s[44:47], s76 offen offset:1024 lds\n"
    "s_add_u32 m0, s87, 16384\n"
    "s_bfe_u32 s77, s10, 0x40008\n"
    "s_add_u32 s77, s77, s62\n"
    "s_sub_u32 s76, s77, 11\n"
    "s_min_u32 s77, s77, s76\n"
    "s_lshl_b32 s76, s77, 16\n"
    "buffer_load_dwordx4 v15, s[44:47], s76 offen offset:0 lds\n"
    "buffer_load_dwordx4 v16, s[44:47], s76 offen offset:1024 lds\n"
    "s_add_u32 m0, s87, 24576\n"
    "s_bfe_u32 s77, s10, 0x4000c\n"
    "s_add_u32 s77, s77, s62\n"
    "s_sub_u32 s76, s77, 11\n"
    "s_min_u32 s77, s77, s76\n"
    "s_lshl_b32 s76, s77, 16\n"
    "buffer_load_dwordx4 v15, s[44:47], s76 offen offset:0 lds\n"
    "buffer_load_dwordx4 v16, s[44:47], s76 offen offset:1024 lds\n"
    "s_add_u32 m0, s87, 32768\n"
    "s_bfe_u32 s77, s10, 0x40010\n"
    "s_add_u32 s77, s77, s62\n"
    "s_sub_u32 s76, s77, 11\n"
    "s_min_u32 s77, s77, s76\n"
    "s_lshl_b32 s76, s77, 16\n"
    "s_waitcnt vmcnt(10)\n"
    "buffer_store_dwordx2 v[24:25], v23, s[56:59], s86 offen sc1\n"
    "s_barrier\n"
    "ds_read_b128 v[112:115], v252 offset:32768\n"
    "s_waitcnt lgkmcnt(3)\n"
    "v_mfma_f32_32x32x16_bf16 v[64:79], v[96:99], a[184:187], v[64:79]\n"
    "ds_read_b128 v[116:119], v252 offset:33792\n"
    "v_mfma_f32_32x32x16_bf16 v[80:95], v[100:103], a[184:187], v[80:95]\n"
    "ds_read_b128 v[96:99], v252 offset:34816\n"
    "s_waitcnt lgkmcnt(3)\n"
    "v_mfma_f32_32x32x16_bf16 v[64:79], v[104:107], a[188:191], v[64:79]\n"
    "ds_read_b128 v[100:103], v252 offset:35840\n"
    "v_mfma_f32_32x32x16_bf16 v[80:95], v[108:111], a[188:191], v[80:95]\n"
    "ds_read_b128 v[104:107], v252 offset:36864\n"
    "s_waitcnt lgkmcnt(3)\n"
    "v_mfma_f32_32x32x16_bf16 v[64:79], v[112:115], a[192:195], v[64:79]\n"
    "ds_read_b128 v[108:111], v252 offset:37888\n"
    "buffer_load_dwordx4 v15, s[44:47], s76 offen offset:0 lds\n"
    "v_mfma_f32_32x32x16_bf16 v[80:95], v[116:119], a[192:195], v[80:95]\n"
    "ds_read_b128 v[112:115], v252 offset:38912\n"
    "s_waitcnt lgkmcnt(3)\n"
    "v_mfma_f32_32x32x16_bf16 v[64:79], v[96:99], a[196:199], v[64:79]\n"
    "ds_read_b128 v[116:119], v252 offset:39936\n"
    "v_mfma_f32_32x32x16_bf16 v[80:95], v[100:103], a[196:199], v[80:95]\n"
    "ds_read_b128 v[96:99], v252 offset:40960\n"
    "buffer_load_dwordx4 v16, s[44:47], s76 offen offset:1024 lds\n"
    "s_waitcnt lgkmcnt(3)\n"
    "v_mfma_f32_32x32x16_bf16 v[64:79], v[104:107], a[200:203], v[64:79]\n"
    "ds_read_b128 v[100:103], v252 offset:41984\n"
    "v_mfma_f32_32x32x16_bf16 v[80:95], v[108:111], a[200:203], v[80:95]\n"
    "ds_read_b128 v[104:107], v252 offset:43008\n"
    "s_waitcnt lgkmcnt(3)\n"
    "v_mfma_f32_32x32x16_bf16 v[64:79], v[112:115], a[204:207], v[64:79]\n"
    "ds_read_b128 v[108:111], v252 offset:44032\n"
    "s_add_u32 m0, s81, 8192\n"
    "s_bfe_u32 s77, s25, 0x40004\n"
    "s_mul_i32 s76, s77, 90112\n"
    "v_mfma_f32_32x32x16_bf16 v[80:95], v[116:119], a[204:207], v[80:95]\n"
    "ds_read_b128 v[112:115], v252 offset:45056\n"
    "buffer_load_dwordx4 v250, s[56:59], s76 offen offset:0 sc1 lds\n"
    "s_waitcnt lgkmcnt(3)\n"
    "v_mfma_f32_32x32x16_bf16 v[64:79], v[96:99], a[208:211], v[64:79]\n"
    "ds_read_b128 v[116:119], v252 offset:46080\n"
    "v_mfma_f32_32x32x16_bf16 v[80:95], v[100:103], a[208:211], v[80:95]\n"
    "ds_read_b128 v[96:99], v252 offset:47104\n"
    "buffer_load_dwordx4 v251, s[56:59], s76 offen offset:1024 sc1 lds\n"
    "s_waitcnt lgkmcnt(3)\n"
    "v_mfma_f32_32x32x16_bf16 v[64:79], v[104:107], a[212:215], v[64:79]\n"
    "ds_read_b128 v[100:103], v252 offset:48128\n"
    "v_mfma_f32_32x32x16_bf16 v[80:95], v[108:111], a[212:215], v[80:95]\n"
    "s_waitcnt vmcnt(13)\n"
    "s_barrier\n"
    "ds_read_b128 v[104:107], v252 offset:49152\n"
    "s_waitcnt lgkmcnt(3)\n"
    "v_mfma_f32_32x32x16_bf16 v[64:79], v[112:115], a[216:219], v[64:79]\n"
    "ds_read_b128 v[108:111], v252 offset:50176\n"
    "s_add_u32 m0, s81, 16384\n"
    "s_bfe_u32 s77, s25, 0x40008\n"
    "s_mul_i32 s76, s77, 90112\n"
    "v_mfma_f32_32x32x16_bf16 v[80:95], v[116:119], a[216:219], v[80:95]\n"
    "ds_read_b128 v[112:115], v252 offset:51200\n"
    "buffer_load_dwordx4 v250, s[56:59], s76 offen offset:0 sc1 lds\n"
    "s_waitcnt lgkmcnt(3)\n"
    "v_mfma_f32_32x32x16_bf16 v[64:79], v[96:99], a[220:223], v[64:79]\n"
    "ds_read_b128 v[116:119], v252 offset:52224\n"
    "v_mfma_f32_32x32x16_bf16 v[80:95], v[100:103], a[220:223], v[80:95]\n"
    "ds_read_b128 v[96:99], v252 offset:53248\n"
    "buffer_load_dwordx4 v251, s[56:59], s76 offen offset:1024 sc1 lds\n"
    "s_waitcnt lgkmcnt(3)\n"
    "v_mfma_f32_32x32x16_bf16 v[64:79], v[104:107], a[224:227], v[64:79]\n"
    "ds_read_b128 v[100:103], v252 offset:54272\n"
    "v_mfma_f32_32x32x16_bf16 v[80:95], v[108:111], a[224:227], v[80:95]\n"
    "ds_read_b128 v[104:107], v252 offset:55296\n"
    "s_waitcnt lgkmcnt(3)\n"
    "v_mfma_f32_32x32x16_bf16 v[64:79], v[112:115], a[228:231], v[64:79]\n"
    "ds_read_b128 v[108:111], v252 offset:56320\n"
    "s_add_u32 m0, s81, 24576\n"
    "s_bfe_u32 s77, s25, 0x4000c\n"
    "s_mul_i32 s76, s77, 90112\n"
    "v_mfma_f32_32x32x16_bf16 v[80:95], v[116:119], a[228:231], v[80:95]\n"
    "ds_read_b128 v[112:115], v252 offset:57344\n"
    "buffer_load_dwordx4 v250, s[56:59], s76 offen offset:0 sc1 lds\n"
    "s_waitcnt lgkmcnt(3)\n"
    "v_mfma_f32_32x32x16_bf16 v[64:79], v[96:99], a[232:235], v[64:79]\n"
    "ds_read_b128 v[116:119], v252 offset:58368\n"
    "v_mfma_f32_32x32x16_bf16 v[80:95], v[100:103], a[232:235], v[80:95]\n"
    "ds_read_b128 v[96:99], v252 offset:59392\n"
    "buffer_load_dwordx4 v251, s[56:59], s76 offen offset:1024 sc1 lds\n"
    "s_waitcnt lgkmcnt(3)\n"
    "v_mfma_f32_32x32x16_bf16 v[64:79], v[104:107], a[236:239], v[64:79]\n"
    "ds_read_b128 v[100:103], v252 offset:60416\n"
    "v_mfma_f32_32x32x16_bf16 v[80:95], v[108:111], a[236:239], v[80:95]\n"
    "ds_read_b128 v[104:107], v252 offset:61440\n"
    "s_waitcnt lgkmcnt(3)\n"
    "v_mfma_f32_32x32x16_bf16 v[64:79], v[112:115], a[240:243], v[64:79]\n"
    "ds_read_b128 v[108:111], v252 offset:62464\n"
    "s_add_u32 m0, s81, 32768\n"
    "s_bfe_u32 s77, s25, 0x40010\n"
    "s_mul_i32 s76, s77, 90112\n"
    "v_mfma_f32_32x32x16_bf16 v[80:95], v[116:119], a[240:243], v[80:95]\n"
    "ds_read_b128 v[112:115], v252 offset:63488\n"
    "buffer_load_dwordx4 v250, s[56:59], s76 offen offset:0 sc1 lds\n"
    "s_waitcnt lgkmcnt(3)\n"
    "v_mfma_f32_32x32x16_bf16 v[64:79], v[96:99], a[244:247], v[64:79]\n"
    "ds_read_b128 v[116:119], v252 offset:64512\n"
    "v_mfma_f32_32x32x16_bf16 v[80:95], v[100:103], a[244:247], v[80:95]\n"
    "buffer_load_dwordx4 v251, s[56:59], s76 offen offset:1024 sc1 lds\n"
    "s_add_u32 m0, s87, 40960\n"
    "s_bfe_u32 s77, s10, 0x40014\n"
    "s_add_u32 s77, s77, s62\n"
    "s_sub_u32 s76, s77, 11\n"
    "s_min_u32 s77, s77, s76\n"
    "s_lshl_b32 s76, s77, 16\n"
    "buffer_load_dwordx4 v15, s[44:47], s76 offen offset:0 lds\n"
    "buffer_load_dwordx4 v16, s[44:47], s76 offen offset:1024 lds\n"
    "s_add_u32 m0, s87, 49152\n"
    "s_bfe_u32 s77, s10, 0x40018\n"
    "s_add_u32 s77, s77, s62\n"
    "s_sub_u32 s76, s77, 11\n"
    "s_min_u32 s77, s77, s76\n"
    "s_lshl_b32 s76, s77, 16\n"
    "buffer_load_dwordx4 v15, s[44:47], s76 offen offset:0 lds\n"
    "buffer_load_dwordx4 v16, s[44:47], s76 offen offset:1024 lds\n"
    "s_add_u32 m0, s87, 57344\n"
    "s_bfe_u32 s77, s10, 0x4001c\n"
    "s_add_u32 s77, s77, s62\n"
    "s_sub_u32 s76, s77, 11\n"
    "s_min_u32 s77, s77, s76\n"
    "s_lshl_b32 s76, s77, 16\n"
    "s_waitcnt vmcnt(10)\n"
    "s_barrier\n"
    "ds_read_b128 v[96:99], v252 offset:0\n"
    "s_waitcnt lgkmcnt(3)\n"
    "v_mfma_f32_32x32x16_bf16 v[64:79], v[104:107], a[248:251], v[64:79]\n"
    "ds_read_b128 v[100:103], v252 offset:1024\n"
    "v_mfma_f32_32x32x16_bf16 v[80:95], v[108:111], a[248:251], v[80:95]\n"
    "ds_read_b128 v[104:107], v252 offset:2048\n"
    "s_waitcnt lgkmcnt(3)\n"
    "v_mfma_f32_32x32x16_bf16 v[64:79], v[112:115], a[252:255], v[64:79]\n"
    "ds_read_b128 v[108:111], v252 offset:3072\n"
    "v_mfma_f32_32x32x16_bf16 v[80:95], v[116:119], a[252:255], v[80:95]\n"
    "ds_read_b128 v[112:115], v252 offset:4096\n"
    "s_waitcnt lgkmcnt(3)\n"
    "v_mfma_f32_32x32x16_bf16 v[64:79], v[96:99], a[0:3], v[64:79]\n"
    "ds_read_b128 v[116:119], v252 offset:5120\n"
    "v_mfma_f32_32x32x16_bf16 v[80:95], v[100:103], a[0:3], v[80:95]\n"
    "ds_read_b128 v[96:99], v252 offset:6144\n"
    "buffer_load_dwordx4 v15, s[44:47], s76 offen offset:0 lds\n"
    "s_waitcnt lgkmcnt(3)\n"
    "v_mfma_f32_32x32x16_bf16 v[64:79], v[104:107], a[4:7], v[64:79]\n"
    "ds_read_b128 v[100:103], v252 offset:7168\n"
    "v_mfma_f32_32x32x16_bf16 v[80:95], v[108:111], a[4:7], v[80:95]\n"
    "ds_read_b128 v[104:107], v252 offset:8192\n"
    "s_waitcnt lgkmcnt(3)\n"
    "v_mfma_f32_32x32x16_bf16 v[64:79], v[112:115], a[8:11], v[64:79]\n"
    "ds_read_b128 v[108:111], v252 offset:9216\n"
    "buffer_load_dwordx4 v16, s[44:47], s76 offen offset:1024 lds\n"
    "v_mfma_f32_32x32x16_bf16 v[80:95], v[116:119], a[8:11], v[80:95]\n"
    "ds_read_b128 v[112:115], v252 offset:10240\n"
    "s_waitcnt lgkmcnt(3)\n"
    "v_mfma_f32_32x32x16_bf16 v[64:79], v[96:99], a[12:15], v[64:79]\n"
    "ds_read_b128 v[116:119], v252 offset:11264\n"
    "s_add_u32 m0, s81, 40960\n"
    "s_bfe_u32 s77, s25, 0x40014\n"
    "s_mul_i32 s76, s77, 90112\n"
    "v_mfma_f32_32x32x16_bf16 v[80:95], v[100:103], a[12:15], v[80:95]\n"
    "ds_read_b128 v[96:99], v252 offset:12288\n"
    "buffer_load_dwordx4 v250, s[56:59], s76 offen offset:0 sc1 lds\n"
    "s_waitcnt lgkmcnt(3)\n"
    "v_mfma_f32_32x32x16_bf16 v[64:79], v[104:107], a[16:19], v[64:79]\n"
    "ds_read_b128 v[100:103], v252 offset:13312\n"
    "v_mfma_f32_32x32x16_bf16 v[80:95], v[108:111], a[16:19], v[80:95]\n"
    "ds_read_b128 v[104:107], v252 offset:14336\n"
    "buffer_load_dwordx4 v251, s[56:59], s76 offen offset:1024 sc1 lds\n"
    "s_waitcnt lgkmcnt(3)\n"
    "v_mfma_f32_32x32x16_bf16 v[64:79], v[112:115], a[20:23], v[64:79]\n"
    "ds_read_b128 v[108:111], v252 offset:15360\n"
    "v_mfma_f32_32x32x16_bf16 v[80:95], v[116:119], a[20:23], v[80:95]\n"
    "s_waitcnt vmcnt(10)\n"
    "s_barrier\n"
    "buffer_load_dwordx2 v[2:3], v29, s[56:59], s88 offen sc1\n"
    "ds_read_b128 v[112:115], v252 offset:16384\n"
    "s_waitcnt lgkmcnt(3)\n"
    "v_mfma_f32_32x32x16_bf16 v[64:79], v[96:99], a[24:27], v[64:79]\n"
    "ds_read_b128 v[116:119], v252 offset:17408\n"
    "s_add_u32 m0, s81, 49152\n"
    "s_bfe_u32 s77, s25, 0x40018\n"
    "s_mul_i32 s76, s77, 90112\n"
    "v_mfma_f32_32x32x16_bf16 v[80:95], v[100:103], a[24:27], v[80:95]\n"
    "ds_read_b128 v[96:99], v252 offset:18432\n"
    "buffer_load_dwordx4 v250, s[56:59], s76 offen offset:0 sc1 lds\n"
    "s_waitcnt lgkmcnt(3)\n"
    "v_mfma_f32_32x32x16_bf16 v[64:79], v[104:107], a[28:31], v[64:79]\n"
    "ds_read_b128 v[100:103], v252 offset:19456\n"
    "buffer_load_dwordx4 v251, s[56:59], s76 offen offset:1024 sc1 lds\n"
    "v_mfma_f32_32x32x16_bf16 v[80:95], v[108:111], a[28:31], v[80:95]\n"
    "ds_read_b128 v[104:107], v252 offset:20480\n"
    "s_waitcnt lgkmcnt(3)\n"
    "v_mfma_f32_32x32x16_bf16 v[64:79], v[112:115], a[32:35], v[64:79]\n"
    "ds_read_b128 v[108:111], v252 offset:21504\n"
    "v_mfma_f32_32x32x16_bf16 v[80:95], v[116:119], a[32:35], v[80:95]\n"
    "ds_read_b128 v[112:115], v252 offset:22528\n"
    "s_waitcnt lgkmcnt(3)\n"
    "v_mfma_f32_32x32x16_bf16 v[64:79], v[96:99], a[36:39], v[64:79]\n"
    "ds_read_b128 v[116:119], v252 offset:23552\n"
    "s_add_u32 m0, s81, 57344\n"
    "s_bfe_u32 s77, s25, 0x4001c\n"
    "s_mul_i32 s76, s77, 90112\n"
    "v_mfma_f32_32x32x16_bf16 v[80:95], v[100:103], a[36:39], v[80:95]\n"
    "ds_read_b128 v[96:99], v252 offset:24576\n"
    "buffer_load_dwordx4 v250, s[56:59], s76 offen offset:0 sc1 lds\n"
    "s_waitcnt lgkmcnt(3)\n"
    "v_mfma_f32_32x32x16_bf16 v[64:79], v[104:107], a[40:43], v[64:79]\n"
    "ds_read_b128 v[100:103], v252 offset:25600\n"
    "v_mfma_f32_32x32x16_bf16 v[80:95], v[108:111], a[40:43], v[80:95]\n"
    "ds_read_b128 v[104:107], v252 offset:26624\n"
    "buffer_load_dwordx4 v251, s[56:59], s76 offen offset:1024 sc1 lds\n"
    "s_waitcnt lgkmcnt(3)\n"
    "v_mfma_f32_32x32x16_bf16 v[64:79], v[112:115], a[44:47], v[64:79]\n"
    "ds_read_b128 v[108:111], v252 offset:27648\n"
    "v_mfma_f32_32x32x16_bf16 v[80:95], v[116:119], a[44:47], v[80:95]\n"
    "ds_read_b128 v[112:115], v252 offset:28672\n"
    "s_waitcnt lgkmcnt(3)\n"
    "v_mfma_f32_32x32x16_bf16 v[64:79], v[96:99], a[48:51], v[64:79]\n"
    "ds_read_b128 v[116:119], v252 offset:29696\n"
    "s_add_u32 m0, s87, 65536\n"
    "s_bfe_u32 s77, s11, 0x40000\n"
    "s_add_u32 s77, s77, s62\n"
    "s_sub_u32 s76, s77, 11\n"
    "v_mfma_f32_32x32x16_bf16 v[80:95], v[100:103], a[48:51], v[80:95]\n"
    "ds_read_b128 v[96:99], v252 offset:30720\n"
    "s_min_u32 s77, s77, s76\n"
    "s_lshl_b32 s76, s77, 16\n"
    "buffer_load_dwordx4 v15, s[44:47], s76 offen offset:0 lds\n"
    "s_waitcnt lgkmcnt(3)\n"
    "v_mfma_f32_32x32x16_bf16 v[64:79], v[104:107], a[52:55], v[64:79]\n"
    "ds_read_b128 v[100:103], v252 offset:31744\n"
    "v_mfma_f32_32x32x16_bf16 v[80:95], v[108:111], a[52:55], v[80:95]\n"
    "buffer_load_dwordx4 v16, s[44:47], s76 offen offset:1024 lds\n"
    "s_add_u32 m0, s87, 73728\n"
    "s_bfe_u32 s77, s11, 0x40004\n"
    "s_add_u32 s77, s77, s62\n"
    "s_sub_u32 s76, s77, 11\n"
    "s_min_u32 s77, s77, s76\n"
    "s_lshl_b32 s76, s77, 16\n"
    "buffer_load_dwordx4 v15, s[44:47], s76 offen offset:0 lds\n"
    "buffer_load_dwordx4 v16, s[44:47], s76 offen offset:1024 lds\n"
    "s_add_u32 m0, s87, 81920\n"
    "s_bfe_u32 s77, s11, 0x40008\n"
    "s_add_u32 s77, s77, s62\n"
    "s_sub_u32 s76, s77, 11\n"
    "s_min_u32 s77, s77, s76\n"
    "s_lshl_b32 s76, s77, 16\n"
    "buffer_load_dwordx4 v15, s[44:47], s76 offen offset:0 lds\n"
    "s_waitcnt vmcnt(10)\n"
    "s_barrier\n"
    "buffer_load_dwordx4 v16, s[44:47], s76 offen offset:1024 lds\n"
    "s_waitcnt vmcnt(10)\n"
    "v_xor_b32 v0, s64, v2\n"
    "v_bitop3_b32 v0, v0, v3, s64 bitop3:0xf6\n"
    "v_cmp_eq_u32 vcc, 0, v0\n"
    "s_nop 0\n"
    "s_cmp_eq_u64 vcc, -1\n"
    "s_cbranch_scc1 L0ok8_%=\n"
    "s_mov_b32 s80, 0x4000\n"
    "buffer_load_dwordx2 v[26:27], v29, s[56:59], s88 offen sc1\n"
    "L0spin9_%=:\n"
    "s_sleep 4\n"
    "buffer_load_dwordx2 v[2:3], v29, s[56:59], s88 offen sc1\n"
    "s_waitcnt vmcnt(1)\n"
    "v_xor_b32 v0, s64, v26\n"
    "v_bitop3_b32 v0, v0, v27, s64 bitop3:0xf6\n"
    "v_cmp_eq_u32 vcc, 0, v0\n"
    "s_nop 0\n"
    "s_cmp_eq_u64 vcc, -1\n"
    "s_cbranch_scc1 L0ok10_%=\n"
    "s_sleep 4\n"
    "buffer_load_dwordx2 v[26:27], v29, s[56:59], s88 offen sc1\n"
    "s_waitcnt vmcnt(1)\n"
    "v_xor_b32 v0, s64, v2\n"
    "v_bitop3_b32 v0, v0, v3, s64 bitop3:0xf6\n"
    "v_cmp_eq_u32 vcc, 0, v0\n"
    "s_nop 0\n"
    "s_cmp_eq_u64 vcc, -1\n"
    "s_cbranch_scc1 L0ok10_%=\n"
    "s_sub_u32 s80, s80, 1\n"
    "s_cmp_lg_u32 s80, 0\n"
    "s_cbranch_scc1 L0spin9_%=\n"
    "L0ok10_%=:\n"
    "L0ok8_%=:\n"
    "buffer_load_dwordx4 v[152:155], v22, s[56:59], s89 offen offset:0 sc1\n"
    "buffer_load_dwordx4 v[156:159], v22, s[56:59], s89 offen offset:1024 sc1\n"
    "buffer_load_dwordx4 v[160:163], v22, s[56:59], s89 offen offset:2048 sc1\n"
    "buffer_load_dwordx4 v[164:167], v22, s[56:59], s89 offen offset:3072 sc1\n"
    "ds_read_b128 v[104:107], v252 offset:32768\n"
    "s_waitcnt lgkmcnt(3)\n"
    "v_mfma_f32_32x32x16_bf16 v[64:79], v[112:115], a[56:59], v[64:79]\n"
    "ds_read_b128 v[108:111], v252 offset:33792\n"
    "v_mfma_f32_32x32x16_bf16 v[80:95], v[116:119], a[56:59], v[80:95]\n"
    "ds_read_b128 v[112:115], v252 offset:34816\n"
    "s_waitcnt lgkmcnt(3)\n"
    "v_mfma_f32_32x32x16_bf16 v[64:79], v[96:99], a[60:63], v[64:79]\n"
    "ds_read_b128 v[116:119], v252 offset:35840\n"
    "v_mfma_f32_32x32x16_bf16 v[80:95], v[100:103], a[60:63], v[80:95]\n"
    "ds_read_b128 v[96:99], v252 offset:36864\n"
    "s_waitcnt lgkmcnt(3)\n"
    "v_mfma_f32_32x32x16_bf16 v[64:79], v[104:107], a[64:67], v[64:79]\n"
    "ds_read_b128 v[100:103], v252 offset:37888\n"
    "v_mfma_f32_32x32x16_bf16 v[80:95], v[108:111], a[64:67], v[80:95]\n"
    "ds_read_b128 v[104:107], v252 offset:38912\n"
    "s_waitcnt lgkmcnt(3)\n"
    "v_mfma_f32_32x32x16_bf16 v[64:79], v[112:115], a[68:71], v[64:79]\n"
    "ds_read_b128 v[108:111], v252 offset:39936\n"
    "v_mfma_f32_32x32x16_bf16 v[80:95], v[116:119], a[68:71], v[80:95]\n"
    "ds_read_b128 v[112:115], v252 offset:40960\n"
    "s_waitcnt lgkmcnt(3)\n"
    "v_mfma_f32_32x32x16_bf16 v[64:79], v[96:99], a[72:75], v[64:79]\n"
    "ds_read_b128 v[116:119], v252 offset:41984\n"
    "v_mfma_f32_32x32x16_bf16 v[80:95], v[100:103], a[72:75], v[80:95]\n"
    "ds_read_b128 v[96:99], v252 offset:43008\n"
    "s_waitcnt lgkmcnt(3)\n"
    "v_mfma_f32_32x32x16_bf16 v[64:79], v[104:107], a[76:79], v[64:79]\n"
    "ds_read_b128 v[100:103], v252 offset:44032\n"
    "v_mfma_f32_32x32x16_bf16 v[80:95], v[108:111], a[76:79], v[80:95]\n"
    "ds_read_b128 v[104:107], v252 offset:45056\n"
    "s_waitcnt lgkmcnt(3)\n"
    "v_mfma_f32_32x32x16_bf16 v[64:79], v[112:115], a[80:83], v[64:79]\n"
    "ds_read_b128 v[108:111], v252 offset:46080\n"
    "v_mfma_f32_32x32x16_bf16 v[80:95], v[116:119], a[80:83], v[80:95]\n"
    "ds_read_b128 v[112:115], v252 offset:47104\n"
    "s_waitcnt lgkmcnt(3)\n"
    "v_mfma_f32_32x32x16_bf16 v[64:79], v[96:99], a[84:87], v[64:79]\n"
    "ds_read_b128 v[116:119], v252 offset:48128\n"
    "v_mfma_f32_32x32x16_bf16 v[80:95], v[100:103], a[84:87], v[80:95]\n"
    "s_waitcnt vmcnt(10)\n"
    "s_barrier\n"
    "ds_read_b128 v[96:99], v252 offset:49152\n"
    "s_waitcnt lgkmcnt(3)\n"
    "v_mfma_f32_32x32x16_bf16 v[64:79], v[104:107], a[88:91], v[64:79]\n"
    "ds_read_b128 v[100:103], v252 offset:50176\n"
    "v_mfma_f32_32x32x16_bf16 v[80:95], v[108:111], a[88:91], v[80:95]\n"
    "ds_read_b128 v[104:107], v252 offset:51200\n"
    "s_waitcnt lgkmcnt(3)\n"
    "v_mfma_f32_32x32x16_bf16 v[64:79], v[112:115], a[92:95], v[64:79]\n"
    "ds_read_b128 v[108:111], v252 offset:52224\n"
    "v_mfma_f32_32x32x16_bf16 v[80:95], v[116:119], a[92:95], v[80:95]\n"
    "ds_read_b128 v[112:115], v252 offset:53248\n"
    "s_waitcnt lgkmcnt(3)\n"
    "v_mfma_f32_32x32x16_bf16 v[64:79], v[96:99], a[96:99], v[64:79]\n"
    "ds_read_b128 v[116:119], v252 offset:54272\n"
    "v_mfma_f32_32x32x16_bf16 v[80:95], v[100:103], a[96:99], v[80:95]\n"
    "ds_read_b128 v[96:99], v252 offset:55296\n"
    "s_waitcnt lgkmcnt(3)\n"
    "v_mfma_f32_32x32x16_bf16 v[64:79], v[104:107], a[100:103], v[64:79]\n"
    "ds_read_b128 v[100:103], v252 offset:56320\n"
    "v_mfma_f32_32x32x16_bf16 v[80:95], v[108:111], a[100:103], v[80:95]\n"
    "ds_read_b128 v[104:107], v252 offset:57344\n"
    "s_waitcnt lgkmcnt(3)\n"
    "v_mfma_f32_32x32x16_bf16 v[64:79], v[112:115], a[104:107], v[64:79]\n"
    "ds_read_b128 v[108:111], v252 offset:58368\n"
    "v_mfma_f32_32x32x16_bf16 v[80:95], v[116:119], a[104:107], v[80:95]\n"
    "ds_read_b128 v[112:115], v252 offset:59392\n"
    "s_waitcnt lgkmcnt(3)\n"
    "v_mfma_f32_32x32x16_bf16 v[64:79], v[96:99], a[108:111], v[64:79]\n"
    "ds_read_b128 v[116:119], v252 offset:60416\n"
    "v_mfma_f32_32x32x16_bf16 v[80:95], v[100:103], a[108:111], v[80:95]\n"
    "ds_read_b128 v[96:99], v252 offset:61440\n"
    "s_waitcnt lgkmcnt(3)\n"
    "v_mfma_f32_32x32x16_bf16 v[64:79], v[104:107], a[112:115], v[64:79]\n"
    "ds_read_b128 v[100:103], v252 offset:62464\n"
    "v_mfma_f32_32x32x16_bf16 v[80:95], v[108:111], a[112:115], v[80:95]\n"
    "ds_read_b128 v[104:107], v252 offset:63488\n"
    "s_waitcnt lgkmcnt(3)\n"
    "v_mfma_f32_32x32x16_bf16 v[64:79], v[112:115], a[116:119], v[64:79]\n"
    "ds_read_b128 v[108:111], v252 offset:64512\n"
    "v_mfma_f32_32x32x16_bf16 v[80:95], v[116:119], a[116:119], v[80:95]\n"
    "s_waitcnt lgkmcnt(2)\n"
    "v_mfma_f32_32x32x16_bf16 v[64:79], v[96:99], a[120:123], v[64:79]\n"
    "v_mfma_f32_32x32x16_bf16 v[80:95], v[100:103], a[120:123], v[80:95]\n"
    "s_waitcnt lgkmcnt(0)\n"
    "v_mfma_f32_32x32x16_bf16 v[64:79], v[104:107], a[124:127], v[64:79]\n"
    "v_mfma_f32_32x32x16_bf16 v[80:95], v[108:111], a[124:127], v[80:95]\n"
    "s_mul_i32 s79, s94, 44\n"
    "s_add_u32 s79, s79, s92\n"
    "s_lshl_b32 s79, s79, 10\n"
    "s_add_u32 s97, s79, 0xb00000\n"
    "s_cmp_eq_u32 s62, 5\n"
    "s_cselect_b32 s85, 0x40000000, s97\n"
    "s_mov_b32 s79, 0xffff0000\n"
    "s_waitcnt vmcnt(3)\n"
    "v_lshlrev_b32 v168, 16, v152\n"
    "v_and_b32 v169, s79, v152\n"
    "v_lshlrev_b32 v170, 16, v153\n"
    "v_and_b32 v171, s79, v153\n"
    "v_lshlrev_b32 v172, 16, v154\n"
    "v_and_b32 v173, s79, v154\n"
    "v_lshlrev_b32 v174, 16, v155\n"
    "v_and_b32 v175, s79, v155\n"
    "v_pk_add_f32 v[168:169], v[168:169], v[64:65]\n"
    "v_pk_add_f32 v[170:171], v[170:171], v[66:67]\n"
    "v_pk_add_f32 v[172:173], v[172:173], v[68:69]\n"
    "v_pk_add_f32 v[174:175], v[174:175], v[70:71]\n"
    "v_pk_mul_f32 v[168:169], v[168:169], v[120:121]\n"
    "v_pk_mul_f32 v[170:171], v[170:171], v[122:123]\n"
    "v_pk_mul_f32 v[172:173], v[172:173], v[124:125]\n"
    "v_pk_mul_f32 v[174:175], v[174:175], v[126:127]\n"
    "v_cvt_pk_bf16_f32 v152, v168, v169\n"
    "v_cvt_pk_bf16_f32 v153, v170, v171\n"
    "v_cvt_pk_bf16_f32 v154, v172, v173\n"
    "v_cvt_pk_bf16_f32 v155, v174, v175\n"
    "buffer_store_dwordx4 v[152:155], v22, s[56:59], s97 offen offset:0 sc1\n"
    "s_waitcnt vmcnt(3)\n"
    "v_lshlrev_b32 v168, 16, v156\n"
    "v_and_b32 v169, s79, v156\n"
    "v_lshlrev_b32 v170, 16, v157\n"
    "v_and_b32 v171, s79, v157\n"
    "v_lshlrev_b32 v172, 16, v158\n"
    "v_and_b32 v173, s79, v158\n"
    "v_lshlrev_b32 v174, 16, v159\n"
    "v_and_b32 v175, s79, v159\n"
    "v_pk_add_f32 v[168:169], v[168:169], v[72:73]\n"
    "v_pk_add_f32 v[170:171], v[170:171], v[74:75]\n"
    "v_pk_add_f32 v[172:173], v[172:173], v[76:77]\n"
    "v_pk_add_f32 v[174:175], v[174:175], v[78:79]\n"
    "v_pk_mul_f32 v[168:169], v[168:169], v[128:129]\n"
    "v_pk_mul_f32 v[170:171], v[170:171], v[130:131]\n"
    "v_pk_mul_f32 v[172:173], v[172:173], v[132:133]\n"
    "v_pk_mul_f32 v[174:175], v[174:175], v[134:135]\n"
    "v_cvt_pk_bf16_f32 v156, v168, v169\n"
    "v_cvt_pk_bf16_f32 v157, v170, v171\n"
    "v_cvt_pk_bf16_f32 v158, v172, v173\n"
    "v_cvt_pk_bf16_f32 v159, v174, v175\n"
    "buffer_store_dwordx4 v[156:159], v22, s[56:59], s97 offen offset:1024 sc1\n"
    "s_waitcnt vmcnt(3)\n"
    "v_lshlrev_b32 v168, 16, v160\n"
    "v_and_b32 v169, s79, v160\n"
    "v_lshlrev_b32 v170, 16, v161\n"
    "v_and_b32 v171, s79, v161\n"
    "v_lshlrev_b32 v172, 16, v162\n"
    "v_and_b32 v173, s79, v162\n"
    "v_lshlrev_b32 v174, 16, v163\n"
    "v_and_b32 v175, s79, v163\n"
    "v_pk_add_f32 v[168:169], v[168:169], v[80:81]\n"
    "v_pk_add_f32 v[170:171], v[170:171], v[82:83]\n"
    "v_pk_add_f32 v[172:173], v[172:173], v[84:85]\n"
    "v_pk_add_f32 v[174:175], v[174:175], v[86:87]\n"
    "v_pk_mul_f32 v[168:169], v[168:169], v[136:137]\n"
    "v_pk_mul_f32 v[170:171], v[170:171], v[138:139]\n"
    "v_pk_mul_f32 v[172:173], v[172:173], v[140:141]\n"
    "v_pk_mul_f32 v[174:175], v[174:175], v[142:143]\n"
    "v_cvt_pk_bf16_f32 v160, v168, v169\n"
    "v_cvt_pk_bf16_f32 v161, v170, v171\n"
    "v_cvt_pk_bf16_f32 v162, v172, v173\n"
    "v_cvt_pk_bf16_f32 v163, v174, v175\n"
    "buffer_store_dwordx4 v[160:163], v22, s[56:59], s85 offen offset:2048 sc1\n"
    "s_waitcnt vmcnt(3)\n"
    "v_lshlrev_b32 v168, 16, v164\n"
    "v_and_b32 v169, s79, v164\n"
    "v_lshlrev_b32 v170, 16, v165\n"
    "v_and_b32 v171, s79, v165\n"
    "v_lshlrev_b32 v172, 16, v166\n"
    "v_and_b32 v173, s79, v166\n"
    "v_lshlrev_b32 v174, 16, v167\n"
    "v_and_b32 v175, s79, v167\n"
    "v_pk_add_f32 v[168:169], v[168:169], v[88:89]\n"
    "v_pk_add_f32 v[170:171], v[170:171], v[90:91]\n"
    "v_pk_add_f32 v[172:173], v[172:173], v[92:93]\n"
    "v_pk_add_f32 v[174:175], v[174:175], v[94:95]\n"
    "v_pk_mul_f32 v[168:169], v[168:169], v[144:145]\n"
    "v_pk_mul_f32 v[170:171], v[170:171], v[146:147]\n"
    "v_pk_mul_f32 v[172:173], v[172:173], v[148:149]\n"
    "v_pk_mul_f32 v[174:175], v[174:175], v[150:151]\n"
    "v_cvt_pk_bf16_f32 v164, v168, v169\n"
    "v_cvt_pk_bf16_f32 v165, v170, v171\n"
    "v_cvt_pk_bf16_f32 v166, v172, v173\n"
    "v_cvt_pk_bf16_f32 v167, v174, v175\n"
    "buffer_store_dwordx4 v[164:167], v22, s[56:59], s85 offen offset:3072 sc1\n"
    "v_lshrrev_b32 v5, 5, v4\n"
    "v_bfe_u32 v31, v4, 2, 2\n"
    "v_lshl_add_u32 v5, v5, 3, v31\n"
    "v_lshlrev_b32 v5, 7, v5\n"
    "v_bfe_u32 v31, v4, 4, 1\n"
    "v_lshl_add_u32 v5, v31, 5, v5\n"
    "v_and_b32 v31, 3, v4\n"
    "v_lshl_add_u32 v5, v31, 3, v5\n"
    "v_bfe_u32 v31, v4, 3, 1\n"
    "v_xor_b32 v31, s82, v31\n"
    "v_lshl_add_u32 v5, v31, 6, v5\n"
    "s_lshl_b32 s79, s82, 12\n"
    "v_add_u32 v5, s79, v5\n"
    "v_xor_b32 v6, 64, v5\n"
    "v_add_u32 v7, 49152, v5\n"
    "v_add_u32 v8, 49152, v6\n"
    "v_and_b32 v80, 31, v4\n"
    "v_and_b32 v81, 7, v4\n"
    "v_lshrrev_b32 v31, 5, v4\n"
    "v_xor_b32 v81, v81, v31\n"
    "v_lshlrev_b32 v81, 4, v81\n"
    "v_lshl_add_u32 v80, v80, 8, v81\n"
    "s_lshl1_add_u32 s78, s82, s65\n"
    "s_lshl_b32 s78, s78, 6\n"
    "s_add_u32 s78, s78, s82\n"
    "s_lshl_b32 s78, s78, 7\n"
    "s_add_u32 s78, s78, 98304\n"
    "v_add_u32 v80, s78, v80\n"
    "v_xor_b32 v81, 32, v80\n"
    "v_xor_b32 v82, 64, v80\n"
    "v_xor_b32 v83, 96, v80\n"
    "v_xor_b32 v84, 128, v80\n"
    "v_xor_b32 v85, 160, v80\n"
    "v_xor_b32 v86, 192, v80\n"
    "v_xor_b32 v87, 224, v80\n"
    "v_lshrrev_b32 v31, 4, v4\n"
    "v_and_b32 v88, 15, v4\n"
    "v_xor_b32 v88, v88, v31\n"
    "v_lshlrev_b32 v88, 4, v88\n"
    "v_lshl_add_u32 v88, v31, 8, v88\n"
    "s_lshl_b32 s78, s60, 12\n"
    "s_add_u32 s78, s78, 98304\n"
    "v_add_u32 v88, s78, v88\n"
    "v_xor_b32 v89, 64, v88\n"
    "s_lshl_b32 s78, s60, 8\n"
    "s_add_u32 s93, s78, 90112\n"
    "v_lshl_add_u32 v90, v253, 2, s93\n"
    "s_lshl4_add_u32 s78, s82, s93\n"
    "v_lshl_add_u32 v91, v31, 2, s78\n"
    "v_lshlrev_b32 v31, 2, v4\n"
    "s_lshl_b32 s78, s66, 7\n"
    "s_add_u32 s83, s78, 0x30000\n"
    "s_lshl1_add_u32 s78, s62, s82\n"
    "s_lshl4_add_u32 s78, s66, s78\n"
    "s_lshl_b32 s78, s78, 11\n"
    "s_add_u32 s97, s78, 0xe80000\n"
    "s_mov_b32 s79, 0xffff0000\n"
    "v_lshlrev_b32 v204, 16, v184\n"
    "v_and_b32 v205, s79, v184\n"
    "v_lshlrev_b32 v206, 16, v185\n"
    "v_and_b32 v207, s79, v185\n"
    "s_waitcnt vmcnt(0)\n"
    "s_barrier\n"
    "s_add_u32 s80, s14, 0x11000\n"
    "buffer_store_dwordx2 v[24:25], v23, s[56:59], s80 offen sc1\n"
    "v_lshlrev_b32 v188, 16, v176\n"
    "v_and_b32 v189, s79, v176\n"
    "v_lshlrev_b32 v190, 16, v177\n"
    "v_and_b32 v191, s79, v177\n"
    "v_mul_f32 v188, v188, v204\n"
    "v_mul_f32 v189, v189, v205\n"
    "v_mul_f32 v190, v190, v206\n"
    "v_mul_f32 v191, v191, v207\n"
    "v_lshlrev_b32 v192, 16, v178\n"
    "v_and_b32 v193, s79, v178\n"
    "v_lshlrev_b32 v194, 16, v179\n"
    "v_and_b32 v195, s79, v179\n"
    "v_mul_f32 v192, v192, v204\n"
    "v_mul_f32 v193, v193, v205\n"
    "v_mul_f32 v194, v194, v206\n"
    "v_mul_f32 v195, v195, v207\n"
    "v_lshlrev_b32 v196, 16, v180\n"
    "v_and_b32 v197, s79, v180\n"
    "v_lshlrev_b32 v198, 16, v181\n"
    "v_and_b32 v199, s79, v181\n"
    "v_mul_f32 v196, v196, v204\n"
    "v_mul_f32 v197, v197, v205\n"
    "v_mul_f32 v198, v198, v206\n"
    "v_mul_f32 v199, v199, v207\n"
    "v_lshlrev_b32 v200, 16, v182\n"
    "v_and_b32 v201, s79, v182\n"
    "v_lshlrev_b32 v202, 16, v183\n"
    "v_and_b32 v203, s79, v183\n"
    "v_mul_f32 v200, v200, v204\n"
    "v_mul_f32 v201, v201, v205\n"
    "v_mul_f32 v202, v202, v206\n"
    "v_mul_f32 v203, v203, v207\n"
    "buffer_load_dwordx2 v[2:3], v13, s[56:59], 0 offen sc1\n"
    "s_barrier\n"
    "s_waitcnt vmcnt(0)\n"
    "v_xor_b32 v0, s64, v2\n"
    "v_bitop3_b32 v0, v0, v3, s64 bitop3:0xf6\n"
    "v_cmp_eq_u32 vcc, 0, v0\n"
    "s_nop 0\n"
    "s_cmp_eq_u64 vcc, -1\n"
    "s_cbranch_scc1 L0ok11_%=\n"
    "s_mov_b32 s80, 0x4000\n"
    "buffer_load_dwordx2 v[26:27], v13, s[56:59], 0 offen sc1\n"
    "L0spin12_%=:\n"
    "s_sleep 4\n"
    "buffer_load_dwordx2 v[2:3], v13, s[56:59], 0 offen sc1\n"
    "s_waitcnt vmcnt(1)\n"
    "v_xor_b32 v0, s64, v26\n"
    "v_bitop3_b32 v0, v0, v27, s64 bitop3:0xf6\n"
    "v_cmp_eq_u32 vcc, 0, v0\n"
    "s_nop 0\n"
    "s_cmp_eq_u64 vcc, -1\n"
    "s_cbranch_scc1 L0ok13_%=\n"
    "s_sleep 4\n"
    "buffer_load_dwordx2 v[26:27], v13, s[56:59], 0 offen sc1\n"
    "s_waitcnt vmcnt(1)\n"
    "v_xor_b32 v0, s64, v2\n"
    "v_bitop3_b32 v0, v0, v3, s64 bitop3:0xf6\n"
    "v_cmp_eq_u32 vcc, 0, v0\n"
    "s_nop 0\n"
    "s_cmp_eq_u64 vcc, -1\n"
    "s_cbranch_scc1 L0ok13_%=\n"
    "s_sub_u32 s80, s80, 1\n"
    "s_cmp_lg_u32 s80, 0\n"
    "s_cbranch_scc1 L0spin12_%=\n"
    "L0ok13_%=:\n"
    "L0ok11_%=:\n"
    "s_bfe_u32 s77, s10, 0x40000\n"
    "s_add_u32 s77, s77, s62\n"
    "s_sub_u32 s76, s77, 11\n"
    "s_min_u32 s77, s77, s76\n"
    "s_lshl_b32 s76, s77, 12\n"
    "buffer_load_dwordx4 a[0:3], v28, s[56:59], s76 offen offset:0 sc1\n"
    "buffer_load_dwordx4 a[4:7], v28, s[56:59], s76 offen offset:1024 sc1\n"
    "s_bfe_u32 s77, s10, 0x40004\n"
    "s_add_u32 s77, s77, s62\n"
    "s_sub_u32 s76, s77, 11\n"
    "s_min_u32 s77, s77, s76\n"
    "s_lshl_b32 s76, s77, 12\n"
    "buffer_load_dwordx4 a[8:11], v28, s[56:59], s76 offen offset:0 sc1\n"
    "buffer_load_dwordx4 a[12:15], v28, s[56:59], s76 offen offset:1024 sc1\n"
    "s_bfe_u32 s77, s10, 0x40008\n"
    "s_add_u32 s77, s77, s62\n"
    "s_sub_u32 s76, s77, 11\n"
    "s_min_u32 s77, s77, s76\n"
    "s_lshl_b32 s76, s77, 12\n"
    "buffer_load_dwordx4 a[16:19], v28, s[56:59], s76 offen offset:0 sc1\n"
    "buffer_load_dwordx4 a[20:23], v28, s[56:59], s76 offen offset:1024 sc1\n"
    "s_bfe_u32 s77, s10, 0x4000c\n"
    "s_add_u32 s77, s77, s62\n"
    "s_sub_u32 s76, s77, 11\n"
    "s_min_u32 s77, s77, s76\n"
    "s_lshl_b32 s76, s77, 12\n"
    "buffer_load_dwordx4 a[24:27], v28, s[56:59], s76 offen offset:0 sc1\n"
    "buffer_load_dwordx4 a[28:31], v28, s[56:59], s76 offen offset:1024 sc1\n"
    "s_bfe_u32 s77, s10, 0x40010\n"
    "s_add_u32 s77, s77, s62\n"
    "s_sub_u32 s76, s77, 11\n"
    "s_min_u32 s77, s77, s76\n"
    "s_lshl_b32 s76, s77, 12\n"
    "buffer_load_dwordx4 a[32:35], v28, s[56:59], s76 offen offset:0 sc1\n"
    "buffer_load_dwordx4 a[36:39], v28, s[56:59], s76 offen offset:1024 sc1\n"
    "s_cmp_eq_u32 %1, 0\n"
    "s_cselect_b32 s78, s96, 0x40000000\n"
    "buffer_store_dword v25, v23, s[56:59], s78 offen sc1\n"
    "ds_read_b64_tr_b16 v[32:33], v5 offset:0\n"
    "ds_read_b64_tr_b16 v[34:35], v5 offset:512\n"
    "ds_read_b64_tr_b16 v[36:37], v6 offset:0\n"
    "ds_read_b64_tr_b16 v[38:39], v6 offset:512\n"
    "ds_read_b64_tr_b16 v[40:41], v5 offset:2048\n"
    "ds_read_b64_tr_b16 v[42:43], v5 offset:2560\n"
    "ds_read_b64_tr_b16 v[44:45], v6 offset:2048\n"
    "ds_read_b64_tr_b16 v[46:47], v6 offset:2560\n"
    "ds_read_b64_tr_b16 v[48:49], v5 offset:8192\n"
    "ds_read_b64_tr_b16 v[50:51], v5 offset:8704\n"
    "s_waitcnt lgkmcnt(6)\n"
    "s_bfe_u32 s77, s10, 0x40014\n"
    "s_add_u32 s77, s77, s62\n"
    "s_sub_u32 s76, s77, 11\n"
    "s_min_u32 s77, s77, s76\n"
    "s_lshl_b32 s76, s77, 12\n"
    "s_waitcnt vmcnt(10)\n"
    "v_mfma_f32_32x32x16_bf16 v[96:111], v[32:35], a[0:3], 0\n"
    "ds_read_b64_tr_b16 v[52:53], v6 offset:8192\n"
    "ds_read_b64_tr_b16 v[54:55], v6 offset:8704\n"
    "v_mfma_f32_32x32x16_bf16 v[112:127], v[36:39], a[0:3], 0\n"
    "ds_read_b64_tr_b16 v[32:33], v5 offset:10240\n"
    "ds_read_b64_tr_b16 v[34:35], v5 offset:10752\n"
    "s_waitcnt lgkmcnt(6)\n"
    "s_waitcnt vmcnt(9)\n"
    "v_mfma_f32_32x32x16_bf16 v[96:111], v[40:43], a[4:7], v[96:111]\n"
    "ds_read_b64_tr_b16 v[36:37], v6 offset:10240\n"
    "ds_read_b64_tr_b16 v[38:39], v6 offset:10752\n"
    "buffer_load_dwordx4 a[40:43], v28, s[56:59], s76 offen offset:0 sc1\n"
    "v_mfma_f32_32x32x16_bf16 v[112:127], v[44:47], a[4:7], v[112:127]\n"
    "ds_read_b64_tr_b16 v[40:41], v5 offset:16384\n"
    "ds_read_b64_tr_b16 v[42:43], v5 offset:16896\n"
    "buffer_load_dwordx4 a[44:47], v28, s[56:59], s76 offen offset:1024 sc1\n"
    "buffer_load_dwordx2 v[2:3], v14, s[56:59], 0 offen sc1\n"
    "s_waitcnt lgkmcnt(6)\n"
    "s_waitcnt vmcnt(11)\n"
    "v_mfma_f32_32x32x16_bf16 v[96:111], v[48:51], a[8:11], v[96:111]\n"
    "ds_read_b64_tr_b16 v[44:45], v6 offset:16384\n"
    "ds_read_b64_tr_b16 v[46:47], v6 offset:16896\n"
    "v_mfma_f32_32x32x16_bf16 v[112:127], v[52:55], a[8:11], v[112:127]\n"
    "ds_read_b64_tr_b16 v[48:49], v5 offset:18432\n"
    "ds_read_b64_tr_b16 v[50:51], v5 offset:18944\n"
    "s_waitcnt lgkmcnt(6)\n"
    "s_waitcnt vmcnt(10)\n"
    "v_mfma_f32_32x32x16_bf16 v[96:111], v[32:35], a[12:15], v[96:111]\n"
    "ds_read_b64_tr_b16 v[52:53], v6 offset:18432\n"
    "ds_read_b64_tr_b16 v[54:55], v6 offset:18944\n"
    "s_bfe_u32 s77, s10, 0x40018\n"
    "s_add_u32 s77, s77, s62\n"
    "s_sub_u32 s76, s77, 11\n"
    "v_mfma_f32_32x32x16_bf16 v[112:127], v[36:39], a[12:15], v[112:127]\n"
    "ds_read_b64_tr_b16 v[32:33], v5 offset:24576\n"
    "ds_read_b64_tr_b16 v[34:35], v5 offset:25088\n"
    "s_min_u32 s77, s77, s76\n"
    "s_lshl_b32 s76, s77, 12\n"
    "buffer_load_dwordx4 a[48:51], v28, s[56:59], s76 offen offset:0 sc1\n"
    "buffer_load_dwordx4 a[52:55], v28, s[56:59], s76 offen offset:1024 sc1\n"
    "s_waitcnt lgkmcnt(6)\n"
    "s_waitcnt vmcnt(11)\n"
    "v_mfma_f32_32x32x16_bf16 v[96:111], v[40:43], a[16:19], v[96:111]\n"
    "ds_read_b64_tr_b16 v[36:37], v6 offset:24576\n"
    "ds_read_b64_tr_b16 v[38:39], v6 offset:25088\n"
    "v_mfma_f32_32x32x16_bf16 v[112:127], v[44:47], a[16:19], v[112:127]\n"
    "ds_read_b64_tr_b16 v[40:41], v5 offset:26624\n"
    "ds_read_b64_tr_b16 v[42:43], v5 offset:27136\n"
    "s_waitcnt lgkmcnt(6)\n"
    "s_waitcnt vmcnt(10)\n"
    "v_mfma_f32_32x32x16_bf16 v[96:111], v[48:51], a[20:23], v[96:111]\n"
    "ds_read_b64_tr_b16 v[44:45], v6 offset:26624\n"
    "ds_read_b64_tr_b16 v[46:47], v6 offset:27136\n"
    "s_bfe_u32 s77, s10, 0x4001c\n"
    "s_add_u32 s77, s77, s62\n"
    "s_sub_u32 s76, s77, 11\n"
    "v_mfma_f32_32x32x16_bf16 v[112:127], v[52:55], a[20:23], v[112:127]\n"
    "ds_read_b64_tr_b16 v[48:49], v5 offset:32768\n"
    "ds_read_b64_tr_b16 v[50:51], v5 offset:33280\n"
    "s_min_u32 s77, s77, s76\n"
    "s_lshl_b32 s76, s77, 12\n"
    "buffer_load_dwordx4 a[56:59], v28, s[56:59], s76 offen offset:0 sc1\n"
    "buffer_load_dwordx4 a[60:63], v28, s[56:59], s76 offen offset:1024 sc1\n"
    "s_waitcnt lgkmcnt(6)\n"
    "s_waitcnt vmcnt(11)\n"
    "v_mfma_f32_32x32x16_bf16 v[96:111], v[32:35], a[24:27], v[96:111]\n"
    "ds_read_b64_tr_b16 v[52:53], v6 offset:32768\n"
    "ds_read_b64_tr_b16 v[54:55], v6 offset:33280\n"
    "v_mfma_f32_32x32x16_bf16 v[112:127], v[36:39], a[24:27], v[112:127]\n"
    "ds_read_b64_tr_b16 v[32:33], v5 offset:34816\n"
    "ds_read_b64_tr_b16 v[34:35], v5 offset:35328\n"
    "s_waitcnt lgkmcnt(6)\n"
    "s_waitcnt vmcnt(10)\n"
    "v_mfma_f32_32x32x16_bf16 v[96:111], v[40:43], a[28:31], v[96:111]\n"
    "ds_read_b64_tr_b16 v[36:37], v6 offset:34816\n"
    "ds_read_b64_tr_b16 v[38:39], v6 offset:35328\n"
    "s_bfe_u32 s77, s11, 0x40000\n"
    "s_add_u32 s77, s77, s62\n"
    "s_sub_u32 s76, s77, 11\n"
    "v_mfma_f32_32x32x16_bf16 v[112:127], v[44:47], a[28:31], v[112:127]\n"
    "ds_read_b64_tr_b16 v[40:41], v5 offset:40960\n"
    "ds_read_b64_tr_b16 v[42:43], v5 offset:41472\n"
    "s_min_u32 s77, s77, s76\n"
    "s_lshl_b32 s76, s77, 12\n"
    "buffer_load_dwordx4 a[64:67], v28, s[56:59], s76 offen offset:0 sc1\n"
    "buffer_load_dwordx4 a[68:71], v28, s[56:59], s76 offen offset:1024 sc1\n"
    "s_bfe_u32 s77, s11, 0x40004\n"
    "s_add_u32 s77, s77, s62\n"
    "s_sub_u32 s76, s77, 11\n"
    "s_min_u32 s77, s77, s76\n"
    "s_lshl_b32 s76, s77, 12\n"
    "buffer_load_dwordx4 a[72:75], v28, s[56:59], s76 offen offset:0 sc1\n"
    "buffer_load_dwordx4 a[76:79], v28, s[56:59], s76 offen offset:1024 sc1\n"
    "s_bfe_u32 s77, s11, 0x40008\n"
    "s_add_u32 s77, s77, s62\n"
    "s_sub_u32 s76, s77, 11\n"
    "s_min_u32 s77, s77, s76\n"
    "s_lshl_b32 s76, s77, 12\n"
    "buffer_load_dwordx4 a[80:83], v28, s[56:59], s76 offen offset:0 sc1\n"
    "buffer_load_dwordx4 a[84:87], v28, s[56:59], s76 offen offset:1024 sc1\n"
    "s_waitcnt vmcnt(10)\n"
    "v_xor_b32 v0, s64, v2\n"
    "v_bitop3_b32 v0, v0, v3, s64 bitop3:0xf6\n"
    "v_cmp_eq_u32 vcc, 0, v0\n"
    "s_nop 0\n"
    "s_cmp_eq_u64 vcc, -1\n"
    "s_cbranch_scc1 L0ok14_%=\n"
    "s_mov_b32 s80, 0x4000\n"
    "buffer_load_dwordx2 v[26:27], v14, s[56:59], 0 offen sc1\n"
    "L0spin15_%=:\n"
    "s_sleep 4\n"
    "buffer_load_dwordx2 v[2:3], v14, s[56:59], 0 offen sc1\n"
    "s_waitcnt vmcnt(1)\n"
    "v_xor_b32 v0, s64, v26\n"
    "v_bitop3_b32 v0, v0, v27, s64 bitop3:0xf6\n"
    "v_cmp_eq_u32 vcc, 0, v0\n"
    "s_nop 0\n"
    "s_cmp_eq_u64 vcc, -1\n"
    "s_cbranch_scc1 L0ok16_%=\n"
    "s_sleep 4\n"
    "buffer_load_dwordx2 v[26:27], v14, s[56:59], 0 offen sc1\n"
    "s_waitcnt vmcnt(1)\n"
    "v_xor_b32 v0, s64, v2\n"
    "v_bitop3_b32 v0, v0, v3, s64 bitop3:0xf6\n"
    "v_cmp_eq_u32 vcc, 0, v0\n"
    "s_nop 0\n"
    "s_cmp_eq_u64 vcc, -1\n"
    "s_cbranch_scc1 L0ok16_%=\n"
    "s_sub_u32 s80, s80, 1\n"
    "s_cmp_lg_u32 s80, 0\n"
    "s_cbranch_scc1 L0spin15_%=\n"
    "L0ok16_%=:\n"
    "L0ok14_%=:\n"
    "s_mov_b32 m0, s93\n"
    "buffer_load_dwordx4 v[56:59], v22, s[56:59], s97 offen sc1\n"
    "buffer_load_dwordx4 v[60:63], v22, s[56:59], s97 offen offset:1024 sc1\n"
    "buffer_load_dword v31, s[56:59], s83 offen sc1 lds\n"
    "s_waitcnt lgkmcnt(6)\n"
    "v_mfma_f32_32x32x16_bf16 v[96:111], v[48:51], a[32:35], v[96:111]\n"
    "ds_read_b64_tr_b16 v[44:45], v6 offset:40960\n"
    "ds_read_b64_tr_b16 v[46:47], v6 offset:41472\n"
    "v_mfma_f32_32x32x16_bf16 v[112:127], v[52:55], a[32:35], v[112:127]\n"
    "ds_read_b64_tr_b16 v[48:49], v5 offset:43008\n"
    "ds_read_b64_tr_b16 v[50:51], v5 offset:43520\n"
    "s_waitcnt lgkmcnt(6)\n"
    "v_mfma_f32_32x32x16_bf16 v[96:111], v[32:35], a[36:39], v[96:111]\n"
    "ds_read_b64_tr_b16 v[52:53], v6 offset:43008\n"
    "ds_read_b64_tr_b16 v[54:55], v6 offset:43520\n"
    "v_mfma_f32_32x32x16_bf16 v[112:127], v[36:39], a[36:39], v[112:127]\n"
    "ds_read_b64_tr_b16 v[32:33], v7 offset:0\n"
    "ds_read_b64_tr_b16 v[34:35], v7 offset:512\n"
    "s_waitcnt lgkmcnt(6)\n"
    "v_mfma_f32_32x32x16_bf16 v[96:111], v[40:43], a[40:43], v[96:111]\n"
    "ds_read_b64_tr_b16 v[36:37], v8 offset:0\n"
    "ds_read_b64_tr_b16 v[38:39], v8 offset:512\n"
    "v_mfma_f32_32x32x16_bf16 v[112:127], v[44:47], a[40:43], v[112:127]\n"
    "ds_read_b64_tr_b16 v[40:41], v7 offset:2048\n"
    "ds_read_b64_tr_b16 v[42:43], v7 offset:2560\n"
    "s_waitcnt lgkmcnt(6)\n"
    "v_mfma_f32_32x32x16_bf16 v[96:111], v[48:51], a[44:47], v[96:111]\n"
    "ds_read_b64_tr_b16 v[44:45], v8 offset:2048\n"
    "ds_read_b64_tr_b16 v[46:47], v8 offset:2560\n"
    "v_mfma_f32_32x32x16_bf16 v[112:127], v[52:55], a[44:47], v[112:127]\n"
    "ds_read_b64_tr_b16 v[48:49], v7 offset:8192\n"
    "ds_read_b64_tr_b16 v[50:51], v7 offset:8704\n"
    "s_waitcnt lgkmcnt(6)\n"
    "s_waitcnt vmcnt(12)\n"
    "v_mfma_f32_32x32x16_bf16 v[96:111], v[32:35], a[48:51], v[96:111]\n"
    "ds_read_b64_tr_b16 v[52:53], v8 offset:8192\n"
    "ds_read_b64_tr_b16 v[54:55], v8 offset:8704\n"
    "v_mfma_f32_32x32x16_bf16 v[112:127], v[36:39], a[48:51], v[112:127]\n"
    "ds_read_b64_tr_b16 v[32:33], v7 offset:10240\n"
    "ds_read_b64_tr_b16 v[34:35], v7 offset:10752\n"
    "s_waitcnt lgkmcnt(6)\n"
    "s_waitcnt vmcnt(11)\n"
    "v_mfma_f32_32x32x16_bf16 v[96:111], v[40:43], a[52:55], v[96:111]\n"
    "ds_read_b64_tr_b16 v[36:37], v8 offset:10240\n"
    "ds_read_b64_tr_b16 v[38:39], v8 offset:10752\n"
    "v_mfma_f32_32x32x16_bf16 v[112:127], v[44:47], a[52:55], v[112:127]\n"
    "ds_read_b64_tr_b16 v[40:41], v7 offset:16384\n"
    "ds_read_b64_tr_b16 v[42:43], v7 offset:16896\n"
    "s_waitcnt lgkmcnt(6)\n"
    "s_waitcnt vmcnt(10)\n"
    "v_mfma_f32_32x32x16_bf16 v[96:111], v[48:51], a[56:59], v[96:111]\n"
    "ds_read_b64_tr_b16 v[44:45], v8 offset:16384\n"
    "ds_read_b64_tr_b16 v[46:47], v8 offset:16896\n"
    "v_mfma_f32_32x32x16_bf16 v[112:127], v[52:55], a[56:59], v[112:127]\n"
    "ds_read_b64_tr_b16 v[48:49], v7 offset:18432\n"
    "ds_read_b64_tr_b16 v[50:51], v7 offset:18944\n"
    "s_waitcnt lgkmcnt(6)\n"
    "s_waitcnt vmcnt(9)\n"
    "v_mfma_f32_32x32x16_bf16 v[96:111], v[32:35], a[60:63], v[96:111]\n"
    "ds_read_b64_tr_b16 v[52:53], v8 offset:18432\n"
    "ds_read_b64_tr_b16 v[54:55], v8 offset:18944\n"
    "v_mfma_f32_32x32x16_bf16 v[112:127], v[36:39], a[60:63], v[112:127]\n"
    "ds_read_b64_tr_b16 v[32:33], v7 offset:24576\n"
    "ds_read_b64_tr_b16 v[34:35], v7 offset:25088\n"
    "s_waitcnt lgkmcnt(6)\n"
    "s_waitcnt vmcnt(8)\n"
    "v_mfma_f32_32x32x16_bf16 v[96:111], v[40:43], a[64:67], v[96:111]\n"
    "ds_read_b64_tr_b16 v[36:37], v8 offset:24576\n"
    "ds_read_b64_tr_b16 v[38:39], v8 offset:25088\n"
    "v_mfma_f32_32x32x16_bf16 v[112:127], v[44:47], a[64:67], v[112:127]\n"
    "ds_read_b64_tr_b16 v[40:41], v7 offset:26624\n"
    "ds_read_b64_tr_b16 v[42:43], v7 offset:27136\n"
    "s_waitcnt vmcnt(0)\n"
    "s_waitcnt lgkmcnt(6)\n"
    "v_mfma_f32_32x32x16_bf16 v[96:111], v[48:51], a[68:71], v[96:111]\n"
    "ds_read_b64_tr_b16 v[44:45], v8 offset:26624\n"
    "ds_read_b64_tr_b16 v[46:47], v8 offset:27136\n"
    "ds_read_b32 v170, v90\n"
    "ds_read_b32 v172, v91 offset:0\n"
    "ds_read_b32 v173, v91 offset:32\n"
    "v_mfma_f32_32x32x16_bf16 v[112:127], v[52:55], a[68:71], v[112:127]\n"
    "ds_read_b64_tr_b16 v[48:49], v7 offset:32768\n"
    "ds_read_b64_tr_b16 v[50:51], v7 offset:33280\n"
    "ds_read_b32 v174, v91 offset:64\n"
    "ds_read_b32 v175, v91 offset:96\n"
    "v_lshlrev_b32 v64, 16, v56\n"
    "s_waitcnt lgkmcnt(11)\n"
    "v_mfma_f32_32x32x16_bf16 v[96:111], v[32:35], a[72:75], v[96:111]\n"
    "ds_read_b64_tr_b16 v[52:53], v8 offset:32768\n"
    "ds_read_b64_tr_b16 v[54:55], v8 offset:33280\n"
    "v_and_b32 v65, s79, v56\n"
    "v_lshlrev_b32 v66, 16, v57\n"
    "v_and_b32 v67, s79, v57\n"
    "v_mfma_f32_32x32x16_bf16 v[112:127], v[36:39], a[72:75], v[112:127]\n"
    "ds_read_b64_tr_b16 v[32:33], v7 offset:34816\n"
    "ds_read_b64_tr_b16 v[34:35], v7 offset:35328\n"
    "v_lshlrev_b32 v68, 16, v58\n"
    "v_and_b32 v69, s79, v58\n"
    "v_lshlrev_b32 v70, 16, v59\n"
    "s_waitcnt lgkmcnt(11)\n"
    "v_mfma_f32_32x32x16_bf16 v[96:111], v[40:43], a[76:79], v[96:111]\n"
    "ds_read_b64_tr_b16 v[36:37], v8 offset:34816\n"
    "ds_read_b64_tr_b16 v[38:39], v8 offset:35328\n"
    "v_and_b32 v71, s79, v59\n"
    "v_lshlrev_b32 v72, 16, v60\n"
    "v_and_b32 v73, s79, v60\n"
    "v_mfma_f32_32x32x16_bf16 v[112:127], v[44:47], a[76:79], v[112:127]\n"
    "v_lshlrev_b32 v74, 16, v61\n"
    "v_and_b32 v75, s79, v61\n"
    "v_lshlrev_b32 v76, 16, v62\n"
    "v_and_b32 v77, s79, v62\n"
    "v_lshlrev_b32 v78, 16, v63\n"
    "s_waitcnt lgkmcnt(4)\n"
    "v_mfma_f32_32x32x16_bf16 v[96:111], v[48:51], a[80:83], v[96:111]\n"
    "v_and_b32 v79, s79, v63\n"
    "v_sub_f32 v169, 1.0, v170\n"
    "v_mov_b32 v168, v170\n"
    "v_mul_f32 v64, v170, v64\n"
    "v_mul_f32 v65, v170, v65\n"
    "v_mfma_f32_32x32x16_bf16 v[112:127], v[52:55], a[80:83], v[112:127]\n"
    "v_mul_f32 v66, v170, v66\n"
    "v_mul_f32 v67, v170, v67\n"
    "v_mul_f32 v68, v170, v68\n"
    "v_mul_f32 v69, v170, v69\n"
    "v_mul_f32 v70, v170, v70\n"
    "s_waitcnt lgkmcnt(0)\n"
    "v_mfma_f32_32x32x16_bf16 v[96:111], v[32:35], a[84:87], v[96:111]\n"
    "v_mul_f32 v71, v170, v71\n"
    "v_mul_f32 v72, v170, v72\n"
    "v_mul_f32 v73, v170, v73\n"
    "v_mul_f32 v74, v170, v74\n"
    "v_mul_f32 v75, v170, v75\n"
    "v_mfma_f32_32x32x16_bf16 v[112:127], v[36:39], a[84:87], v[112:127]\n"
    "v_mul_f32 v76, v170, v76\n"
    "v_mul_f32 v77, v170, v77\n"
    "v_mul_f32 v78, v170, v78\n"
    "v_mul_f32 v79, v170, v79\n"
    "s_nop 1\n"
    "v_pk_fma_f32 v[96:97], v[96:97], v[168:169], v[64:65] op_sel:[0,1,0] op_sel_hi:[1,1,1]\n"
    "v_pk_fma_f32 v[98:99], v[98:99], v[168:169], v[66:67] op_sel:[0,1,0] op_sel_hi:[1,1,1]\n"
    "ds_write_b128 v80, v[96:99]\n"
    "v_pk_fma_f32 v[100:101], v[100:101], v[168:169], v[68:69] op_sel:[0,1,0] op_sel_hi:[1,1,1]\n"
    "v_pk_fma_f32 v[102:103], v[102:103], v[168:169], v[70:71] op_sel:[0,1,0] op_sel_hi:[1,1,1]\n"
    "ds_write_b128 v81, v[100:103]\n"
    "v_pk_fma_f32 v[104:105], v[104:105], v[168:169], v[72:73] op_sel:[0,1,0] op_sel_hi:[1,1,1]\n"
    "v_pk_fma_f32 v[106:107], v[106:107], v[168:169], v[74:75] op_sel:[0,1,0] op_sel_hi:[1,1,1]\n"
    "ds_write_b128 v82, v[104:107]\n"
    "v_pk_fma_f32 v[108:109], v[108:109], v[168:169], v[76:77] op_sel:[0,1,0] op_sel_hi:[1,1,1]\n"
    "v_pk_fma_f32 v[110:111], v[110:111], v[168:169], v[78:79] op_sel:[0,1,0] op_sel_hi:[1,1,1]\n"
    "ds_write_b128 v83, v[108:111]\n"
    "v_pk_mul_f32 v[112:113], v[112:113], v[168:169] op_sel:[0,1] op_sel_hi:[1,1]\n"
    "v_pk_mul_f32 v[114:115], v[114:115], v[168:169] op_sel:[0,1] op_sel_hi:[1,1]\n"
    "ds_write_b128 v84, v[112:115]\n"
    "v_pk_mul_f32 v[116:117], v[116:117], v[168:169] op_sel:[0,1] op_sel_hi:[1,1]\n"
    "v_pk_mul_f32 v[118:119], v[118:119], v[168:169] op_sel:[0,1] op_sel_hi:[1,1]\n"
    "ds_write_b128 v85, v[116:119]\n"
    "v_pk_mul_f32 v[120:121], v[120:121], v[168:169] op_sel:[0,1] op_sel_hi:[1,1]\n"
    "v_pk_mul_f32 v[122:123], v[122:123], v[168:169] op_sel:[0,1] op_sel_hi:[1,1]\n"
    "ds_write_b128 v86, v[120:123]\n"
    "v_pk_mul_f32 v[124:125], v[124:125], v[168:169] op_sel:[0,1] op_sel_hi:[1,1]\n"
    "v_pk_mul_f32 v[126:127], v[126:127], v[168:169] op_sel:[0,1] op_sel_hi:[1,1]\n"
    "ds_write_b128 v87, v[124:127]\n"
    "s_waitcnt lgkmcnt(0)\n"
    "s_barrier\n"
    "ds_read_b128 v[208:211], v88 offset:0\n"
    "ds_read_b128 v[212:215], v88 offset:16384\n"
    "ds_read_b128 v[216:219], v89 offset:1024\n"
    "ds_read_b128 v[220:223], v89 offset:17408\n"
    "ds_read_b128 v[224:227], v88 offset:2048\n"
    "ds_read_b128 v[228:231], v88 offset:18432\n"
    "ds_read_b128 v[232:235], v89 offset:3072\n"
    "ds_read_b128 v[236:239], v89 offset:19456\n"
    "s_waitcnt lgkmcnt(7)\n"
    "v_fma_f32 v188, v188, v172, v208\n"
    "v_fma_f32 v189, v189, v172, v209\n"
    "v_fma_f32 v190, v190, v172, v210\n"
    "v_fma_f32 v191, v191, v172, v211\n"
    "s_waitcnt lgkmcnt(6)\n"
    "v_pk_add_f32 v[188:189], v[188:189], v[212:213]\n"
    "v_pk_add_f32 v[190:191], v[190:191], v[214:215]\n"
    "s_mov_b32 s76, 0\n"
    "buffer_store_dwordx4 v[188:191], v9, s[52:55], s76 offen\n"
    "s_waitcnt lgkmcnt(5)\n"
    "v_fma_f32 v192, v192, v173, v216\n"
    "v_fma_f32 v193, v193, v173, v217\n"
    "v_fma_f32 v194, v194, v173, v218\n"
    "v_fma_f32 v195, v195, v173, v219\n"
    "s_waitcnt lgkmcnt(4)\n"
    "v_pk_add_f32 v[192:193], v[192:193], v[220:221]\n"
    "v_pk_add_f32 v[194:195], v[194:195], v[222:223]\n"
    "s_mov_b32 s76, 16384\n"
    "buffer_store_dwordx4 v[192:195], v9, s[52:55], s76 offen\n"
    "s_waitcnt lgkmcnt(3)\n"
    "v_fma_f32 v196, v196, v174, v224\n"
    "v_fma_f32 v197, v197, v174, v225\n"
    "v_fma_f32 v198, v198, v174, v226\n"
    "v_fma_f32 v199, v199, v174, v227\n"
    "s_waitcnt lgkmcnt(2)\n"
    "v_pk_add_f32 v[196:197], v[196:197], v[228:229]\n"
    "v_pk_add_f32 v[198:199], v[198:199], v[230:231]\n"
    "s_mov_b32 s76, 32768\n"
    "buffer_store_dwordx4 v[196:199], v9, s[52:55], s76 offen\n"
    "s_waitcnt lgkmcnt(1)\n"
    "v_fma_f32 v200, v200, v175, v232\n"
    "v_fma_f32 v201, v201, v175, v233\n"
    "v_fma_f32 v202, v202, v175, v234\n"
    "v_fma_f32 v203, v203, v175, v235\n"
    "s_waitcnt lgkmcnt(0)\n"
    "v_pk_add_f32 v[200:201], v[200:201], v[236:237]\n"
    "v_pk_add_f32 v[202:203], v[202:203], v[238:239]\n"
    "s_mov_b32 s76, 49152\n"
    "buffer_store_dwordx4 v[200:203], v9, s[52:55], s76 offen\n"
    "s_branch Lend_%=\n"
    "Lssm_%=:\n"
    "s_load_dwordx16 s[8:23], %0, 0\n"
    "s_load_dwordx16 s[24:39], %0, 64\n"
    "v_and_b32 v4, 63, %2\n"
    "v_lshrrev_b32 v26, 6, %2\n"
    "s_lshr_b32 s66, %1, 3\n"
    "v_readfirstlane_b32 s60, v26\n"
    "s_and_b32 s63, %1, 1\n"
    "s_bfe_u32 s77, %1, 0x20001\n"
    "s_lshr_b32 s90, s66, 3\n"
    "s_lshl2_add_u32 s61, s77, s90\n"
    "s_and_b32 s62, s66, 7\n"
    "s_lshl1_add_u32 s91, s62, s63\n"
    "s_mov_b32 s65, 0x76543210\n"
    "s_cmp_eq_u32 s90, 1\n"
    "s_cselect_b32 s65, 0x10765432, s65\n"
    "s_cmp_eq_u32 s90, 2\n"
    "s_cselect_b32 s65, 0x32107654, s65\n"
    "s_cmp_eq_u32 s90, 3\n"
    "s_cselect_b32 s65, 0x54321076, s65\n"
    "s_lshl2_add_u32 s94, s61, s60\n"
    "s_mul_i32 s67, s60, 20480\n"
    "s_lshl_b32 s81, s60, 11\n"
    "s_add_u32 s81, s81, 98304\n"
    "s_mul_i32 s77, s62, 0x11111111\n"
    "s_add_u32 s65, s65, s77\n"
    "s_bfe_u32 s68, s65, 0x30000\n"
    "s_lshl_b32 s68, s68, 7\n"
    "s_bfe_u32 s69, s65, 0x30004\n"
    "s_lshl_b32 s69, s69, 7\n"
    "s_bfe_u32 s70, s65, 0x30008\n"
    "s_lshl_b32 s70, s70, 7\n"
    "s_bfe_u32 s71, s65, 0x3000c\n"
    "s_lshl_b32 s71, s71, 7\n"
    "s_bfe_u32 s72, s65, 0x30010\n"
    "s_lshl_b32 s72, s72, 7\n"
    "s_bfe_u32 s73, s65, 0x30014\n"
    "s_lshl_b32 s73, s73, 7\n"
    "s_bfe_u32 s74, s65, 0x30018\n"
    "s_lshl_b32 s74, s74, 7\n"
    "s_bfe_u32 s75, s65, 0x3001c\n"
    "s_lshl_b32 s75, s75, 7\n"
    "s_mul_i32 s77, s62, 3\n"
    "s_min_u32 s77, s77, 20\n"
    "s_mul_i32 s78, s63, 22\n"
    "s_add_u32 s82, s77, s78\n"
    "s_lshl2_add_u32 s92, s62, s78\n"
    "s_cmp_ge_u32 s62, 6\n"
    "s_cselect_b32 s93, 2, 3\n"
    "s_cmp_eq_u32 s91, 15\n"
    "s_cselect_b32 s79, 1, s93\n"
    "s_lshl_b32 s79, s79, 1\n"
    "v_lshrrev_b32 v26, 3, v4\n"
    "v_and_b32 v27, 7, v4\n"
    "v_lshrrev_b32 v28, 4, v4\n"
    "v_xor_b32 v28, v27, v28\n"
    "v_lshlrev_b32 v28, 4, v28\n"
    "v_lshrrev_b32 v29, 5, v4\n"
    "v_and_b32 v31, 3, v26\n"
    "v_lshl_add_u32 v29, v29, 3, v31\n"
    "s_lshl_b32 s77, s94, 5\n"
    "v_add_u32 v29, s77, v29\n"
    "v_lshl_add_u32 v5, v29, 10, v28\n"
    "v_xor_b32 v31, 64, v5\n"
    "v_add_u32 v6, 15360, v31\n"
    "v_add_u32 v7, 2048, v5\n"
    "v_add_u32 v8, 17408, v31\n"
    "v_mul_u32_u24 v27, 43, v4\n"
    "v_lshrrev_b32 v27, 8, v27\n"
    "v_mul_u32_u24 v31, 6, v27\n"
    "v_sub_u32 v31, v4, v31\n"
    "v_and_b32 v9, 4, v27\n"
    "v_add_u32 v27, v27, v9\n"
    "s_lshl_b32 s77, s60, 4\n"
    "v_add_u32 v27, s77, v27\n"
    "v_mul_u32_u24 v27, 2752, v27\n"
    "v_lshl_add_u32 v27, v31, 4, v27\n"
    "s_lshl_b32 s77, s82, 5\n"
    "s_add_u32 s77, s77, 1376\n"
    "v_add_u32 v27, s77, v27\n"
    "v_cmp_gt_u32 vcc, s79, v31\n"
    "v_mov_b32 v31, 0x40000000\n"
    "s_nop 0\n"
    "v_cndmask_b32 v27, v31, v27, vcc\n"
    "v_cmp_gt_u32 vcc, 48, v4\n"
    "s_nop 1\n"
    "v_cndmask_b32 v9, v31, v27, vcc\n"
    "v_add_u32 v10, 9984, v9\n"
    "s_waitcnt lgkmcnt(0)\n"
    "s_mov_b32 s40, s8\n"
    "s_and_b32 s41, s9, 0xffff\n"
    "s_mov_b32 s42, 0x200000\n"
    "s_mov_b32 s43, 0x20000\n"
    "s_mov_b32 s44, s10\n"
    "s_and_b32 s45, s11, 0xffff\n"
    "s_mov_b32 s46, 0x158000\n"
    "s_mov_b32 s47, 0x20000\n"
    "s_add_u32 m0, s67, 0\n"
    "s_nop 0\n"
    "buffer_load_dwordx4 v5, s[40:43], s68 offen offset:0 lds\n"
    "buffer_load_dwordx4 v6, s[40:43], s68 offen offset:1024 lds\n"
    "buffer_load_dwordx4 v7, s[40:43], s68 offen offset:2048 lds\n"
    "buffer_load_dwordx4 v8, s[40:43], s68 offen offset:3072 lds\n"
    "s_add_u32 m0, s81, 0\n"
    "s_mul_i32 s76, s68, 1376\n"
    "buffer_load_dwordx4 v9, s[44:47], s76 offen offset:0 lds\n"
    "buffer_load_dwordx4 v10, s[44:47], s76 offen offset:1024 lds\n"
    "s_lshl_b32 s77, s91, 6\n"
    "v_and_b32 v31, 31, v4\n"
    "v_lshl_add_u32 v31, v31, 1, s77\n"
    "global_load_ushort v220, v31, s[18:19]\n"
    "global_load_ushort v221, v31, s[20:21]\n"
    "global_load_ushort v222, v31, s[22:23]\n"
    "v_mov_b32 v27, 0\n"
    "global_load_dword v229, v27, s[16:17] sc1\n"
    "s_mov_b32 s48, s38\n"
    "s_and_b32 s49, s39, 0xffff\n"
    "s_cmp_eq_u32 s91, 15\n"
    "s_cselect_b32 s50, 1024, 0\n"
    "s_mov_b32 s51, 0x20000\n"
    "v_lshlrev_b32 v27, 2, v26\n"
    "v_lshrrev_b32 v27, v27, s65\n"
    "v_and_b32 v27, 7, v27\n"
    "v_and_b32 v31, 7, v4\n"
    "v_lshlrev_b32 v31, 4, v31\n"
    "v_lshl_add_u32 v27, v27, 7, v31\n"
    "s_lshl_b32 s77, s60, 10\n"
    "s_add_u32 s77, s77, 84992\n"
    "s_mov_b32 m0, s77\n"
    "v_lshrrev_b32 v104, 4, v4\n"
    "buffer_load_dwordx4 v27, s[48:51], 0 offen lds\n"
    "v_lshl_add_u32 v104, v104, 4, s77\n"
    "s_getpc_b64 s[98:99]\n"
    "Lpc1_%=:\n"
    "s_sub_u32 s98, s98, Lpc1_%=-Lbeg_%=\n"
    "s_subb_u32 s99, s99, 0\n"
    "s_lshl2_add_u32 s77, s66, s60\n"
    "v_and_b32 v27, 3, v4\n"
    "v_lshl_add_u32 v27, s77, 2, v27\n"
    "v_lshlrev_b32 v27, 7, v27\n"
    "v_min_u32 v27, Lend_%=-Lbeg_%=-4, v27\n"
    "v_and_b32 v27, -4, v27\n"
    "global_load_dword v230, v27, s[98:99]\n"
    "s_add_u32 m0, s67, 4096\n"
    "s_nop 0\n"
    "buffer_load_dwordx4 v5, s[40:43], s69 offen offset:0 lds\n"
    "buffer_load_dwordx4 v6, s[40:43], s69 offen offset:1024 lds\n"
    "buffer_load_dwordx4 v7, s[40:43], s69 offen offset:2048 lds\n"
    "buffer_load_dwordx4 v8, s[40:43], s69 offen offset:3072 lds\n"
    "s_mov_b32 s56, s16\n"
    "s_and_b32 s57, s17, 0xffff\n"
    "s_mov_b32 s58, 0x1100000\n"
    "s_mov_b32 s59, 0x20000\n"
    "s_mov_b32 s52, s26\n"
    "s_and_b32 s53, s27, 0xffff\n"
    "s_mov_b32 s54, 0x80000\n"
    "s_mov_b32 s55, 0x20000\n"
    "s_bitcmp1_b32 s60, 0\n"
    "s_cselect_b32 s48, s14, s12\n"
    "s_cselect_b32 s49, s15, s13\n"
    "s_and_b32 s49, s49, 0xffff\n"
    "s_mov_b32 s50, 0x80000\n"
    "s_mov_b32 s51, 0x20000\n"
    "s_lshr_b32 s78, s60, 1\n"
    "s_lshl1_add_u32 s78, s91, s78\n"
    "s_lshl_b32 s78, s78, 14\n"
    "v_lshl_add_u32 v11, v26, 10, v28\n"
    "v_add_u32 v11, s78, v11\n"
    "v_xor_b32 v31, 64, v11\n"
    "v_add_u32 v12, 7168, v31\n"
    "v_and_b32 v27, 15, v4\n"
    "v_lshrrev_b32 v31, 4, v4\n"
    "v_lshrrev_b32 v15, 1, v27\n"
    "v_xor_b32 v15, v15, v31\n"
    "v_lshlrev_b32 v15, 4, v15\n"
    "v_lshl_add_u32 v15, v27, 7, v15\n"
    "v_add_u32 v13, s67, v15\n"
    "v_xor_b32 v14, 64, v13\n"
    "v_add_u32 v15, 98304, v15\n"
    "v_xor_b32 v16, 64, v15\n"
    "v_bfe_u32 v17, v4, 2, 3\n"
    "v_mul_u32_u24 v17, 96, v17\n"
    "v_and_b32 v27, 3, v4\n"
    "v_lshl_add_u32 v17, v27, 3, v17\n"
    "v_lshrrev_b32 v27, 5, v4\n"
    "v_lshl_add_u32 v17, v27, 11, v17\n"
    "v_add_u32 v17, 98304, v17\n"
    "v_lshlrev_b32 v22, 4, v4\n"
    "s_mov_b32 s18, 0xbdd2d3e8\n"
    "s_mov_b32 s19, 0xc0135761\n"
    "s_mov_b32 s20, 0xcccccccc\n"
    "s_mov_b32 s21, 0xcccccccc\n"
    "s_mov_b32 s22, 0xaaaaaaaa\n"
    "s_mov_b32 s23, 0xaaaaaaaa\n"
    "s_mov_b32 s16, 0xffff0000\n"
    "s_mov_b32 s17, 0xffff0000\n"
    "s_cmp_eq_u32 s60, 0\n"
    "s_cselect_b32 s96, 0, 0x40000000\n"
    "v_and_b32 v244, 3, v4\n"
    "v_bfe_u32 v245, v4, 2, 1\n"
    "v_lshl_add_u32 v244, v245, 3, v244\n"
    "v_bfe_u32 v245, v4, 3, 1\n"
    "v_lshl_add_u32 v244, v245, 2, v244\n"
    "v_and_b32 v245, 16, v4\n"
    "v_add_u32 v244, v245, v244\n"
    "v_lshlrev_b32 v244, 4, v244\n"
    "v_lshrrev_b32 v245, 5, v4\n"
    "v_mul_u32_u24 v245, 22528, v245\n"
    "v_add_u32 v23, v244, v245\n"
    "v_lshrrev_b32 v244, 4, v4\n"
    "v_and_b32 v245, 7, v4\n"
    "v_lshl_add_u32 v244, v244, 3, v245\n"
    "v_and_b32 v245, 8, v4\n"
    "v_lshl_add_u32 v24, v244, 4, v245\n"
    "v_and_b32 v244, 31, v4\n"
    "v_bfe_u32 v245, v4, 1, 3\n"
    "v_lshrrev_b32 v246, 5, v4\n"
    "v_xor_b32 v245, v245, v246\n"
    "v_lshlrev_b32 v245, 4, v245\n"
    "v_lshl_add_u32 v244, v244, 7, v245\n"
    "v_add_u32 v18, s67, v244\n"
    "v_xor_b32 v19, 32, v18\n"
    "v_xor_b32 v20, 64, v18\n"
    "v_xor_b32 v21, 96, v18\n"
    "v_bfe_u32 v253, v4, 2, 2\n"
    "v_and_b32 v244, 3, v4\n"
    "v_lshl_add_u32 v253, v253, 3, v244\n"
    "v_bfe_u32 v244, v4, 4, 1\n"
    "v_lshl_add_u32 v253, v244, 2, v253\n"
    "s_lshl_b32 s79, s94, 7\n"
    "s_add_u32 s79, s79, 0x30000\n"
    "s_cmp_eq_u32 s91, 15\n"
    "s_cselect_b32 s79, s79, 0x40000000\n"
    "v_lshl_add_u32 v105, v253, 2, s79\n"
    "v_and_b32 v244, 15, v4\n"
    "s_lshl_b32 s79, s61, 11\n"
    "s_add_u32 s79, s79, 0x9000\n"
    "v_lshl_add_u32 v254, v244, 7, s79\n"
    "s_lshl_b32 s79, s61, 8\n"
    "s_add_u32 s79, s79, s60\n"
    "s_lshl_b32 s79, s79, 10\n"
    "s_add_u32 s79, s79, 0x700000\n"
    "v_add_u32 v248, s79, v22\n"
    "v_add_u32 v249, 64512, v248\n"
    "v_add_u32 v250, 131072, v248\n"
    "v_add_u32 v251, 195584, v248\n"
    "v_add_u32 v252, 98304, v22\n"
    "s_mul_i32 s79, s94, 45056\n"
    "s_lshl_b32 s97, s82, 9\n"
    "s_add_u32 s79, s79, s97\n"
    "s_add_u32 s86, s79, 0x100000\n"
    "s_lshl_b32 s95, s94, 13\n"
    "s_add_u32 s95, s95, 0x80000\n"
    "s_lshl_b32 s79, s91, 9\n"
    "v_add_u32 v24, s79, v24\n"
    "v_add_u32 v31, 4096, v22\n"
    "v_bfe_u32 v106, v4, 3, 2\n"
    "v_and_b32 v244, 7, v4\n"
    "v_lshl_add_u32 v106, v106, 4, v244\n"
    "v_lshrrev_b32 v244, 5, v4\n"
    "v_lshl_add_u32 v106, v244, 3, v106\n"
    "s_lshl_b32 s79, s60, 8\n"
    "s_add_u32 s97, s79, 81920\n"
    "v_lshl_add_u32 v106, v106, 2, s97\n"
    "v_lshrrev_b32 v244, 4, v4\n"
    "v_lshl_add_u32 v107, v244, 6, s97\n"
    "v_and_b32 v244, 15, v4\n"
    "v_lshlrev_b32 v244, 3, v244\n"
    "v_add_u32 v109, 82944, v244\n"
    "v_add_u32 v108, s79, v109\n"
    "v_add_u32 v111, 83968, v244\n"
    "v_and_b32 v244, 31, v4\n"
    "s_add_u32 s79, s79, 83968\n"
    "v_lshl_add_u32 v110, v244, 3, s79\n"
    "s_lshl_b32 s79, s61, 4\n"
    "s_add_u32 s79, s79, s91\n"
    "s_lshl_b32 s79, s79, 9\n"
    "s_add_u32 s79, s79, 0x40000\n"
    "s_add_u32 s79, s79, s96\n"
    "v_add_u32 v114, s79, v22\n"
    "v_cmp_gt_u32 vcc, 32, v4\n"
    "v_mov_b32 v244, 0x40000000\n"
    "s_nop 0\n"
    "v_cndmask_b32 v114, v244, v114, vcc\n"
    "s_lshl_b32 s79, s60, 10\n"
    "s_add_u32 s79, s79, 84992\n"
    "v_and_b32 v112, 15, v4\n"
    "v_and_b32 v244, 0x30, v4\n"
    "v_lshl_add_u32 v112, v112, 6, v244\n"
    "v_add_u32 v112, s79, v112\n"
    "v_lshrrev_b32 v113, 5, v4\n"
    "v_bfe_u32 v244, v4, 2, 2\n"
    "v_lshl_add_u32 v113, v113, 3, v244\n"
    "v_lshlrev_b32 v113, 6, v113\n"
    "v_and_b32 v244, 3, v4\n"
    "v_lshl_add_u32 v113, v244, 4, v113\n"
    "v_bfe_u32 v244, v4, 4, 1\n"
    "v_lshl_add_u32 v113, v244, 3, v113\n"
    "v_add_u32 v113, s79, v113\n"
    "s_lshl_b32 s79, s61, 4\n"
    "s_add_u32 s79, s79, s91\n"
    "s_lshl_b32 s79, s79, 7\n"
    "s_add_u32 s14, s79, s96\n"
    "s_waitcnt vmcnt(9)\n"
    "v_lshlrev_b32 v220, 16, v220\n"
    "s_waitcnt vmcnt(8)\n"
    "v_lshlrev_b32 v221, 16, v221\n"
    "s_waitcnt vmcnt(7)\n"
    "v_lshlrev_b32 v222, 16, v222\n"
    "v_mul_f32 v222, 0x3fb8aa3b, v222\n"
    "v_exp_f32 v222, v222\n"
    "s_nop 0\n"
    "v_cvt_pk_bf16_f32 v222, v222, v222\n"
    "v_lshlrev_b32 v222, 16, v222\n"
    "v_mul_f32 v236, v222, v220\n"
    "v_mul_f32 v237, v222, v221\n"
    "v_mul_f32 v236, 0x3fb8aa3b, v236\n"
    "v_mul_f32 v237, 0x3e22f983, v237\n"
    "v_mov_b32 v232, v236\n"
    "v_mov_b32 v234, v236\n"
    "v_mov_b32 v223, v220\n"
    "v_mov_b32 v233, v237\n"
    "v_mov_b32 v235, v237\n"
    "v_mov_b32 v224, v221\n"
    "v_permlane16_swap_b32 v232, v234\n"
    "v_permlane16_swap_b32 v220, v223\n"
    "v_permlane16_swap_b32 v233, v235\n"
    "v_permlane16_swap_b32 v221, v224\n"
    "s_waitcnt vmcnt(6)\n"
    "v_readfirstlane_b32 s64, v229\n"
    "s_nop 0\n"
    "s_add_u32 s64, s64, 1\n"
    "v_mov_b32 v25, s64\n"
    "v_lshrrev_b32 v238, 4, v4\n"
    "v_lshlrev_b32 v238, 3, v238\n"
    "s_lshl_b32 s79, s60, 5\n"
    "v_add_u32 v239, s79, v238\n"
    "v_cvt_f32_u32 v238, v238\n"
    "v_cvt_f32_u32 v239, v239\n"
    "v_exp_f32 v244, v232\n"
    "v_fract_f32 v245, v233\n"
    "v_cos_f32 v184, v245\n"
    "v_sin_f32 v185, v245\n"
    "v_mul_f32 v184, v184, v244\n"
    "v_mul_f32 v185, v185, v244\n"
    "v_mul_f32 v244, 0x41000000, v232\n"
    "v_mul_f32 v245, 0x41000000, v233\n"
    "v_exp_f32 v244, v244\n"
    "v_fract_f32 v245, v245\n"
    "v_cos_f32 v186, v245\n"
    "v_sin_f32 v187, v245\n"
    "v_mul_f32 v186, v186, v244\n"
    "v_mul_f32 v187, v187, v244\n"
    "v_mul_f32 v188, v186, v186\n"
    "v_mul_f32 v189, v186, v187\n"
    "s_add_u32 m0, s81, 8192\n"
    "s_mul_i32 s76, s69, 1376\n"
    "buffer_load_dwordx4 v9, s[44:47], s76 offen offset:0 lds\n"
    "v_fma_f32 v188, -v187, v187, v188\n"
    "v_add_f32 v189, v189, v189\n"
    "v_mul_f32 v190, v188, v188\n"
    "v_mul_f32 v191, v188, v189\n"
    "v_fma_f32 v190, -v189, v189, v190\n"
    "v_add_f32 v191, v191, v191\n"
    "v_cndmask_b32 v192, 1.0, v186, s[16:17]\n"
    "v_cndmask_b32 v193, 0, v187, s[16:17]\n"
    "v_mul_f32 v244, v238, v232\n"
    "v_mul_f32 v245, v238, v233\n"
    "v_exp_f32 v244, v244\n"
    "v_fract_f32 v245, v245\n"
    "v_cos_f32 v194, v245\n"
    "v_sin_f32 v195, v245\n"
    "v_mul_f32 v194, v194, v244\n"
    "v_mul_f32 v195, v195, v244\n"
    "buffer_load_dwordx4 v10, s[44:47], s76 offen offset:1024 lds\n"
    "v_mul_f32 v244, v239, v232\n"
    "v_mul_f32 v245, v239, v233\n"
    "v_exp_f32 v244, v244\n"
    "v_fract_f32 v245, v245\n"
    "v_cos_f32 v196, v245\n"
    "v_sin_f32 v197, v245\n"
    "v_mul_f32 v196, v196, v244\n"
    "v_mul_f32 v197, v197, v244\n"
    "v_cmp_gt_u32 vcc, 32, v4\n"
    "s_nop 1\n"
    "v_cndmask_b32 v192, v192, 0, vcc\n"
    "v_cndmask_b32 v193, v193, 0, vcc\n"
    "v_mul_f32 v244, v220, v220\n"
    "v_fmac_f32 v244, v221, v221\n"
    "v_rcp_f32 v244, v244\n"
    "v_add_f32 v245, -1.0, v184\n"
    "v_add_f32 v244, v244, v244\n"
    "s_add_u32 m0, s67, 8192\n"
    "s_nop 0\n"
    "buffer_load_dwordx4 v5, s[40:43], s70 offen offset:0 lds\n"
    "v_mul_f32 v198, v245, v220\n"
    "v_fmac_f32 v198, v185, v221\n"
    "v_mul_f32 v199, v185, v220\n"
    "v_fma_f32 v199, -v245, v221, v199\n"
    "v_mul_f32 v198, v198, v244\n"
    "v_mul_f32 v199, v199, v244\n"
    "v_exp_f32 v244, v234\n"
    "v_fract_f32 v245, v235\n"
    "v_cos_f32 v200, v245\n"
    "v_sin_f32 v201, v245\n"
    "v_mul_f32 v200, v200, v244\n"
    "v_mul_f32 v201, v201, v244\n"
    "v_mul_f32 v244, 0x41000000, v234\n"
    "v_mul_f32 v245, 0x41000000, v235\n"
    "v_exp_f32 v244, v244\n"
    "v_fract_f32 v245, v245\n"
    "buffer_load_dwordx4 v6, s[40:43], s70 offen offset:1024 lds\n"
    "v_cos_f32 v202, v245\n"
    "v_sin_f32 v203, v245\n"
    "v_mul_f32 v202, v202, v244\n"
    "v_mul_f32 v203, v203, v244\n"
    "v_mul_f32 v204, v202, v202\n"
    "v_mul_f32 v205, v202, v203\n"
    "v_fma_f32 v204, -v203, v203, v204\n"
    "v_add_f32 v205, v205, v205\n"
    "v_mul_f32 v206, v204, v204\n"
    "v_mul_f32 v207, v204, v205\n"
    "v_fma_f32 v206, -v205, v205, v206\n"
    "v_add_f32 v207, v207, v207\n"
    "v_cndmask_b32 v208, 1.0, v202, s[16:17]\n"
    "v_cndmask_b32 v209, 0, v203, s[16:17]\n"
    "v_mul_f32 v244, v238, v234\n"
    "v_mul_f32 v245, v238, v235\n"
    "buffer_load_dwordx4 v7, s[40:43], s70 offen offset:2048 lds\n"
    "v_exp_f32 v244, v244\n"
    "v_fract_f32 v245, v245\n"
    "v_cos_f32 v210, v245\n"
    "v_sin_f32 v211, v245\n"
    "v_mul_f32 v210, v210, v244\n"
    "v_mul_f32 v211, v211, v244\n"
    "v_mul_f32 v244, v239, v234\n"
    "v_mul_f32 v245, v239, v235\n"
    "v_exp_f32 v244, v244\n"
    "v_fract_f32 v245, v245\n"
    "v_cos_f32 v212, v245\n"
    "v_sin_f32 v213, v245\n"
    "v_mul_f32 v212, v212, v244\n"
    "v_mul_f32 v213, v213, v244\n"
    "v_cmp_gt_u32 vcc, 32, v4\n"
    "s_nop 1\n"
    "v_cndmask_b32 v208, v208, 0, vcc\n"
    "buffer_load_dwordx4 v8, s[40:43], s70 offen offset:3072 lds\n"
    "v_cndmask_b32 v209, v209, 0, vcc\n"
    "v_mul_f32 v244, v223, v223\n"
    "v_fmac_f32 v244, v224, v224\n"
    "v_rcp_f32 v244, v244\n"
    "v_add_f32 v245, -1.0, v200\n"
    "v_add_f32 v244, v244, v244\n"
    "v_mul_f32 v214, v245, v223\n"
    "v_fmac_f32 v214, v201, v224\n"
    "v_mul_f32 v215, v201, v223\n"
    "v_fma_f32 v215, -v245, v224, v215\n"
    "v_mul_f32 v214, v214, v244\n"
    "v_mul_f32 v215, v215, v244\n"
    "v_lshrrev_b32 v246, 5, v4\n"
    "s_lshl2_add_u32 s79, s60, 0\n"
    "v_add_u32 v246, s79, v246\n"
    "v_lshlrev_b32 v2, 13, v246\n"
    "s_add_u32 m0, s81, 16384\n"
    "s_mul_i32 s76, s70, 1376\n"
    "buffer_load_dwordx4 v9, s[44:47], s76 offen offset:0 lds\n"
    "v_sub_u32 v246, s61, v246\n"
    "v_add_u32 v246, -1, v246\n"
    "v_cmp_le_i32 vcc, 0, v246\n"
    "v_max_i32 v246, 0, v246\n"
    "v_lshlrev_b32 v246, 7, v246\n"
    "v_cvt_f32_u32 v246, v246\n"
    "s_lshl_b32 s79, s91, 9\n"
    "s_add_u32 s79, s79, 0x40000\n"
    "v_and_b32 v247, 0x1f0, v22\n"
    "v_add_u32 v247, s79, v247\n"
    "v_add_u32 v2, v2, v247\n"
    "v_mov_b32 v247, 0x40000000\n"
    "v_cndmask_b32 v2, v247, v2, vcc\n"
    "v_mul_f32 v244, v246, v236\n"
    "v_mul_f32 v245, v246, v237\n"
    "v_exp_f32 v244, v244\n"
    "buffer_load_dwordx4 v10, s[44:47], s76 offen offset:1024 lds\n"
    "v_fract_f32 v245, v245\n"
    "v_cos_f32 v216, v245\n"
    "v_sin_f32 v217, v245\n"
    "v_mul_f32 v216, v216, v244\n"
    "v_mul_f32 v217, v217, v244\n"
    "v_lshrrev_b32 v246, 5, v4\n"
    "s_lshl2_add_u32 s79, s60, 2\n"
    "v_add_u32 v246, s79, v246\n"
    "v_lshlrev_b32 v3, 13, v246\n"
    "v_sub_u32 v246, s61, v246\n"
    "v_add_u32 v246, -1, v246\n"
    "v_cmp_le_i32 vcc, 0, v246\n"
    "v_max_i32 v246, 0, v246\n"
    "v_lshlrev_b32 v246, 7, v246\n"
    "v_cvt_f32_u32 v246, v246\n"
    "s_lshl_b32 s79, s91, 9\n"
    "s_add_u32 m0, s67, 12288\n"
    "s_nop 0\n"
    "buffer_load_dwordx4 v5, s[40:43], s71 offen offset:0 lds\n"
    "s_add_u32 s79, s79, 0x40000\n"
    "v_and_b32 v247, 0x1f0, v22\n"
    "v_add_u32 v247, s79, v247\n"
    "v_add_u32 v3, v3, v247\n"
    "v_mov_b32 v247, 0x40000000\n"
    "v_cndmask_b32 v3, v247, v3, vcc\n"
    "v_mul_f32 v244, v246, v236\n"
    "v_mul_f32 v245, v246, v237\n"
    "v_exp_f32 v244, v244\n"
    "v_fract_f32 v245, v245\n"
    "v_cos_f32 v218, v245\n"
    "v_sin_f32 v219, v245\n"
    "v_mul_f32 v218, v218, v244\n"
    "v_mul_f32 v219, v219, v244\n"
    "buffer_load_dwordx4 v6, s[40:43], s71 offen offset:1024 lds\n"
    "buffer_load_dwordx4 v7, s[40:43], s71 offen offset:2048 lds\n"
    "buffer_load_dwordx4 v8, s[40:43], s71 offen offset:3072 lds\n"
    "s_add_u32 m0, s81, 24576\n"
    "s_mul_i32 s76, s71, 1376\n"
    "buffer_load_dwordx4 v9, s[44:47], s76 offen offset:0 lds\n"
    "buffer_load_dwordx4 v10, s[44:47], s76 offen offset:1024 lds\n"
    "s_waitcnt vmcnt(19)\n"
    "s_barrier\n"
    "ds_read_b128 a[0:3], v13 offset:0\n"
    "ds_read_b128 a[4:7], v13 offset:2048\n"
    "ds_read_b64_tr_b16 v[64:65], v17 offset:0\n"
    "ds_read_b64_tr_b16 v[66:67], v17 offset:1024\n"
    "ds_read_b64_tr_b16 v[68:69], v17 offset:32\n"
    "ds_read_b64_tr_b16 v[70:71], v17 offset:1056\n"
    "ds_read_b128 v[72:75], v104 offset:0\n"
    "ds_read_b128 a[8:11], v14 offset:0\n"
    "ds_read_b128 a[12:15], v14 offset:2048\n"
    "ds_read_b64_tr_b16 v[76:77], v17 offset:4096\n"
    "ds_read_b64_tr_b16 v[78:79], v17 offset:5120\n"
    "s_waitcnt lgkmcnt(5)\n"
    "v_mfma_f32_16x16x32_bf16 v[120:123], a[0:3], v[64:67], 0\n"
    "ds_read_b64_tr_b16 v[80:81], v17 offset:4128\n"
    "v_mfma_f32_16x16x32_bf16 v[124:127], a[4:7], v[64:67], 0\n"
    "ds_read_b64_tr_b16 v[82:83], v17 offset:5152\n"
    "v_mfma_f32_16x16x32_bf16 v[128:131], a[0:3], v[68:71], 0\n"
    "ds_read_b128 v[84:87], v104 offset:64\n"
    "v_mfma_f32_16x16x32_bf16 v[132:135], a[4:7], v[68:71], 0\n"
    "s_waitcnt lgkmcnt(3)\n"
    "v_mfma_f32_16x16x32_bf16 v[96:99], v[72:75], a[0:3], 0\n"
    "s_waitcnt vmcnt(12)\n"
    "s_barrier\n"
    "v_mfma_f32_16x16x32_bf16 v[100:103], v[72:75], a[4:7], 0\n"
    "ds_read_b128 a[16:19], v13 offset:4096\n"
    "ds_read_b128 a[20:23], v13 offset:6144\n"
    "ds_read_b64_tr_b16 v[88:89], v17 offset:8192\n"
    "ds_read_b64_tr_b16 v[90:91], v17 offset:9216\n"
    "v_mfma_f32_16x16x32_bf16 v[120:123], a[8:11], v[76:79], v[120:123]\n"
    "ds_read_b64_tr_b16 v[92:93], v17 offset:8224\n"
    "v_mfma_f32_16x16x32_bf16 v[124:127], a[12:15], v[76:79], v[124:127]\n"
    "ds_read_b64_tr_b16 v[94:95], v17 offset:9248\n"
    "s_waitcnt lgkmcnt(6)\n"
    "v_mfma_f32_16x16x32_bf16 v[128:131], a[8:11], v[80:83], v[128:131]\n"
    "ds_read_b128 v[64:67], v104 offset:128\n"
    "s_add_u32 m0, s67, 16384\n"
    "v_mfma_f32_16x16x32_bf16 v[132:135], a[12:15], v[80:83], v[132:135]\n"
    "buffer_load_dwordx4 v5, s[40:43], s72 offen offset:0 lds\n"
    "v_mfma_f32_16x16x32_bf16 v[96:99], v[84:87], a[8:11], v[96:99]\n"
    "ds_read_b128 a[24:27], v14 offset:4096\n"
    "ds_read_b128 a[28:31], v14 offset:6144\n"
    "v_mfma_f32_16x16x32_bf16 v[100:103], v[84:87], a[12:15], v[100:103]\n"
    "ds_read_b64_tr_b16 v[68:69], v17 offset:12288\n"
    "ds_read_b64_tr_b16 v[70:71], v17 offset:13312\n"
    "s_waitcnt lgkmcnt(5)\n"
    "v_mfma_f32_16x16x32_bf16 v[120:123], a[16:19], v[88:91], v[120:123]\n"
    "ds_read_b64_tr_b16 v[72:73], v17 offset:12320\n"
    "v_mfma_f32_16x16x32_bf16 v[124:127], a[20:23], v[88:91], v[124:127]\n"
    "ds_read_b64_tr_b16 v[74:75], v17 offset:13344\n"
    "v_mfma_f32_16x16x32_bf16 v[128:131], a[16:19], v[92:95], v[128:131]\n"
    "ds_read_b128 v[76:79], v104 offset:192\n"
    "v_mfma_f32_16x16x32_bf16 v[132:135], a[20:23], v[92:95], v[132:135]\n"
    "s_waitcnt lgkmcnt(3)\n"
    "v_mfma_f32_16x16x32_bf16 v[96:99], v[64:67], a[16:19], v[96:99]\n"
    "buffer_load_dwordx4 v6, s[40:43], s72 offen offset:1024 lds\n"
    "buffer_load_dwordx4 v7, s[40:43], s72 offen offset:2048 lds\n"
    "buffer_load_dwordx4 v8, s[40:43], s72 offen offset:3072 lds\n"
    "s_waitcnt vmcnt(10)\n"
    "s_barrier\n"
    "v_mfma_f32_16x16x32_bf16 v[100:103], v[64:67], a[20:23], v[100:103]\n"
    "ds_read_b128 a[32:35], v13 offset:8192\n"
    "ds_read_b128 a[36:39], v13 offset:10240\n"
    "ds_read_b64_tr_b16 v[80:81], v17 offset:16384\n"
    "ds_read_b64_tr_b16 v[82:83], v17 offset:17408\n"
    "v_mfma_f32_16x16x32_bf16 v[120:123], a[24:27], v[68:71], v[120:123]\n"
    "ds_read_b64_tr_b16 v[84:85], v17 offset:16416\n"
    "v_mfma_f32_16x16x32_bf16 v[124:127], a[28:31], v[68:71], v[124:127]\n"
    "ds_read_b64_tr_b16 v[86:87], v17 offset:17440\n"
    "s_waitcnt lgkmcnt(6)\n"
    "v_mfma_f32_16x16x32_bf16 v[128:131], a[24:27], v[72:75], v[128:131]\n"
    "ds_read_b128 v[88:91], v104 offset:256\n"
    "v_mfma_f32_16x16x32_bf16 v[132:135], a[28:31], v[72:75], v[132:135]\n"
    "v_mfma_f32_16x16x32_bf16 v[96:99], v[76:79], a[24:27], v[96:99]\n"
    "ds_read_b128 a[40:43], v14 offset:8192\n"
    "ds_read_b128 a[44:47], v14 offset:10240\n"
    "v_mfma_f32_16x16x32_bf16 v[100:103], v[76:79], a[28:31], v[100:103]\n"
    "ds_read_b64_tr_b16 v[92:93], v17 offset:20480\n"
    "ds_read_b64_tr_b16 v[94:95], v17 offset:21504\n"
    "s_waitcnt lgkmcnt(5)\n"
    "v_mfma_f32_16x16x32_bf16 v[120:123], a[32:35], v[80:83], v[120:123]\n"
    "ds_read_b64_tr_b16 v[64:65], v17 offset:20512\n"
    "s_add_u32 m0, s81, 32768\n"
    "v_mfma_f32_16x16x32_bf16 v[124:127], a[36:39], v[80:83], v[124:127]\n"
    "ds_read_b64_tr_b16 v[66:67], v17 offset:21536\n"
    "s_mul_i32 s76, s72, 1376\n"
    "v_mfma_f32_16x16x32_bf16 v[128:131], a[32:35], v[84:87], v[128:131]\n"
    "ds_read_b128 v[68:71], v104 offset:320\n"
    "buffer_load_dwordx4 v9, s[44:47], s76 offen offset:0 lds\n"
    "v_mfma_f32_16x16x32_bf16 v[132:135], a[36:39], v[84:87], v[132:135]\n"
    "buffer_load_dwordx4 v10, s[44:47], s76 offen offset:1024 lds\n"
    "s_waitcnt lgkmcnt(3)\n"
    "v_mfma_f32_16x16x32_bf16 v[96:99], v[88:91], a[32:35], v[96:99]\n"
    "s_add_u32 m0, s67, 0\n"
    "s_nop 0\n"
    "buffer_load_dwordx4 v5, s[40:43], s73 offen offset:0 lds\n"
    "buffer_load_dwordx4 v6, s[40:43], s73 offen offset:1024 lds\n"
    "buffer_load_dwordx4 v7, s[40:43], s73 offen offset:2048 lds\n"
    "buffer_load_dwordx4 v8, s[40:43], s73 offen offset:3072 lds\n"
    "s_add_u32 m0, s81, 40960\n"
    "s_mul_i32 s76, s73, 1376\n"
    "s_waitcnt vmcnt(10)\n"
    "s_barrier\n"
    "v_mfma_f32_16x16x32_bf16 v[100:103], v[88:91], a[36:39], v[100:103]\n"
    "ds_read_b128 a[48:51], v13 offset:12288\n"
    "ds_read_b128 a[52:55], v13 offset:14336\n"
    "ds_read_b64_tr_b16 v[72:73], v17 offset:24576\n"
    "ds_read_b64_tr_b16 v[74:75], v17 offset:25600\n"
    "v_mfma_f32_16x16x32_bf16 v[120:123], a[40:43], v[92:95], v[120:123]\n"
    "ds_read_b64_tr_b16 v[76:77], v17 offset:24608\n"
    "v_mfma_f32_16x16x32_bf16 v[124:127], a[44:47], v[92:95], v[124:127]\n"
    "ds_read_b64_tr_b16 v[78:79], v17 offset:25632\n"
    "s_waitcnt lgkmcnt(6)\n"
    "v_mfma_f32_16x16x32_bf16 v[128:131], a[40:43], v[64:67], v[128:131]\n"
    "ds_read_b128 v[80:83], v104 offset:384\n"
    "v_mfma_f32_16x16x32_bf16 v[132:135], a[44:47], v[64:67], v[132:135]\n"
    "v_mfma_f32_16x16x32_bf16 v[96:99], v[68:71], a[40:43], v[96:99]\n"
    "ds_read_b128 a[56:59], v14 offset:12288\n"
    "ds_read_b128 a[60:63], v14 offset:14336\n"
    "v_mfma_f32_16x16x32_bf16 v[100:103], v[68:71], a[44:47], v[100:103]\n"
    "ds_read_b64_tr_b16 v[84:85], v17 offset:28672\n"
    "ds_read_b64_tr_b16 v[86:87], v17 offset:29696\n"
    "s_waitcnt lgkmcnt(5)\n"
    "v_mfma_f32_16x16x32_bf16 v[120:123], a[48:51], v[72:75], v[120:123]\n"
    "ds_read_b64_tr_b16 v[88:89], v17 offset:28704\n"
    "buffer_load_dwordx4 v9, s[44:47], s76 offen offset:0 lds\n"
    "v_mfma_f32_16x16x32_bf16 v[124:127], a[52:55], v[72:75], v[124:127]\n"
    "ds_read_b64_tr_b16 v[90:91], v17 offset:29728\n"
    "v_mfma_f32_16x16x32_bf16 v[128:131], a[48:51], v[76:79], v[128:131]\n"
    "ds_read_b128 v[92:95], v104 offset:448\n"
    "buffer_load_dwordx4 v10, s[44:47], s76 offen offset:1024 lds\n"
    "v_mfma_f32_16x16x32_bf16 v[132:135], a[52:55], v[76:79], v[132:135]\n"
    "s_waitcnt lgkmcnt(3)\n"
    "v_mfma_f32_16x16x32_bf16 v[96:99], v[80:83], a[48:51], v[96:99]\n"
    "s_add_u32 m0, s67, 4096\n"
    "s_nop 0\n"
    "buffer_load_dwordx4 v5, s[40:43], s74 offen offset:0 lds\n"
    "buffer_load_dwordx4 v6, s[40:43], s74 offen offset:1024 lds\n"
    "buffer_load_dwordx4 v7, s[40:43], s74 offen offset:2048 lds\n"
    "buffer_load_dwordx4 v8, s[40:43], s74 offen offset:3072 lds\n"
    "s_add_u32 m0, s81, 49152\n"
    "s_mul_i32 s76, s74, 1376\n"
    "s_waitcnt vmcnt(10)\n"
    "s_barrier\n"
    "v_mfma_f32_16x16x32_bf16 v[100:103], v[80:83], a[52:55], v[100:103]\n"
    "ds_read_b128 a[64:67], v13 offset:16384\n"
    "ds_read_b128 a[68:71], v13 offset:18432\n"
    "ds_read_b64_tr_b16 v[64:65], v17 offset:32768\n"
    "ds_read_b64_tr_b16 v[66:67], v17 offset:33792\n"
    "v_mfma_f32_16x16x32_bf16 v[120:123], a[56:59], v[84:87], v[120:123]\n"
    "ds_read_b64_tr_b16 v[68:69], v17 offset:32800\n"
    "v_mfma_f32_16x16x32_bf16 v[124:127], a[60:63], v[84:87], v[124:127]\n"
    "ds_read_b64_tr_b16 v[70:71], v17 offset:33824\n"
    "s_waitcnt lgkmcnt(6)\n"
    "v_mfma_f32_16x16x32_bf16 v[128:131], a[56:59], v[88:91], v[128:131]\n"
    "ds_read_b128 v[72:75], v104 offset:512\n"
    "v_mfma_f32_16x16x32_bf16 v[132:135], a[60:63], v[88:91], v[132:135]\n"
    "v_mfma_f32_16x16x32_bf16 v[96:99], v[92:95], a[56:59], v[96:99]\n"
    "ds_read_b128 a[72:75], v14 offset:16384\n"
    "ds_read_b128 a[76:79], v14 offset:18432\n"
    "v_mfma_f32_16x16x32_bf16 v[100:103], v[92:95], a[60:63], v[100:103]\n"
    "ds_read_b64_tr_b16 v[76:77], v17 offset:36864\n"
    "ds_read_b64_tr_b16 v[78:79], v17 offset:37888\n"
    "s_waitcnt lgkmcnt(5)\n"
    "v_mfma_f32_16x16x32_bf16 v[120:123], a[64:67], v[64:67], v[120:123]\n"
    "ds_read_b64_tr_b16 v[80:81], v17 offset:36896\n"
    "buffer_load_dwordx4 v9, s[44:47], s76 offen offset:0 lds\n"
    "v_mfma_f32_16x16x32_bf16 v[124:127], a[68:71], v[64:67], v[124:127]\n"
    "ds_read_b64_tr_b16 v[82:83], v17 offset:37920\n"
    "v_mfma_f32_16x16x32_bf16 v[128:131], a[64:67], v[68:71], v[128:131]\n"
    "ds_read_b128 v[84:87], v104 offset:576\n"
    "buffer_load_dwordx4 v10, s[44:47], s76 offen offset:1024 lds\n"
    "v_mfma_f32_16x16x32_bf16 v[132:135], a[68:71], v[68:71], v[132:135]\n"
    "s_waitcnt lgkmcnt(3)\n"
    "v_mfma_f32_16x16x32_bf16 v[96:99], v[72:75], a[64:67], v[96:99]\n"
    "s_add_u32 m0, s67, 8192\n"
    "s_nop 0\n"
    "buffer_load_dwordx4 v5, s[40:43], s75 offen offset:0 lds\n"
    "buffer_load_dwordx4 v6, s[40:43], s75 offen offset:1024 lds\n"
    "buffer_load_dwordx4 v7, s[40:43], s75 offen offset:2048 lds\n"
    "buffer_load_dwordx4 v8, s[40:43], s75 offen offset:3072 lds\n"
    "s_add_u32 m0, s81, 57344\n"
    "s_mul_i32 s76, s75, 1376\n"
    "s_waitcnt vmcnt(10)\n"
    "s_barrier\n"
    "v_mfma_f32_16x16x32_bf16 v[100:103], v[72:75], a[68:71], v[100:103]\n"
    "ds_read_b128 a[80:83], v13 offset:0\n"
    "ds_read_b128 a[84:87], v13 offset:2048\n"
    "ds_read_b64_tr_b16 v[88:89], v17 offset:40960\n"
    "ds_read_b64_tr_b16 v[90:91], v17 offset:41984\n"
    "v_mfma_f32_16x16x32_bf16 v[120:123], a[72:75], v[76:79], v[120:123]\n"
    "ds_read_b64_tr_b16 v[92:93], v17 offset:40992\n"
    "v_mfma_f32_16x16x32_bf16 v[124:127], a[76:79], v[76:79], v[124:127]\n"
    "ds_read_b64_tr_b16 v[94:95], v17 offset:42016\n"
    "s_waitcnt lgkmcnt(6)\n"
    "v_mfma_f32_16x16x32_bf16 v[128:131], a[72:75], v[80:83], v[128:131]\n"
    "ds_read_b128 v[64:67], v104 offset:640\n"
    "v_mfma_f32_16x16x32_bf16 v[132:135], a[76:79], v[80:83], v[132:135]\n"
    "v_mfma_f32_16x16x32_bf16 v[96:99], v[84:87], a[72:75], v[96:99]\n"
    "ds_read_b128 a[88:91], v14 offset:0\n"
    "ds_read_b128 a[92:95], v14 offset:2048\n"
    "v_mfma_f32_16x16x32_bf16 v[100:103], v[84:87], a[76:79], v[100:103]\n"
    "ds_read_b64_tr_b16 v[68:69], v17 offset:45056\n"
    "ds_read_b64_tr_b16 v[70:71], v17 offset:46080\n"
    "s_waitcnt lgkmcnt(5)\n"
    "v_mfma_f32_16x16x32_bf16 v[120:123], a[80:83], v[88:91], v[120:123]\n"
    "ds_read_b64_tr_b16 v[72:73], v17 offset:45088\n"
    "buffer_load_dwordx4 v9, s[44:47], s76 offen offset:0 lds\n"
    "v_mfma_f32_16x16x32_bf16 v[124:127], a[84:87], v[88:91], v[124:127]\n"
    "ds_read_b64_tr_b16 v[74:75], v17 offset:46112\n"
    "v_mfma_f32_16x16x32_bf16 v[128:131], a[80:83], v[92:95], v[128:131]\n"
    "ds_read_b128 v[76:79], v104 offset:704\n"
    "buffer_load_dwordx4 v10, s[44:47], s76 offen offset:1024 lds\n"
    "v_mfma_f32_16x16x32_bf16 v[132:135], a[84:87], v[92:95], v[132:135]\n"
    "s_waitcnt lgkmcnt(3)\n"
    "v_mfma_f32_16x16x32_bf16 v[96:99], v[64:67], a[80:83], v[96:99]\n"
    "s_add_u32 m0, s81, 0\n"
    "s_nop 0\n"
    "buffer_load_dwordx4 v11, s[48:51], s68 offen offset:0 lds\n"
    "buffer_load_dwordx4 v12, s[48:51], s68 offen offset:1024 lds\n"
    "s_add_u32 m0, s81, 8192\n"
    "s_nop 0\n"
    "buffer_load_dwordx4 v11, s[48:51], s69 offen offset:0 lds\n"
    "buffer_load_dwordx4 v12, s[48:51], s69 offen offset:1024 lds\n"
    "s_add_u32 m0, s81, 16384\n"
    "s_waitcnt vmcnt(10)\n"
    "s_barrier\n"
    "v_mfma_f32_16x16x32_bf16 v[100:103], v[64:67], a[84:87], v[100:103]\n"
    "ds_read_b128 a[96:99], v13 offset:4096\n"
    "ds_read_b128 a[100:103], v13 offset:6144\n"
    "ds_read_b64_tr_b16 v[80:81], v17 offset:49152\n"
    "ds_read_b64_tr_b16 v[82:83], v17 offset:50176\n"
    "v_mfma_f32_16x16x32_bf16 v[120:123], a[88:91], v[68:71], v[120:123]\n"
    "ds_read_b64_tr_b16 v[84:85], v17 offset:49184\n"
    "v_mfma_f32_16x16x32_bf16 v[124:127], a[92:95], v[68:71], v[124:127]\n"
    "ds_read_b64_tr_b16 v[86:87], v17 offset:50208\n"
    "s_waitcnt lgkmcnt(6)\n"
    "v_mfma_f32_16x16x32_bf16 v[128:131], a[88:91], v[72:75], v[128:131]\n"
    "ds_read_b128 v[88:91], v104 offset:768\n"
    "v_mfma_f32_16x16x32_bf16 v[132:135], a[92:95], v[72:75], v[132:135]\n"
    "v_mfma_f32_16x16x32_bf16 v[96:99], v[76:79], a[88:91], v[96:99]\n"
    "ds_read_b128 a[104:107], v14 offset:4096\n"
    "ds_read_b128 a[108:111], v14 offset:6144\n"
    "v_mfma_f32_16x16x32_bf16 v[100:103], v[76:79], a[92:95], v[100:103]\n"
    "ds_read_b64_tr_b16 v[92:93], v17 offset:53248\n"
    "ds_read_b64_tr_b16 v[94:95], v17 offset:54272\n"
    "s_waitcnt lgkmcnt(5)\n"
    "v_mfma_f32_16x16x32_bf16 v[120:123], a[96:99], v[80:83], v[120:123]\n"
    "ds_read_b64_tr_b16 v[64:65], v17 offset:53280\n"
    "buffer_load_dwordx4 v11, s[48:51], s70 offen offset:0 lds\n"
    "v_mfma_f32_16x16x32_bf16 v[124:127], a[100:103], v[80:83], v[124:127]\n"
    "ds_read_b64_tr_b16 v[66:67], v17 offset:54304\n"
    "v_mfma_f32_16x16x32_bf16 v[128:131], a[96:99], v[84:87], v[128:131]\n"
    "ds_read_b128 v[68:71], v104 offset:832\n"
    "buffer_load_dwordx4 v12, s[48:51], s70 offen offset:1024 lds\n"
    "v_mfma_f32_16x16x32_bf16 v[132:135], a[100:103], v[84:87], v[132:135]\n"
    "s_waitcnt lgkmcnt(3)\n"
    "v_mfma_f32_16x16x32_bf16 v[96:99], v[88:91], a[96:99], v[96:99]\n"
    "s_add_u32 m0, s81, 24576\n"
    "s_nop 0\n"
    "buffer_load_dwordx4 v11, s[48:51], s71 offen offset:0 lds\n"
    "buffer_load_dwordx4 v12, s[48:51], s71 offen offset:1024 lds\n"
    "s_add_u32 m0, s81, 32768\n"
    "s_nop 0\n"
    "buffer_load_dwordx4 v11, s[48:51], s72 offen offset:0 lds\n"
    "buffer_load_dwordx4 v12, s[48:51], s72 offen offset:1024 lds\n"
    "s_waitcnt vmcnt(10)\n"
    "s_barrier\n"
    "v_mfma_f32_16x16x32_bf16 v[100:103], v[88:91], a[100:103], v[100:103]\n"
    "ds_read_b128 a[112:115], v13 offset:8192\n"
    "ds_read_b128 a[116:119], v13 offset:10240\n"
    "ds_read_b64_tr_b16 v[72:73], v17 offset:57344\n"
    "ds_read_b64_tr_b16 v[74:75], v17 offset:58368\n"
    "v_mfma_f32_16x16x32_bf16 v[120:123], a[104:107], v[92:95], v[120:123]\n"
    "ds_read_b64_tr_b16 v[76:77], v17 offset:57376\n"
    "v_mfma_f32_16x16x32_bf16 v[124:127], a[108:111], v[92:95], v[124:127]\n"
    "ds_read_b64_tr_b16 v[78:79], v17 offset:58400\n"
    "s_waitcnt lgkmcnt(6)\n"
    "v_mfma_f32_16x16x32_bf16 v[128:131], a[104:107], v[64:67], v[128:131]\n"
    "ds_read_b128 v[80:83], v104 offset:896\n"
    "v_mfma_f32_16x16x32_bf16 v[132:135], a[108:111], v[64:67], v[132:135]\n"
    "v_mfma_f32_16x16x32_bf16 v[96:99], v[68:71], a[104:107], v[96:99]\n"
    "ds_read_b128 a[120:123], v14 offset:8192\n"
    "ds_read_b128 a[124:127], v14 offset:10240\n"
    "v_mfma_f32_16x16x32_bf16 v[100:103], v[68:71], a[108:111], v[100:103]\n"
    "ds_read_b64_tr_b16 v[84:85], v17 offset:61440\n"
    "ds_read_b64_tr_b16 v[86:87], v17 offset:62464\n"
    "s_waitcnt lgkmcnt(5)\n"
    "v_mfma_f32_16x16x32_bf16 v[120:123], a[112:115], v[72:75], v[120:123]\n"
    "ds_read_b64_tr_b16 v[88:89], v17 offset:61472\n"
    "v_mfma_f32_16x16x32_bf16 v[124:127], a[116:119], v[72:75], v[124:127]\n"
    "ds_read_b64_tr_b16 v[90:91], v17 offset:62496\n"
    "v_mfma_f32_16x16x32_bf16 v[128:131], a[112:115], v[76:79], v[128:131]\n"
    "ds_read_b128 v[92:95], v104 offset:960\n"
    "v_mfma_f32_16x16x32_bf16 v[132:135], a[116:119], v[76:79], v[132:135]\n"
    "s_waitcnt lgkmcnt(3)\n"
    "v_mfma_f32_16x16x32_bf16 v[96:99], v[80:83], a[112:115], v[96:99]\n"
    "v_mfma_f32_16x16x32_bf16 v[100:103], v[80:83], a[116:119], v[100:103]\n"
    "v_mfma_f32_16x16x32_bf16 v[120:123], a[120:123], v[84:87], v[120:123]\n"
    "v_mfma_f32_16x16x32_bf16 v[124:127], a[124:127], v[84:87], v[124:127]\n"
    "s_waitcnt lgkmcnt(0)\n"
    "v_mfma_f32_16x16x32_bf16 v[128:131], a[120:123], v[88:91], v[128:131]\n"
    "v_mfma_f32_16x16x32_bf16 v[132:135], a[124:127], v[88:91], v[132:135]\n"
    "v_mfma_f32_16x16x32_bf16 v[96:99], v[92:95], a[120:123], v[96:99]\n"
    "v_mfma_f32_16x16x32_bf16 v[100:103], v[92:95], a[124:127], v[100:103]\n"
    "s_nop 1\n"
    "v_pk_mul_f32 v[220:221], v[120:121], v[120:121]\n"
    "v_pk_mul_f32 v[222:223], v[122:123], v[122:123]\n"
    "v_pk_mul_f32 v[224:225], v[124:125], v[124:125]\n"
    "v_pk_mul_f32 v[226:227], v[126:127], v[126:127]\n"
    "v_pk_fma_f32 v[220:221], v[220:221], s[18:19], s[18:19] op_sel:[0,0,1] op_sel_hi:[1,0,1]\n"
    "v_pk_fma_f32 v[222:223], v[222:223], s[18:19], s[18:19] op_sel:[0,0,1] op_sel_hi:[1,0,1]\n"
    "v_pk_fma_f32 v[224:225], v[224:225], s[18:19], s[18:19] op_sel:[0,0,1] op_sel_hi:[1,0,1]\n"
    "v_pk_fma_f32 v[226:227], v[226:227], s[18:19], s[18:19] op_sel:[0,0,1] op_sel_hi:[1,0,1]\n"
    "v_pk_mul_f32 v[220:221], v[220:221], v[120:121]\n"
    "v_pk_mul_f32 v[222:223], v[222:223], v[122:123]\n"
    "v_pk_mul_f32 v[224:225], v[224:225], v[124:125]\n"
    "v_pk_mul_f32 v[226:227], v[226:227], v[126:127]\n"
    "v_exp_f32 v220, v220\n"
    "v_exp_f32 v221, v221\n"
    "v_exp_f32 v222, v222\n"
    "v_exp_f32 v223, v223\n"
    "v_exp_f32 v224, v224\n"
    "v_exp_f32 v225, v225\n"
    "v_exp_f32 v226, v226\n"
    "v_exp_f32 v227, v227\n"
    "v_pk_add_f32 v[220:221], v[220:221], 1.0 op_sel_hi:[1,0]\n"
    "v_pk_add_f32 v[222:223], v[222:223], 1.0 op_sel_hi:[1,0]\n"
    "v_pk_add_f32 v[224:225], v[224:225], 1.0 op_sel_hi:[1,0]\n"
    "v_pk_add_f32 v[226:227], v[226:227], 1.0 op_sel_hi:[1,0]\n"
    "v_rcp_f32 v220, v220\n"
    "v_rcp_f32 v221, v221\n"
    "v_rcp_f32 v222, v222\n"
    "v_rcp_f32 v223, v223\n"
    "v_rcp_f32 v224, v224\n"
    "v_rcp_f32 v225, v225\n"
    "v_rcp_f32 v226, v226\n"
    "v_rcp_f32 v227, v227\n"
    "v_pk_mul_f32 v[120:121], v[220:221], v[120:121]\n"
    "v_pk_mul_f32 v[122:123], v[222:223], v[122:123]\n"
    "v_pk_mul_f32 v[124:125], v[224:225], v[124:125]\n"
    "v_pk_mul_f32 v[126:127], v[226:227], v[126:127]\n"
    "v_pk_mul_f32 v[220:221], v[128:129], v[128:129]\n"
    "v_pk_mul_f32 v[222:223], v[130:131], v[130:131]\n"
    "v_pk_mul_f32 v[224:225], v[132:133], v[132:133]\n"
    "v_pk_mul_f32 v[226:227], v[134:135], v[134:135]\n"
    "v_pk_fma_f32 v[220:221], v[220:221], s[18:19], s[18:19] op_sel:[0,0,1] op_sel_hi:[1,0,1]\n"
    "v_pk_fma_f32 v[222:223], v[222:223], s[18:19], s[18:19] op_sel:[0,0,1] op_sel_hi:[1,0,1]\n"
    "v_pk_fma_f32 v[224:225], v[224:225], s[18:19], s[18:19] op_sel:[0,0,1] op_sel_hi:[1,0,1]\n"
    "v_pk_fma_f32 v[226:227], v[226:227], s[18:19], s[18:19] op_sel:[0,0,1] op_sel_hi:[1,0,1]\n"
    "v_pk_mul_f32 v[220:221], v[220:221], v[128:129]\n"
    "v_pk_mul_f32 v[222:223], v[222:223], v[130:131]\n"
    "v_pk_mul_f32 v[224:225], v[224:225], v[132:133]\n"
    "v_pk_mul_f32 v[226:227], v[226:227], v[134:135]\n"
    "v_exp_f32 v220, v220\n"
    "v_exp_f32 v221, v221\n"
    "v_exp_f32 v222, v222\n"
    "v_exp_f32 v223, v223\n"
    "v_exp_f32 v224, v224\n"
    "v_exp_f32 v225, v225\n"
    "v_exp_f32 v226, v226\n"
    "v_exp_f32 v227, v227\n"
    "v_pk_add_f32 v[220:221], v[220:221], 1.0 op_sel_hi:[1,0]\n"
    "v_pk_add_f32 v[222:223], v[222:223], 1.0 op_sel_hi:[1,0]\n"
    "v_pk_add_f32 v[224:225], v[224:225], 1.0 op_sel_hi:[1,0]\n"
    "v_pk_add_f32 v[226:227], v[226:227], 1.0 op_sel_hi:[1,0]\n"
    "v_rcp_f32 v220, v220\n"
    "v_rcp_f32 v221, v221\n"
    "v_rcp_f32 v222, v222\n"
    "v_rcp_f32 v223, v223\n"
    "v_rcp_f32 v224, v224\n"
    "v_rcp_f32 v225, v225\n"
    "v_rcp_f32 v226, v226\n"
    "v_rcp_f32 v227, v227\n"
    "v_pk_mul_f32 v[128:129], v[220:221], v[128:129]\n"
    "v_pk_mul_f32 v[130:131], v[222:223], v[130:131]\n"
    "v_pk_mul_f32 v[132:133], v[224:225], v[132:133]\n"
    "v_pk_mul_f32 v[134:135], v[226:227], v[134:135]\n"
    "v_pk_mul_f32 v[236:237], v[120:121], v[120:121]\n"
    "v_pk_add_f32 v[228:229], v[120:121], v[128:129]\n"
    "v_pk_mul_f32 v[238:239], v[122:123], v[122:123]\n"
    "v_pk_add_f32 v[230:231], v[122:123], v[130:131]\n"
    "v_pk_mul_f32 v[240:241], v[124:125], v[124:125]\n"
    "v_pk_add_f32 v[232:233], v[124:125], v[132:133]\n"
    "v_pk_mul_f32 v[242:243], v[126:127], v[126:127]\n"
    "v_pk_add_f32 v[234:235], v[126:127], v[134:135]\n"
    "v_pk_fma_f32 v[236:237], v[128:129], v[128:129], v[236:237]\n"
    "v_pk_fma_f32 v[238:239], v[130:131], v[130:131], v[238:239]\n"
    "v_pk_fma_f32 v[240:241], v[132:133], v[132:133], v[240:241]\n"
    "v_pk_fma_f32 v[242:243], v[134:135], v[134:135], v[242:243]\n"
    "v_add_f32_dpp v32, v228, v228 row_mirror row_mask:0xf bank_mask:0x3\n"
    "v_add_f32_dpp v33, v229, v229 row_mirror row_mask:0xf bank_mask:0x3\n"
    "v_add_f32_dpp v34, v230, v230 row_mirror row_mask:0xf bank_mask:0x3\n"
    "v_add_f32_dpp v35, v231, v231 row_mirror row_mask:0xf bank_mask:0x3\n"
    "v_add_f32_dpp v36, v232, v232 row_mirror row_mask:0xf bank_mask:0x3\n"
    "v_add_f32_dpp v37, v233, v233 row_mirror row_mask:0xf bank_mask:0x3\n"
    "v_add_f32_dpp v38, v234, v234 row_mirror row_mask:0xf bank_mask:0x3\n"
    "v_add_f32_dpp v39, v235, v235 row_mirror row_mask:0xf bank_mask:0x3\n"
    "v_add_f32_dpp v32, v236, v236 row_mirror row_mask:0xf bank_mask:0xc\n"
    "v_add_f32_dpp v33, v237, v237 row_mirror row_mask:0xf bank_mask:0xc\n"
    "v_add_f32_dpp v34, v238, v238 row_mirror row_mask:0xf bank_mask:0xc\n"
    "v_add_f32_dpp v35, v239, v239 row_mirror row_mask:0xf bank_mask:0xc\n"
    "v_add_f32_dpp v36, v240, v240 row_mirror row_mask:0xf bank_mask:0xc\n"
    "v_add_f32_dpp v37, v241, v241 row_mirror row_mask:0xf bank_mask:0xc\n"
    "v_add_f32_dpp v38, v242, v242 row_mirror row_mask:0xf bank_mask:0xc\n"
    "v_add_f32_dpp v39, v243, v243 row_mirror row_mask:0xf bank_mask:0xc\n"
    "v_add_f32_dpp v40, v32, v32 row_half_mirror row_mask:0xf bank_mask:0x5\n"
    "v_add_f32_dpp v41, v33, v33 row_half_mirror row_mask:0xf bank_mask:0x5\n"
    "v_add_f32_dpp v42, v34, v34 row_half_mirror row_mask:0xf bank_mask:0x5\n"
    "v_add_f32_dpp v43, v35, v35 row_half_mirror row_mask:0xf bank_mask:0x5\n"
    "v_add_f32_dpp v40, v36, v36 row_half_mirror row_mask:0xf bank_mask:0xa\n"
    "v_add_f32_dpp v41, v37, v37 row_half_mirror row_mask:0xf bank_mask:0xa\n"
    "v_add_f32_dpp v42, v38, v38 row_half_mirror row_mask:0xf bank_mask:0xa\n"
    "v_add_f32_dpp v43, v39, v39 row_half_mirror row_mask:0xf bank_mask:0xa\n"
    "v_add_f32_dpp v32, v40, v40 quad_perm:[2,3,0,1] row_mask:0xf bank_mask:0xf\n"
    "v_add_f32_dpp v33, v41, v41 quad_perm:[2,3,0,1] row_mask:0xf bank_mask:0xf\n"
    "v_add_f32_dpp v34, v42, v42 quad_perm:[2,3,0,1] row_mask:0xf bank_mask:0xf\n"
    "v_add_f32_dpp v35, v43, v43 quad_perm:[2,3,0,1] row_mask:0xf bank_mask:0xf\n"
    "v_cndmask_b32 v40, v32, v34, s[20:21]\n"
    "v_cndmask_b32 v41, v33, v35, s[20:21]\n"
    "s_nop 0\n"
    "v_add_f32_dpp v32, v40, v40 quad_perm:[1,0,3,2] row_mask:0xf bank_mask:0xf\n"
    "v_add_f32_dpp v33, v41, v41 quad_perm:[1,0,3,2] row_mask:0xf bank_mask:0xf\n"
    "v_cndmask_b32 v40, v32, v33, s[22:23]\n"
    "v_mov_b32 v41, s64\n"
    "buffer_store_dwordx2 v[40:41], v24, s[56:59], s95 offen sc1\n"
    "v_cndmask_b32 v96, v96, v100, s[16:17]\n"
    "s_add_u32 m0, s81, 40960\n"
    "v_mul_f32 v96, 0xbfb8aa3b, v96\n"
    "v_exp_f32 v96, v96\n"
    "s_nop 0\n"
    "v_add_f32 v96, 1.0, v96\n"
    "buffer_load_dwordx4 v11, s[48:51], s73 offen offset:0 lds\n"
    "v_rcp_f32 v96, v96\n"
    "buffer_store_dword v96, v105, s[56:59], 0 offen sc1\n"
    "buffer_load_dwordx4 v12, s[48:51], s73 offen offset:1024 lds\n"
    "s_waitcnt vmcnt(12)\n"
    "s_barrier\n"
    "ds_read_b128 v[64:67], v15 offset:0\n"
    "ds_read_b128 v[68:71], v15 offset:2048\n"
    "ds_read_b128 v[72:75], v15 offset:4096\n"
    "ds_read_b128 v[76:79], v15 offset:6144\n"
    "ds_read_b128 v[80:83], v16 offset:0\n"
    "ds_read_b128 v[84:87], v16 offset:2048\n"
    "s_waitcnt lgkmcnt(4)\n"
    "v_mfma_f32_16x16x32_bf16 v[152:155], a[0:3], v[64:67], 0\n"
    "ds_read_b128 v[88:91], v16 offset:4096\n"
    "s_add_u32 m0, s81, 49152\n"
    "v_mfma_f32_16x16x32_bf16 v[156:159], a[4:7], v[64:67], 0\n"
    "buffer_load_dwordx4 v11, s[48:51], s74 offen offset:0 lds\n"
    "v_mfma_f32_16x16x32_bf16 v[160:163], a[0:3], v[68:71], 0\n"
    "ds_read_b128 v[92:95], v16 offset:6144\n"
    "buffer_load_dwordx4 v12, s[48:51], s74 offen offset:1024 lds\n"
    "v_mfma_f32_16x16x32_bf16 v[164:167], a[4:7], v[68:71], 0\n"
    "s_add_u32 m0, s81, 57344\n"
    "s_nop 0\n"
    "buffer_load_dwordx4 v11, s[48:51], s75 offen offset:0 lds\n"
    "s_waitcnt lgkmcnt(4)\n"
    "v_mfma_f32_16x16x32_bf16 v[168:171], a[0:3], v[72:75], 0\n"
    "buffer_load_dwordx4 v12, s[48:51], s75 offen offset:1024 lds\n"
    "s_waitcnt vmcnt(14)\n"
    "s_barrier\n"
    "v_mfma_f32_16x16x32_bf16 v[172:175], a[4:7], v[72:75], 0\n"
    "ds_read_b128 v[64:67], v15 offset:8192\n"
    "s_lshl1_add_u32 s77, s63, s62\n"
    "v_mfma_f32_16x16x32_bf16 v[176:179], a[0:3], v[76:79], 0\n"
    "ds_read_b128 v[68:71], v15 offset:10240\n"
    "s_sub_u32 s77, s77, 6\n"
    "v_mfma_f32_16x16x32_bf16 v[180:183], a[4:7], v[76:79], 0\n"
    "s_lshl2_add_u32 s83, s77, s60\n"
    "s_lshl_b32 s77, s83, 15\n"
    "s_waitcnt lgkmcnt(4)\n"
    "v_mfma_f32_16x16x32_bf16 v[152:155], a[8:11], v[80:83], v[152:155]\n"
    "ds_read_b128 v[72:75], v15 offset:12288\n"
    "v_lshl_add_u32 v5, v26, 10, v28\n"
    "v_mfma_f32_16x16x32_bf16 v[156:159], a[12:15], v[80:83], v[156:159]\n"
    "v_add_u32 v5, s77, v5\n"
    "v_xor_b32 v8, 64, v5\n"
    "v_mfma_f32_16x16x32_bf16 v[160:163], a[8:11], v[84:87], v[160:163]\n"
    "ds_read_b128 v[76:79], v15 offset:14336\n"
    "v_add_u32 v6, 7168, v8\n"
    "v_mfma_f32_16x16x32_bf16 v[164:167], a[12:15], v[84:87], v[164:167]\n"
    "v_add_u32 v7, 14336, v5\n"
    "v_add_u32 v8, 21504, v8\n"
    "s_waitcnt lgkmcnt(4)\n"
    "v_mfma_f32_16x16x32_bf16 v[168:171], a[8:11], v[88:91], v[168:171]\n"
    "ds_read_b128 v[80:83], v16 offset:8192\n"
    "s_add_u32 m0, s67, 12288\n"
    "v_mfma_f32_16x16x32_bf16 v[172:175], a[12:15], v[88:91], v[172:175]\n"
    "buffer_load_dwordx4 v5, s[52:55], s68 offen offset:0 lds\n"
    "v_mfma_f32_16x16x32_bf16 v[176:179], a[8:11], v[92:95], v[176:179]\n"
    "ds_read_b128 v[84:87], v16 offset:10240\n"
    "buffer_load_dwordx4 v6, s[52:55], s68 offen offset:1024 lds\n"
    "v_mfma_f32_16x16x32_bf16 v[180:183], a[12:15], v[92:95], v[180:183]\n"
    "s_waitcnt lgkmcnt(4)\n"
    "v_mfma_f32_16x16x32_bf16 v[152:155], a[16:19], v[64:67], v[152:155]\n"
    "ds_read_b128 v[88:91], v16 offset:12288\n"
    "buffer_load_dwordx4 v7, s[52:55], s68 offen offset:2048 lds\n"
    "v_mfma_f32_16x16x32_bf16 v[156:159], a[20:23], v[64:67], v[156:159]\n"
    "v_mfma_f32_16x16x32_bf16 v[160:163], a[16:19], v[68:71], v[160:163]\n"
    "ds_read_b128 v[92:95], v16 offset:14336\n"
    "v_mfma_f32_16x16x32_bf16 v[164:167], a[20:23], v[68:71], v[164:167]\n"
    "s_waitcnt lgkmcnt(4)\n"
    "v_mfma_f32_16x16x32_bf16 v[168:171], a[16:19], v[72:75], v[168:171]\n"
    "buffer_load_dwordx4 v8, s[52:55], s68 offen offset:3072 lds\n"
    "s_waitcnt vmcnt(16)\n"
    "s_barrier\n"
    "v_mfma_f32_16x16x32_bf16 v[172:175], a[20:23], v[72:75], v[172:175]\n"
    "ds_read_b128 v[64:67], v15 offset:16384\n"
    "v_mfma_f32_16x16x32_bf16 v[176:179], a[16:19], v[76:79], v[176:179]\n"
    "ds_read_b128 v[68:71], v15 offset:18432\n"
    "v_mfma_f32_16x16x32_bf16 v[180:183], a[20:23], v[76:79], v[180:183]\n"
    "s_waitcnt lgkmcnt(4)\n"
    "v_mfma_f32_16x16x32_bf16 v[152:155], a[24:27], v[80:83], v[152:155]\n"
    "ds_read_b128 v[72:75], v15 offset:20480\n"
    "v_mfma_f32_16x16x32_bf16 v[156:159], a[28:31], v[80:83], v[156:159]\n"
    "v_mfma_f32_16x16x32_bf16 v[160:163], a[24:27], v[84:87], v[160:163]\n"
    "ds_read_b128 v[76:79], v15 offset:22528\n"
    "v_mfma_f32_16x16x32_bf16 v[164:167], a[28:31], v[84:87], v[164:167]\n"
    "s_waitcnt lgkmcnt(4)\n"
    "v_mfma_f32_16x16x32_bf16 v[168:171], a[24:27], v[88:91], v[168:171]\n"
    "ds_read_b128 v[80:83], v16 offset:16384\n"
    "v_mfma_f32_16x16x32_bf16 v[172:175], a[28:31], v[88:91], v[172:175]\n"
    "v_mfma_f32_16x16x32_bf16 v[176:179], a[24:27], v[92:95], v[176:179]\n"
    "ds_read_b128 v[84:87], v16 offset:18432\n"
    "v_mfma_f32_16x16x32_bf16 v[180:183], a[28:31], v[92:95], v[180:183]\n"
    "s_waitcnt lgkmcnt(4)\n"
    "v_mfma_f32_16x16x32_bf16 v[152:155], a[32:35], v[64:67], v[152:155]\n"
    "ds_read_b128 v[88:91], v16 offset:20480\n"
    "v_mfma_f32_16x16x32_bf16 v[156:159], a[36:39], v[64:67], v[156:159]\n"
    "v_mfma_f32_16x16x32_bf16 v[160:163], a[32:35], v[68:71], v[160:163]\n"
    "ds_read_b128 v[92:95], v16 offset:22528\n"
    "v_mfma_f32_16x16x32_bf16 v[164:167], a[36:39], v[68:71], v[164:167]\n"
    "s_waitcnt lgkmcnt(4)\n"
    "v_mfma_f32_16x16x32_bf16 v[168:171], a[32:35], v[72:75], v[168:171]\n"
    "s_waitcnt vmcnt(14)\n"
    "s_barrier\n"
    "v_mfma_f32_16x16x32_bf16 v[172:175], a[36:39], v[72:75], v[172:175]\n"
    "ds_read_b128 v[64:67], v15 offset:24576\n"
    "v_mfma_f32_16x16x32_bf16 v[176:179], a[32:35], v[76:79], v[176:179]\n"
    "ds_read_b128 v[68:71], v15 offset:26624\n"
    "v_mfma_f32_16x16x32_bf16 v[180:183], a[36:39], v[76:79], v[180:183]\n"
    "s_waitcnt lgkmcnt(4)\n"
    "v_mfma_f32_16x16x32_bf16 v[152:155], a[40:43], v[80:83], v[152:155]\n"
    "ds_read_b128 v[72:75], v15 offset:28672\n"
    "v_mfma_f32_16x16x32_bf16 v[156:159], a[44:47], v[80:83], v[156:159]\n"
    "v_mfma_f32_16x16x32_bf16 v[160:163], a[40:43], v[84:87], v[160:163]\n"
    "ds_read_b128 v[76:79], v15 offset:30720\n"
    "s_add_u32 m0, s67, 16384\n"
    "v_mfma_f32_16x16x32_bf16 v[164:167], a[44:47], v[84:87], v[164:167]\n"
    "buffer_load_dwordx4 v5, s[52:55], s69 offen offset:0 lds\n"
    "s_waitcnt lgkmcnt(4)\n"
    "v_mfma_f32_16x16x32_bf16 v[168:171], a[40:43], v[88:91], v[168:171]\n"
    "ds_read_b128 v[80:83], v16 offset:24576\n"
    "v_mfma_f32_16x16x32_bf16 v[172:175], a[44:47], v[88:91], v[172:175]\n"
    "v_mfma_f32_16x16x32_bf16 v[176:179], a[40:43], v[92:95], v[176:179]\n"
    "ds_read_b128 v[84:87], v16 offset:26624\n"
    "v_mfma_f32_16x16x32_bf16 v[180:183], a[44:47], v[92:95], v[180:183]\n"
    "s_waitcnt lgkmcnt(4)\n"
    "v_mfma_f32_16x16x32_bf16 v[152:155], a[48:51], v[64:67], v[152:155]\n"
    "ds_read_b128 v[88:91], v16 offset:28672\n"
    "buffer_load_dwordx4 v6, s[52:55], s69 offen offset:1024 lds\n"
    "v_mfma_f32_16x16x32_bf16 v[156:159], a[52:55], v[64:67], v[156:159]\n"
    "v_mfma_f32_16x16x32_bf16 v[160:163], a[48:51], v[68:71], v[160:163]\n"
    "ds_read_b128 v[92:95], v16 offset:30720\n"
    "v_mfma_f32_16x16x32_bf16 v[164:167], a[52:55], v[68:71], v[164:167]\n"
    "s_waitcnt lgkmcnt(4)\n"
    "v_mfma_f32_16x16x32_bf16 v[168:171], a[48:51], v[72:75], v[168:171]\n"
    "buffer_load_dwordx4 v7, s[52:55], s69 offen offset:2048 lds\n"
    "buffer_load_dwordx4 v8, s[52:55], s69 offen offset:3072 lds\n"
    "s_waitcnt vmcnt(16)\n"
    "s_barrier\n"
    "v_mfma_f32_16x16x32_bf16 v[172:175], a[52:55], v[72:75], v[172:175]\n"
    "ds_read_b128 v[64:67], v15 offset:32768\n"
    "v_mfma_f32_16x16x32_bf16 v[176:179], a[48:51], v[76:79], v[176:179]\n"
    "ds_read_b128 v[68:71], v15 offset:34816\n"
    "v_mfma_f32_16x16x32_bf16 v[180:183], a[52:55], v[76:79], v[180:183]\n"
    "s_waitcnt lgkmcnt(4)\n"
    "v_mfma_f32_16x16x32_bf16 v[152:155], a[56:59], v[80:83], v[152:155]\n"
    "ds_read_b128 v[72:75], v15 offset:36864\n"
    "v_mfma_f32_16x16x32_bf16 v[156:159], a[60:63], v[80:83], v[156:159]\n"
    "v_mfma_f32_16x16x32_bf16 v[160:163], a[56:59], v[84:87], v[160:163]\n"
    "ds_read_b128 v[76:79], v15 offset:38912\n"
    "v_mfma_f32_16x16x32_bf16 v[164:167], a[60:63], v[84:87], v[164:167]\n"
    "s_waitcnt lgkmcnt(4)\n"
    "v_mfma_f32_16x16x32_bf16 v[168:171], a[56:59], v[88:91], v[168:171]\n"
    "ds_read_b128 v[80:83], v16 offset:32768\n"
    "v_mfma_f32_16x16x32_bf16 v[172:175], a[60:63], v[88:91], v[172:175]\n"
    "v_mfma_f32_16x16x32_bf16 v[176:179], a[56:59], v[92:95], v[176:179]\n"
    "ds_read_b128 v[84:87], v16 offset:34816\n"
    "v_mfma_f32_16x16x32_bf16 v[180:183], a[60:63], v[92:95], v[180:183]\n"
    "s_waitcnt lgkmcnt(4)\n"
    "v_mfma_f32_16x16x32_bf16 v[152:155], a[64:67], v[64:67], v[152:155]\n"
    "ds_read_b128 v[88:91], v16 offset:36864\n"
    "v_mfma_f32_16x16x32_bf16 v[156:159], a[68:71], v[64:67], v[156:159]\n"
    "v_mfma_f32_16x16x32_bf16 v[160:163], a[64:67], v[68:71], v[160:163]\n"
    "ds_read_b128 v[92:95], v16 offset:38912\n"
    "v_mfma_f32_16x16x32_bf16 v[164:167], a[68:71], v[68:71], v[164:167]\n"
    "s_waitcnt lgkmcnt(4)\n"
    "v_mfma_f32_16x16x32_bf16 v[168:171], a[64:67], v[72:75], v[168:171]\n"
    "s_waitcnt vmcnt(12)\n"
    "s_barrier\n"
    "v_mfma_f32_16x16x32_bf16 v[172:175], a[68:71], v[72:75], v[172:175]\n"
    "ds_read_b128 v[64:67], v15 offset:40960\n"
    "v_mfma_f32_16x16x32_bf16 v[176:179], a[64:67], v[76:79], v[176:179]\n"
    "ds_read_b128 v[68:71], v15 offset:43008\n"
    "v_mfma_f32_16x16x32_bf16 v[180:183], a[68:71], v[76:79], v[180:183]\n"
    "s_waitcnt lgkmcnt(4)\n"
    "v_mfma_f32_16x16x32_bf16 v[152:155], a[72:75], v[80:83], v[152:155]\n"
    "ds_read_b128 v[72:75], v15 offset:45056\n"
    "v_mfma_f32_16x16x32_bf16 v[156:159], a[76:79], v[80:83], v[156:159]\n"
    "v_mfma_f32_16x16x32_bf16 v[160:163], a[72:75], v[84:87], v[160:163]\n"
    "ds_read_b128 v[76:79], v15 offset:47104\n"
    "v_mfma_f32_16x16x32_bf16 v[164:167], a[76:79], v[84:87], v[164:167]\n"
    "s_waitcnt lgkmcnt(4)\n"
    "v_mfma_f32_16x16x32_bf16 v[168:171], a[72:75], v[88:91], v[168:171]\n"
    "ds_read_b128 v[80:83], v16 offset:40960\n"
    "v_mfma_f32_16x16x32_bf16 v[172:175], a[76:79], v[88:91], v[172:175]\n"
    "v_mfma_f32_16x16x32_bf16 v[176:179], a[72:75], v[92:95], v[176:179]\n"
    "ds_read_b128 v[84:87], v16 offset:43008\n"
    "v_mfma_f32_16x16x32_bf16 v[180:183], a[76:79], v[92:95], v[180:183]\n"
    "s_waitcnt lgkmcnt(4)\n"
    "v_mfma_f32_16x16x32_bf16 v[152:155], a[80:83], v[64:67], v[152:155]\n"
    "ds_read_b128 v[88:91], v16 offset:45056\n"
    "v_mfma_f32_16x16x32_bf16 v[156:159], a[84:87], v[64:67], v[156:159]\n"
    "v_mfma_f32_16x16x32_bf16 v[160:163], a[80:83], v[68:71], v[160:163]\n"
    "ds_read_b128 v[92:95], v16 offset:47104\n"
    "v_mfma_f32_16x16x32_bf16 v[164:167], a[84:87], v[68:71], v[164:167]\n"
    "s_waitcnt lgkmcnt(4)\n"
    "v_mfma_f32_16x16x32_bf16 v[168:171], a[80:83], v[72:75], v[168:171]\n"
    "s_waitcnt vmcnt(10)\n"
    "s_barrier\n"
    "v_mfma_f32_16x16x32_bf16 v[172:175], a[84:87], v[72:75], v[172:175]\n"
    "ds_read_b128 v[64:67], v15 offset:49152\n"
    "v_mfma_f32_16x16x32_bf16 v[176:179], a[80:83], v[76:79], v[176:179]\n"
    "ds_read_b128 v[68:71], v15 offset:51200\n"
    "v_mfma_f32_16x16x32_bf16 v[180:183], a[84:87], v[76:79], v[180:183]\n"
    "s_waitcnt lgkmcnt(4)\n"
    "v_mfma_f32_16x16x32_bf16 v[152:155], a[88:91], v[80:83], v[152:155]\n"
    "ds_read_b128 v[72:75], v15 offset:53248\n"
    "v_mfma_f32_16x16x32_bf16 v[156:159], a[92:95], v[80:83], v[156:159]\n"
    "v_mfma_f32_16x16x32_bf16 v[160:163], a[88:91], v[84:87], v[160:163]\n"
    "ds_read_b128 v[76:79], v15 offset:55296\n"
    "v_mfma_f32_16x16x32_bf16 v[164:167], a[92:95], v[84:87], v[164:167]\n"
    "s_waitcnt lgkmcnt(4)\n"
    "v_mfma_f32_16x16x32_bf16 v[168:171], a[88:91], v[88:91], v[168:171]\n"
    "ds_read_b128 v[80:83], v16 offset:49152\n"
    "v_mfma_f32_16x16x32_bf16 v[172:175], a[92:95], v[88:91], v[172:175]\n"
    "v_mfma_f32_16x16x32_bf16 v[176:179], a[88:91], v[92:95], v[176:179]\n"
    "ds_read_b128 v[84:87], v16 offset:51200\n"
    "v_mfma_f32_16x16x32_bf16 v[180:183], a[92:95], v[92:95], v[180:183]\n"
    "s_waitcnt lgkmcnt(4)\n"
    "v_mfma_f32_16x16x32_bf16 v[152:155], a[96:99], v[64:67], v[152:155]\n"
    "ds_read_b128 v[88:91], v16 offset:53248\n"
    "v_mfma_f32_16x16x32_bf16 v[156:159], a[100:103], v[64:67], v[156:159]\n"
    "v_mfma_f32_16x16x32_bf16 v[160:163], a[96:99], v[68:71], v[160:163]\n"
    "ds_read_b128 v[92:95], v16 offset:55296\n"
    "v_mfma_f32_16x16x32_bf16 v[164:167], a[100:103], v[68:71], v[164:167]\n"
    "s_waitcnt lgkmcnt(4)\n"
    "v_mfma_f32_16x16x32_bf16 v[168:171], a[96:99], v[72:75], v[168:171]\n"
    "s_waitcnt vmcnt(8)\n"
    "s_barrier\n"
    "v_mfma_f32_16x16x32_bf16 v[172:175], a[100:103], v[72:75], v[172:175]\n"
    "ds_read_b128 v[64:67], v15 offset:57344\n"
    "buffer_load_dwordx4 v[32:35], v22, s[56:59], s95 offen offset:0 sc1\n"
    "v_mfma_f32_16x16x32_bf16 v[176:179], a[96:99], v[76:79], v[176:179]\n"
    "ds_read_b128 v[68:71], v15 offset:59392\n"
    "buffer_load_dwordx4 v[36:39], v22, s[56:59], s95 offen offset:1024 sc1\n"
    "v_mfma_f32_16x16x32_bf16 v[180:183], a[100:103], v[76:79], v[180:183]\n"
    "buffer_load_dwordx4 v[40:43], v22, s[56:59], s95 offen offset:2048 sc1\n"
    "s_waitcnt lgkmcnt(4)\n"
    "v_mfma_f32_16x16x32_bf16 v[152:155], a[104:107], v[80:83], v[152:155]\n"
    "ds_read_b128 v[72:75], v15 offset:61440\n"
    "v_mfma_f32_16x16x32_bf16 v[156:159], a[108:111], v[80:83], v[156:159]\n"
    "v_mfma_f32_16x16x32_bf16 v[160:163], a[104:107], v[84:87], v[160:163]\n"
    "ds_read_b128 v[76:79], v15 offset:63488\n"
    "v_mfma_f32_16x16x32_bf16 v[164:167], a[108:111], v[84:87], v[164:167]\n"
    "s_waitcnt lgkmcnt(4)\n"
    "v_mfma_f32_16x16x32_bf16 v[168:171], a[104:107], v[88:91], v[168:171]\n"
    "ds_read_b128 v[80:83], v16 offset:57344\n"
    "v_mfma_f32_16x16x32_bf16 v[172:175], a[108:111], v[88:91], v[172:175]\n"
    "v_mfma_f32_16x16x32_bf16 v[176:179], a[104:107], v[92:95], v[176:179]\n"
    "ds_read_b128 v[84:87], v16 offset:59392\n"
    "buffer_load_dwordx4 v[44:47], v22, s[56:59], s95 offen offset:3072 sc1\n"
    "v_mfma_f32_16x16x32_bf16 v[180:183], a[108:111], v[92:95], v[180:183]\n"
    "s_waitcnt lgkmcnt(4)\n"
    "v_mfma_f32_16x16x32_bf16 v[152:155], a[112:115], v[64:67], v[152:155]\n"
    "ds_read_b128 v[88:91], v16 offset:61440\n"
    "v_mfma_f32_16x16x32_bf16 v[156:159], a[116:119], v[64:67], v[156:159]\n"
    "v_mfma_f32_16x16x32_bf16 v[160:163], a[112:115], v[68:71], v[160:163]\n"
    "ds_read_b128 v[92:95], v16 offset:63488\n"
    "v_mfma_f32_16x16x32_bf16 v[164:167], a[116:119], v[68:71], v[164:167]\n"
    "s_waitcnt lgkmcnt(4)\n"
    "v_mfma_f32_16x16x32_bf16 v[168:171], a[112:115], v[72:75], v[168:171]\n"
    "v_mfma_f32_16x16x32_bf16 v[172:175], a[116:119], v[72:75], v[172:175]\n"
    "v_mfma_f32_16x16x32_bf16 v[176:179], a[112:115], v[76:79], v[176:179]\n"
    "v_mfma_f32_16x16x32_bf16 v[180:183], a[116:119], v[76:79], v[180:183]\n"
    "s_waitcnt lgkmcnt(2)\n"
    "v_mfma_f32_16x16x32_bf16 v[152:155], a[120:123], v[80:83], v[152:155]\n"
    "v_mfma_f32_16x16x32_bf16 v[156:159], a[124:127], v[80:83], v[156:159]\n"
    "v_mfma_f32_16x16x32_bf16 v[160:163], a[120:123], v[84:87], v[160:163]\n"
    "v_mfma_f32_16x16x32_bf16 v[164:167], a[124:127], v[84:87], v[164:167]\n"
    "s_waitcnt lgkmcnt(0)\n"
    "v_mfma_f32_16x16x32_bf16 v[168:171], a[120:123], v[88:91], v[168:171]\n"
    "buffer_load_dwordx4 v[48:51], v31, s[56:59], s95 offen offset:0 sc1\n"
    "v_mfma_f32_16x16x32_bf16 v[172:175], a[124:127], v[88:91], v[172:175]\n"
    "v_mfma_f32_16x16x32_bf16 v[176:179], a[120:123], v[92:95], v[176:179]\n"
    "v_mfma_f32_16x16x32_bf16 v[180:183], a[124:127], v[92:95], v[180:183]\n"
    "v_fma_f32 v66, v184, v152, v153\n"
    "v_fma_f32 v67, v184, v160, v161\n"
    "v_fma_f32 v66, -v185, v160, v66\n"
    "v_fma_f32 v67, v185, v152, v67\n"
    "v_fma_f32 v70, v200, v168, v169\n"
    "s_nop 1\n"
    "v_fma_f32 v71, v200, v176, v177\n"
    "v_fma_f32 v70, -v201, v176, v70\n"
    "v_fma_f32 v71, v201, v168, v71\n"
    "v_fma_f32 v64, v184, v66, v154\n"
    "v_fma_f32 v65, v184, v67, v162\n"
    "v_fma_f32 v64, -v185, v67, v64\n"
    "v_fma_f32 v65, v185, v66, v65\n"
    "v_fma_f32 v68, v200, v70, v170\n"
    "v_fma_f32 v69, v200, v71, v178\n"
    "v_fma_f32 v68, -v201, v71, v68\n"
    "v_fma_f32 v69, v201, v70, v69\n"
    "v_fma_f32 v66, v184, v64, v155\n"
    "v_fma_f32 v67, v184, v65, v163\n"
    "v_fma_f32 v66, -v185, v65, v66\n"
    "v_fma_f32 v67, v185, v64, v67\n"
    "v_fma_f32 v70, v200, v68, v171\n"
    "v_fma_f32 v71, v200, v69, v179\n"
    "v_fma_f32 v70, -v201, v69, v70\n"
    "v_fma_f32 v71, v201, v68, v71\n"
    "v_fma_f32 v64, v184, v66, v156\n"
    "v_fma_f32 v65, v184, v67, v164\n"
    "v_fma_f32 v64, -v185, v67, v64\n"
    "v_fma_f32 v65, v185, v66, v65\n"
    "v_fma_f32 v68, v200, v70, v172\n"
    "v_fma_f32 v69, v200, v71, v180\n"
    "v_fma_f32 v68, -v201, v71, v68\n"
    "v_fma_f32 v69, v201, v70, v69\n"
    "v_fma_f32 v66, v184, v64, v157\n"
    "v_fma_f32 v67, v184, v65, v165\n"
    "v_fma_f32 v66, -v185, v65, v66\n"
    "v_fma_f32 v67, v185, v64, v67\n"
    "v_fma_f32 v70, v200, v68, v173\n"
    "v_fma_f32 v71, v200, v69, v181\n"
    "v_fma_f32 v70, -v201, v69, v70\n"
    "v_fma_f32 v71, v201, v68, v71\n"
    "v_fma_f32 v64, v184, v66, v158\n"
    "v_fma_f32 v65, v184, v67, v166\n"
    "v_fma_f32 v64, -v185, v67, v64\n"
    "v_fma_f32 v65, v185, v66, v65\n"
    "v_fma_f32 v68, v200, v70, v174\n"
    "v_fma_f32 v69, v200, v71, v182\n"
    "v_fma_f32 v68, -v201, v71, v68\n"
    "buffer_load_dwordx4 v[52:55], v31, s[56:59], s95 offen offset:1024 sc1\n"
    "v_fma_f32 v69, v201, v70, v69\n"
    "v_fma_f32 v66, v184, v64, v159\n"
    "v_fma_f32 v67, v184, v65, v167\n"
    "v_fma_f32 v66, -v185, v65, v66\n"
    "v_fma_f32 v67, v185, v64, v67\n"
    "v_fma_f32 v70, v200, v68, v175\n"
    "v_fma_f32 v71, v200, v69, v183\n"
    "v_fma_f32 v70, -v201, v69, v70\n"
    "v_fma_f32 v71, v201, v68, v71\n"
    "v_mov_b32 v72, v66\n"
    "v_mov_b32 v76, v66\n"
    "v_mov_b32 v73, v67\n"
    "v_mov_b32 v77, v67\n"
    "v_mov_b32 v74, v70\n"
    "v_mov_b32 v78, v70\n"
    "v_mov_b32 v75, v71\n"
    "v_mov_b32 v79, v71\n"
    "v_permlane16_swap_b32 v72, v76\n"
    "v_permlane16_swap_b32 v73, v77\n"
    "v_permlane16_swap_b32 v74, v78\n"
    "v_permlane16_swap_b32 v75, v79\n"
    "v_fma_f32 v80, v186, v72, v76\n"
    "v_fma_f32 v81, v186, v73, v77\n"
    "v_fma_f32 v80, -v187, v73, v80\n"
    "v_fma_f32 v81, v187, v72, v81\n"
    "v_fma_f32 v82, v202, v74, v78\n"
    "v_fma_f32 v83, v202, v75, v79\n"
    "v_fma_f32 v82, -v203, v75, v82\n"
    "v_fma_f32 v83, v203, v74, v83\n"
    "v_mov_b32 v84, v80\n"
    "v_mov_b32 v85, v81\n"
    "v_mov_b32 v86, v82\n"
    "v_mov_b32 v87, v83\n"
    "v_permlane32_swap_b32 v80, v84\n"
    "v_permlane32_swap_b32 v81, v85\n"
    "v_permlane32_swap_b32 v82, v86\n"
    "v_permlane32_swap_b32 v83, v87\n"
    "v_fma_f32 v88, v188, v80, v84\n"
    "v_fma_f32 v89, v188, v81, v85\n"
    "v_fma_f32 v88, -v189, v81, v88\n"
    "v_fma_f32 v89, v189, v80, v89\n"
    "v_fma_f32 v90, v204, v82, v86\n"
    "v_fma_f32 v91, v204, v83, v87\n"
    "v_fma_f32 v90, -v205, v83, v90\n"
    "v_fma_f32 v91, v205, v82, v91\n"
    "ds_write_b64 v108, v[88:89] offset:0\n"
    "ds_write_b64 v108, v[90:91] offset:128\n"
    "v_cndmask_b32 v72, 0, v72, s[16:17]\n"
    "v_cndmask_b32 v73, 0, v73, s[16:17]\n"
    "v_cndmask_b32 v74, 0, v74, s[16:17]\n"
    "v_cndmask_b32 v75, 0, v75, s[16:17]\n"
    "v_fma_f32 v220, v192, v80, v72\n"
    "v_fma_f32 v221, v192, v81, v73\n"
    "v_fma_f32 v220, -v193, v81, v220\n"
    "v_fma_f32 v221, v193, v80, v221\n"
    "v_fma_f32 v222, v208, v82, v74\n"
    "buffer_load_dwordx4 v[56:59], v31, s[56:59], s95 offen offset:2048 sc1\n"
    "v_fma_f32 v223, v208, v83, v75\n"
    "v_fma_f32 v222, -v209, v83, v222\n"
    "v_fma_f32 v223, v209, v82, v223\n"
    "s_waitcnt lgkmcnt(0)\n"
    "s_barrier\n"
    "ds_read_b64 v[72:73], v109 offset:0\n"
    "ds_read_b64 v[74:75], v109 offset:128\n"
    "ds_read_b64 v[76:77], v109 offset:256\n"
    "ds_read_b64 v[78:79], v109 offset:384\n"
    "ds_read_b64 v[80:81], v109 offset:512\n"
    "ds_read_b64 v[82:83], v109 offset:640\n"
    "ds_read_b64 v[84:85], v109 offset:768\n"
    "ds_read_b64 v[86:87], v109 offset:896\n"
    "s_waitcnt lgkmcnt(5)\n"
    "v_fma_f32 v224, v190, v72, v76\n"
    "v_fma_f32 v225, v190, v73, v77\n"
    "v_fma_f32 v224, -v191, v73, v224\n"
    "v_fma_f32 v225, v191, v72, v225\n"
    "s_waitcnt lgkmcnt(4)\n"
    "v_fma_f32 v226, v206, v74, v78\n"
    "v_fma_f32 v227, v206, v75, v79\n"
    "buffer_load_dwordx4 v[60:63], v31, s[56:59], s95 offen offset:3072 sc1\n"
    "v_fma_f32 v226, -v207, v75, v226\n"
    "v_fma_f32 v227, v207, v74, v227\n"
    "s_waitcnt lgkmcnt(3)\n"
    "v_fma_f32 v228, v190, v224, v80\n"
    "s_waitcnt vmcnt(12)\n"
    "v_fma_f32 v229, v190, v225, v81\n"
    "v_fma_f32 v228, -v191, v225, v228\n"
    "v_fma_f32 v229, v191, v224, v229\n"
    "ds_read_b128 a[128:131], v18 offset:12288\n"
    "s_waitcnt lgkmcnt(3)\n"
    "v_fma_f32 v230, v206, v226, v82\n"
    "v_fma_f32 v231, v206, v227, v83\n"
    "v_fma_f32 v230, -v207, v227, v230\n"
    "ds_read_b128 a[132:135], v19 offset:12288\n"
    "v_fma_f32 v231, v207, v226, v231\n"
    "s_waitcnt lgkmcnt(3)\n"
    "v_fma_f32 v232, v190, v228, v84\n"
    "v_fma_f32 v233, v190, v229, v85\n"
    "ds_read_b128 a[136:139], v20 offset:12288\n"
    "v_fma_f32 v232, -v191, v229, v232\n"
    "v_fma_f32 v233, v191, v228, v233\n"
    "s_waitcnt lgkmcnt(3)\n"
    "v_fma_f32 v234, v206, v230, v86\n"
    "ds_read_b128 a[140:143], v21 offset:12288\n"
    "v_fma_f32 v235, v206, v231, v87\n"
    "v_fma_f32 v234, -v207, v231, v234\n"
    "v_fma_f32 v235, v207, v230, v235\n"
    "v_cndmask_b32 v68, v232, v234, s[16:17]\n"
    "v_cndmask_b32 v70, v233, v235, s[16:17]\n"
    "v_mov_b32 v69, s64\n"
    "v_mov_b32 v71, s64\n"
    "buffer_store_dwordx4 v[68:71], v114, s[56:59], 0 offen sc1\n"
    "v_mov_b32 v64, 0\n"
    "v_mov_b32 v65, 0\n"
    "v_mov_b32 v66, 0\n"
    "v_mov_b32 v67, 0\n"
    "s_cmp_eq_u32 s60, 1\n"
    "s_cselect_b64 vcc, -1, 0\n"
    "v_cndmask_b32 v64, v64, v72, vcc\n"
    "v_cndmask_b32 v65, v65, v73, vcc\n"
    "v_cndmask_b32 v66, v66, v74, vcc\n"
    "v_cndmask_b32 v67, v67, v75, vcc\n"
    "s_cmp_eq_u32 s60, 2\n"
    "s_cselect_b64 vcc, -1, 0\n"
    "v_cndmask_b32 v64, v64, v224, vcc\n"
    "v_cndmask_b32 v65, v65, v225, vcc\n"
    "v_cndmask_b32 v66, v66, v226, vcc\n"
    "v_cndmask_b32 v67, v67, v227, vcc\n"
    "s_cmp_eq_u32 s60, 3\n"
    "s_cselect_b64 vcc, -1, 0\n"
    "v_cndmask_b32 v64, v64, v228, vcc\n"
    "v_cndmask_b32 v65, v65, v229, vcc\n"
    "v_cndmask_b32 v66, v66, v230, vcc\n"
    "v_cndmask_b32 v67, v67, v231, vcc\n"
    "v_fma_f32 v220, v194, v64, v220\n"
    "v_fma_f32 v221, v194, v65, v221\n"
    "v_fma_f32 v220, -v195, v65, v220\n"
    "v_fma_f32 v221, v195, v64, v221\n"
    "v_fma_f32 v222, v210, v66, v222\n"
    "v_fma_f32 v223, v210, v67, v223\n"
    "v_fma_f32 v222, -v211, v67, v222\n"
    "v_fma_f32 v223, v211, v66, v223\n"
    "s_waitcnt vmcnt(8)\n"
    "v_xor_b32 v0, s64, v33\n"
    "v_bitop3_b32 v0, v0, v35, s64 bitop3:0xf6\n"
    "s_waitcnt vmcnt(7)\n"
    "v_bitop3_b32 v0, v0, v37, s64 bitop3:0xf6\n"
    "v_bitop3_b32 v0, v0, v39, s64 bitop3:0xf6\n"
    "s_waitcnt vmcnt(6)\n"
    "v_bitop3_b32 v0, v0, v41, s64 bitop3:0xf6\n"
    "v_bitop3_b32 v0, v0, v43, s64 bitop3:0xf6\n"
    "s_waitcnt vmcnt(5)\n"
    "v_bitop3_b32 v0, v0, v45, s64 bitop3:0xf6\n"
    "v_bitop3_b32 v0, v0, v47, s64 bitop3:0xf6\n"
    "s_waitcnt vmcnt(4)\n"
    "v_bitop3_b32 v0, v0, v49, s64 bitop3:0xf6\n"
    "v_bitop3_b32 v0, v0, v51, s64 bitop3:0xf6\n"
    "s_waitcnt vmcnt(3)\n"
    "v_bitop3_b32 v0, v0, v53, s64 bitop3:0xf6\n"
    "v_bitop3_b32 v0, v0, v55, s64 bitop3:0xf6\n"
    "s_waitcnt vmcnt(2)\n"
    "v_bitop3_b32 v0, v0, v57, s64 bitop3:0xf6\n"
    "v_bitop3_b32 v0, v0, v59, s64 bitop3:0xf6\n"
    "s_waitcnt vmcnt(1)\n"
    "v_bitop3_b32 v0, v0, v61, s64 bitop3:0xf6\n"
    "v_bitop3_b32 v0, v0, v63, s64 bitop3:0xf6\n"
    "v_cmp_eq_u32 vcc, 0, v0\n"
    "s_nop 0\n"
    "s_cmp_eq_u64 vcc, -1\n"
    "s_cbranch_scc1 L1ok1_%=\n"
    "s_mov_b32 s80, 0x4000\n"
    "L1spin2_%=:\n"
    "s_sleep 1\n"
    "buffer_load_dwordx4 v[32:35], v22, s[56:59], s95 offen offset:0 sc1\n"
    "buffer_load_dwordx4 v[36:39], v22, s[56:59], s95 offen offset:1024 sc1\n"
    "buffer_load_dwordx4 v[40:43], v22, s[56:59], s95 offen offset:2048 sc1\n"
    "buffer_load_dwordx4 v[44:47], v22, s[56:59], s95 offen offset:3072 sc1\n"
    "buffer_load_dwordx4 v[48:51], v31, s[56:59], s95 offen offset:0 sc1\n"
    "buffer_load_dwordx4 v[52:55], v31, s[56:59], s95 offen offset:1024 sc1\n"
    "buffer_load_dwordx4 v[56:59], v31, s[56:59], s95 offen offset:2048 sc1\n"
    "buffer_load_dwordx4 v[60:63], v31, s[56:59], s95 offen offset:3072 sc1\n"
    "s_waitcnt vmcnt(0)\n"
    "v_xor_b32 v0, s64, v33\n"
    "v_bitop3_b32 v0, v0, v35, s64 bitop3:0xf6\n"
    "v_bitop3_b32 v0, v0, v37, s64 bitop3:0xf6\n"
    "v_bitop3_b32 v0, v0, v39, s64 bitop3:0xf6\n"
    "v_bitop3_b32 v0, v0, v41, s64 bitop3:0xf6\n"
    "v_bitop3_b32 v0, v0, v43, s64 bitop3:0xf6\n"
    "v_bitop3_b32 v0, v0, v45, s64 bitop3:0xf6\n"
    "v_bitop3_b32 v0, v0, v47, s64 bitop3:0xf6\n"
    "v_bitop3_b32 v0, v0, v49, s64 bitop3:0xf6\n"
    "v_bitop3_b32 v0, v0, v51, s64 bitop3:0xf6\n"
    "v_bitop3_b32 v0, v0, v53, s64 bitop3:0xf6\n"
    "v_bitop3_b32 v0, v0, v55, s64 bitop3:0xf6\n"
    "v_bitop3_b32 v0, v0, v57, s64 bitop3:0xf6\n"
    "v_bitop3_b32 v0, v0, v59, s64 bitop3:0xf6\n"
    "v_bitop3_b32 v0, v0, v61, s64 bitop3:0xf6\n"
    "v_bitop3_b32 v0, v0, v63, s64 bitop3:0xf6\n"
    "v_cmp_eq_u32 vcc, 0, v0\n"
    "s_nop 0\n"
    "s_cmp_eq_u64 vcc, -1\n"
    "s_cbranch_scc1 L1ok1_%=\n"
    "s_sub_u32 s80, s80, 1\n"
    "s_cmp_lg_u32 s80, 0\n"
    "s_cbranch_scc1 L1spin2_%=\n"
    "L1ok1_%=:\n"
    "v_add_f32 v32, v32, v36\n"
    "v_add_f32 v34, v34, v38\n"
    "v_add_f32 v32, v32, v40\n"
    "v_add_f32 v34, v34, v42\n"
    "v_add_f32 v32, v32, v44\n"
    "v_add_f32 v34, v34, v46\n"
    "v_add_f32 v32, v32, v48\n"
    "v_add_f32 v34, v34, v50\n"
    "v_add_f32 v32, v32, v52\n"
    "v_add_f32 v34, v34, v54\n"
    "v_add_f32 v32, v32, v56\n"
    "v_add_f32 v34, v34, v58\n"
    "v_add_f32 v32, v32, v60\n"
    "v_add_f32 v34, v34, v62\n"
    "v_mov_b32 v33, v32\n"
    "v_mov_b32 v35, v34\n"
    "s_nop 0\n"
    "v_permlane32_swap_b32 v32, v33\n"
    "v_permlane32_swap_b32 v34, v35\n"
    "v_add_f32 v32, v32, v33\n"
    "v_add_f32 v34, v34, v35\n"
    "v_mul_f32 v32, 0x3abe82fa, v32\n"
    "v_mul_f32 v34, 0x3abe82fa, v34\n"
    "v_fma_f32 v34, -v32, v32, v34\n"
    "v_max_f32 v34, 0, v34\n"
    "v_add_f32 v34, 0x3727c5ac, v34\n"
    "v_rsq_f32 v34, v34\n"
    "v_cmp_gt_u32 vcc, 32, v4\n"
    "v_mul_f32 v32, -v32, v34\n"
    "s_nop 0\n"
    "v_cndmask_b32 v32, v32, v34, vcc\n"
    "ds_write_b32 v106, v32\n"
    "ds_read_b128 v[36:39], v107 offset:0\n"
    "ds_read_b128 v[40:43], v107 offset:16\n"
    "ds_read_b128 v[44:47], v107 offset:32\n"
    "ds_read_b128 v[48:51], v107 offset:48\n"
    "s_waitcnt lgkmcnt(1)\n"
    "v_pk_fma_f32 v[120:121], v[120:121], v[36:37], v[44:45]\n"
    "v_pk_fma_f32 v[122:123], v[122:123], v[38:39], v[46:47]\n"
    "s_waitcnt lgkmcnt(0)\n"
    "v_pk_fma_f32 v[124:125], v[124:125], v[40:41], v[48:49]\n"
    "v_pk_fma_f32 v[126:127], v[126:127], v[42:43], v[50:51]\n"
    "v_cvt_pk_bf16_f32 v52, v120, v121\n"
    "v_cvt_pk_bf16_f32 v53, v122, v123\n"
    "v_cvt_pk_bf16_f32 v54, v124, v125\n"
    "v_cvt_pk_bf16_f32 v55, v126, v127\n"
    "buffer_store_dwordx4 v[52:55], v23, s[56:59], s86 offen offset:0 sc1\n"
    "v_pk_fma_f32 v[128:129], v[128:129], v[36:37], v[44:45]\n"
    "v_pk_fma_f32 v[130:131], v[130:131], v[38:39], v[46:47]\n"
    "v_pk_fma_f32 v[132:133], v[132:133], v[40:41], v[48:49]\n"
    "v_pk_fma_f32 v[134:135], v[134:135], v[42:43], v[50:51]\n"
    "v_cvt_pk_bf16_f32 v56, v128, v129\n"
    "v_cvt_pk_bf16_f32 v57, v130, v131\n"
    "v_cvt_pk_bf16_f32 v58, v132, v133\n"
    "v_cvt_pk_bf16_f32 v59, v134, v135\n"
    "buffer_store_dwordx4 v[56:59], v23, s[56:59], s86 offen offset:512 sc1\n"
    "v_mov_b32 v24, s64\n"
    "v_cmp_eq_u32 vcc, 0, v4\n"
    "v_mov_b32 v23, 0x40000000\n"
    "s_nop 0\n"
    "v_cndmask_b32 v23, v23, 0, vcc\n"
    "s_add_u32 m0, s67, 0\n"
    "s_nop 0\n"
    "buffer_load_dwordx4 v5, s[52:55], s70 offen offset:0 lds\n"
    "buffer_load_dwordx4 v6, s[52:55], s70 offen offset:1024 lds\n"
    "buffer_load_dwordx4 v7, s[52:55], s70 offen offset:2048 lds\n"
    "buffer_load_dwordx4 v8, s[52:55], s70 offen offset:3072 lds\n"
    "s_add_u32 m0, s67, 4096\n"
    "s_nop 0\n"
    "buffer_load_dwordx4 v5, s[52:55], s71 offen offset:0 lds\n"
    "buffer_load_dwordx4 v6, s[52:55], s71 offen offset:1024 lds\n"
    "buffer_load_dwordx4 v7, s[52:55], s71 offen offset:2048 lds\n"
    "buffer_load_dwordx4 v8, s[52:55], s71 offen offset:3072 lds\n"
    "s_add_u32 m0, s67, 8192\n"
    "s_nop 0\n"
    "buffer_load_dwordx4 v5, s[52:55], s72 offen offset:0 lds\n"
    "buffer_load_dwordx4 v6, s[52:55], s72 offen offset:1024 lds\n"
    "s_waitcnt vmcnt(10)\n"
    "s_barrier\n"
    "s_add_u32 s80, s14, 0x1000\n"
    "buffer_store_dwordx2 v[24:25], v23, s[56:59], s80 offen sc1\n"
    "buffer_load_dwordx4 v7, s[52:55], s72 offen offset:2048 lds\n"
    "buffer_load_dwordx4 v8, s[52:55], s72 offen offset:3072 lds\n"
    "s_add_u32 m0, s67, 12288\n"
    "s_nop 0\n"
    "buffer_load_dwordx4 v5, s[52:55], s73 offen offset:0 lds\n"
    "buffer_load_dwordx4 v6, s[52:55], s73 offen offset:1024 lds\n"
    "buffer_load_dwordx4 v7, s[52:55], s73 offen offset:2048 lds\n"
    "buffer_load_dwordx4 v8, s[52:55], s73 offen offset:3072 lds\n"
    "buffer_load_dwordx4 v[32:35], v2, s[56:59], 0 offen sc1\n"
    "buffer_load_dwordx4 v[36:39], v3, s[56:59], 0 offen sc1\n"
    "v_cmp_gt_u32 vcc, 0x40000000, v2\n"
    "s_nop 1\n"
    "v_cndmask_b32 v40, 0, v25, vcc\n"
    "v_cmp_gt_u32 vcc, 0x40000000, v3\n"
    "s_nop 1\n"
    "v_cndmask_b32 v41, 0, v25, vcc\n"
    "ds_read_b128 a[144:147], v18 offset:16384\n"
    "ds_read_b128 a[148:151], v19 offset:16384\n"
    "ds_read_b128 a[152:155], v20 offset:16384\n"
    "ds_read_b128 a[156:159], v21 offset:16384\n"
    "s_waitcnt lgkmcnt(0)\n"
    "s_add_u32 m0, s67, 16384\n"
    "s_nop 0\n"
    "buffer_load_dwordx4 v5, s[52:55], s74 offen offset:0 lds\n"
    "buffer_load_dwordx4 v6, s[52:55], s74 offen offset:1024 lds\n"
    "buffer_load_dwordx4 v7, s[52:55], s74 offen offset:2048 lds\n"
    "buffer_load_dwordx4 v8, s[52:55], s74 offen offset:3072 lds\n"
    "s_waitcnt vmcnt(19)\n"
    "ds_read_b128 a[160:163], v18 offset:0\n"
    "ds_read_b128 a[164:167], v19 offset:0\n"
    "ds_read_b128 a[168:171], v20 offset:0\n"
    "ds_read_b128 a[172:175], v21 offset:0\n"
    "s_waitcnt lgkmcnt(0)\n"
    "s_add_u32 m0, s67, 0\n"
    "s_nop 0\n"
    "buffer_load_dwordx4 v5, s[52:55], s75 offen offset:0 lds\n"
    "buffer_load_dwordx4 v6, s[52:55], s75 offen offset:1024 lds\n"
    "buffer_load_dwordx4 v7, s[52:55], s75 offen offset:2048 lds\n"
    "buffer_load_dwordx4 v8, s[52:55], s75 offen offset:3072 lds\n"
    "s_waitcnt vmcnt(19)\n"
    "ds_read_b128 a[176:179], v18 offset:4096\n"
    "ds_read_b128 a[180:183], v19 offset:4096\n"
    "ds_read_b128 a[184:187], v20 offset:4096\n"
    "ds_read_b128 a[188:191], v21 offset:4096\n"
    "s_waitcnt lgkmcnt(0)\n"
    "s_mov_b32 s52, s28\n"
    "s_and_b32 s53, s29, 0xffff\n"
    "s_waitcnt vmcnt(9)\n"
    "v_xor_b32 v0, v40, v33\n"
    "v_bitop3_b32 v0, v0, v35, v40 bitop3:0xf6\n"
    "s_waitcnt vmcnt(8)\n"
    "v_bitop3_b32 v0, v0, v37, v41 bitop3:0xf6\n"
    "v_bitop3_b32 v0, v0, v39, v41 bitop3:0xf6\n"
    "v_cmp_eq_u32 vcc, 0, v0\n"
    "s_nop 0\n"
    "s_cmp_eq_u64 vcc, -1\n"
    "s_cbranch_scc1 L1ok3_%=\n"
    "s_mov_b32 s80, 0x4000\n"
    "L1spin4_%=:\n"
    "s_sleep 1\n"
    "buffer_load_dwordx4 v[32:35], v2, s[56:59], 0 offen sc1\n"
    "buffer_load_dwordx4 v[36:39], v3, s[56:59], 0 offen sc1\n"
    "s_waitcnt vmcnt(0)\n"
    "v_xor_b32 v0, v40, v33\n"
    "v_bitop3_b32 v0, v0, v35, v40 bitop3:0xf6\n"
    "v_bitop3_b32 v0, v0, v37, v41 bitop3:0xf6\n"
    "v_bitop3_b32 v0, v0, v39, v41 bitop3:0xf6\n"
    "v_cmp_eq_u32 vcc, 0, v0\n"
    "s_nop 0\n"
    "s_cmp_eq_u64 vcc, -1\n"
    "s_cbranch_scc1 L1ok3_%=\n"
    "s_sub_u32 s80, s80, 1\n"
    "s_cmp_lg_u32 s80, 0\n"
    "s_cbranch_scc1 L1spin4_%=\n"
    "L1ok3_%=:\n"
    "v_mul_f32 v42, v216, v32\n"
    "v_mul_f32 v43, v216, v34\n"
    "s_add_u32 m0, s67, 4096\n"
    "v_fma_f32 v42, -v217, v34, v42\n"
    "v_fma_f32 v43, v217, v32, v43\n"
    "v_fma_f32 v42, v218, v36, v42\n"
    "buffer_load_dwordx4 v5, s[52:55], s68 offen offset:0 lds\n"
    "v_fma_f32 v43, v218, v38, v43\n"
    "v_fma_f32 v42, -v219, v38, v42\n"
    "v_fma_f32 v43, v219, v36, v43\n"
    "v_mov_b32 v44, v42\n"
    "v_mov_b32 v45, v43\n"
    "s_nop 0\n"
    "v_permlane32_swap_b32 v42, v44\n"
    "v_permlane32_swap_b32 v43, v45\n"
    "v_add_f32 v42, v42, v44\n"
    "buffer_load_dwordx4 v6, s[52:55], s68 offen offset:1024 lds\n"
    "v_add_f32 v43, v43, v45\n"
    "ds_write_b64 v110, v[42:43]\n"
    "s_lshl_b32 s79, s94, 16\n"
    "s_lshl_b32 s78, s91, 11\n"
    "s_add_u32 s79, s79, s78\n"
    "s_add_u32 s97, s79, 0x700000\n"
    "s_add_u32 s93, s97, 0x8000\n"
    "s_waitcnt lgkmcnt(0)\n"
    "s_barrier\n"
    "ds_read_b64 v[48:49], v111 offset:0\n"
    "ds_read_b64 v[50:51], v111 offset:128\n"
    "ds_read_b64 v[52:53], v111 offset:256\n"
    "ds_read_b64 v[54:55], v111 offset:384\n"
    "ds_read_b64 v[56:57], v111 offset:512\n"
    "ds_read_b64 v[58:59], v111 offset:640\n"
    "ds_read_b64 v[60:61], v111 offset:768\n"
    "ds_read_b64 v[62:63], v111 offset:896\n"
    "s_waitcnt lgkmcnt(5)\n"
    "v_pk_add_f32 v[48:49], v[48:49], v[52:53]\n"
    "s_waitcnt lgkmcnt(1)\n"
    "v_pk_add_f32 v[56:57], v[56:57], v[60:61]\n"
    "buffer_load_dwordx4 v7, s[52:55], s68 offen offset:2048 lds\n"
    "v_pk_add_f32 v[50:51], v[50:51], v[54:55]\n"
    "s_waitcnt lgkmcnt(0)\n"
    "v_pk_add_f32 v[58:59], v[58:59], v[62:63]\n"
    "v_pk_add_f32 v[48:49], v[48:49], v[56:57]\n"
    "buffer_load_dwordx4 v8, s[52:55], s68 offen offset:3072 lds\n"
    "v_pk_add_f32 v[50:51], v[50:51], v[58:59]\n"
    "v_fma_f32 v220, v196, v48, v220\n"
    "v_fma_f32 v221, v196, v49, v221\n"
    "v_fma_f32 v220, -v197, v49, v220\n"
    "v_fma_f32 v221, v197, v48, v221\n"
    "v_fma_f32 v222, v212, v50, v222\n"
    "ds_read_b128 a[192:195], v18 offset:8192\n"
    "v_fma_f32 v223, v212, v51, v223\n"
    "v_fma_f32 v222, -v213, v51, v222\n"
    "v_fma_f32 v223, v213, v50, v223\n"
    "ds_read_b128 a[196:199], v19 offset:8192\n"
    "v_fma_f32 v152, v184, v220, v152\n"
    "v_fma_f32 v160, v184, v221, v160\n"
    "v_fma_f32 v152, -v185, v221, v152\n"
    "ds_read_b128 a[200:203], v20 offset:8192\n"
    "v_fma_f32 v160, v185, v220, v160\n"
    "v_fma_f32 v168, v200, v222, v168\n"
    "v_fma_f32 v176, v200, v223, v176\n"
    "ds_read_b128 a[204:207], v21 offset:8192\n"
    "v_fma_f32 v168, -v201, v223, v168\n"
    "v_fma_f32 v176, v201, v222, v176\n"
    "v_fma_f32 v153, v184, v152, v153\n"
    "v_fma_f32 v161, v184, v160, v161\n"
    "v_fma_f32 v153, -v185, v160, v153\n"
    "v_fma_f32 v161, v185, v152, v161\n"
    "ds_read_b128 a[208:211], v18 offset:12288\n"
    "v_fma_f32 v169, v200, v168, v169\n"
    "v_fma_f32 v177, v200, v176, v177\n"
    "v_fma_f32 v169, -v201, v176, v169\n"
    "ds_read_b128 a[212:215], v19 offset:12288\n"
    "v_fma_f32 v177, v201, v168, v177\n"
    "v_fma_f32 v154, v184, v153, v154\n"
    "v_fma_f32 v162, v184, v161, v162\n"
    "ds_read_b128 a[216:219], v20 offset:12288\n"
    "v_fma_f32 v154, -v185, v161, v154\n"
    "v_fma_f32 v162, v185, v153, v162\n"
    "v_fma_f32 v170, v200, v169, v170\n"
    "ds_read_b128 a[220:223], v21 offset:12288\n"
    "v_fma_f32 v178, v200, v177, v178\n"
    "v_fma_f32 v170, -v201, v177, v170\n"
    "v_fma_f32 v178, v201, v169, v178\n"
    "s_waitcnt vmcnt(8)\n"
    "v_fma_f32 v155, v184, v154, v155\n"
    "v_fma_f32 v163, v184, v162, v163\n"
    "v_fma_f32 v155, -v185, v162, v155\n"
    "ds_read_b128 a[224:227], v18 offset:16384\n"
    "v_fma_f32 v163, v185, v154, v163\n"
    "v_fma_f32 v171, v200, v170, v171\n"
    "v_fma_f32 v179, v200, v178, v179\n"
    "ds_read_b128 a[228:231], v19 offset:16384\n"
    "v_fma_f32 v171, -v201, v178, v171\n"
    "v_fma_f32 v179, v201, v170, v179\n"
    "v_fma_f32 v156, v184, v155, v156\n"
    "ds_read_b128 a[232:235], v20 offset:16384\n"
    "v_fma_f32 v164, v184, v163, v164\n"
    "v_fma_f32 v156, -v185, v163, v156\n"
    "v_fma_f32 v164, v185, v155, v164\n"
    "ds_read_b128 a[236:239], v21 offset:16384\n"
    "v_fma_f32 v172, v200, v171, v172\n"
    "v_fma_f32 v180, v200, v179, v180\n"
    "v_fma_f32 v172, -v201, v179, v172\n"
    "v_fma_f32 v180, v201, v171, v180\n"
    "v_fma_f32 v157, v184, v156, v157\n"
    "v_fma_f32 v165, v184, v164, v165\n"
    "v_fma_f32 v157, -v185, v164, v157\n"
    "v_fma_f32 v165, v185, v156, v165\n"
    "v_fma_f32 v173, v200, v172, v173\n"
    "v_fma_f32 v181, v200, v180, v181\n"
    "v_fma_f32 v173, -v201, v180, v173\n"
    "v_fma_f32 v181, v201, v172, v181\n"
    "v_fma_f32 v158, v184, v157, v158\n"
    "v_fma_f32 v166, v184, v165, v166\n"
    "v_fma_f32 v158, -v185, v165, v158\n"
    "v_fma_f32 v166, v185, v157, v166\n"
    "v_fma_f32 v174, v200, v173, v174\n"
    "v_fma_f32 v182, v200, v181, v182\n"
    "v_fma_f32 v174, -v201, v181, v174\n"
    "v_fma_f32 v182, v201, v173, v182\n"
    "v_fma_f32 v159, v184, v158, v159\n"
    "v_fma_f32 v167, v184, v166, v167\n"
    "v_fma_f32 v159, -v185, v166, v159\n"
    "v_fma_f32 v167, v185, v158, v167\n"
    "v_fma_f32 v175, v200, v174, v175\n"
    "v_fma_f32 v183, v200, v182, v183\n"
    "v_fma_f32 v175, -v201, v182, v175\n"
    "v_fma_f32 v183, v201, v174, v183\n"
    "v_pk_mul_f32 v[32:33], v[152:153], v[198:199] op_sel_hi:[1,0]\n"
    "v_pk_mul_f32 v[40:41], v[160:161], v[198:199] op_sel_hi:[1,0] neg_lo:[0,1] neg_hi:[0,1]\n"
    "v_pk_mul_f32 v[34:35], v[154:155], v[198:199] op_sel_hi:[1,0]\n"
    "v_pk_mul_f32 v[42:43], v[162:163], v[198:199] op_sel_hi:[1,0] neg_lo:[0,1] neg_hi:[0,1]\n"
    "v_pk_mul_f32 v[36:37], v[156:157], v[198:199] op_sel_hi:[1,0]\n"
    "v_pk_mul_f32 v[44:45], v[164:165], v[198:199] op_sel_hi:[1,0] neg_lo:[0,1] neg_hi:[0,1]\n"
    "v_pk_mul_f32 v[38:39], v[158:159], v[198:199] op_sel_hi:[1,0]\n"
    "v_pk_mul_f32 v[46:47], v[166:167], v[198:199] op_sel_hi:[1,0] neg_lo:[0,1] neg_hi:[0,1]\n"
    "v_pk_fma_f32 v[32:33], v[160:161], v[198:199], v[32:33] op_sel:[0,1,0] op_sel_hi:[1,1,1] neg_lo:[0,1,0] neg_hi:[0,1,0]\n"
    "v_pk_fma_f32 v[40:41], v[152:153], v[198:199], v[40:41] op_sel:[0,1,0] op_sel_hi:[1,1,1] neg_lo:[0,1,0] neg_hi:[0,1,0]\n"
    "v_pk_fma_f32 v[34:35], v[162:163], v[198:199], v[34:35] op_sel:[0,1,0] op_sel_hi:[1,1,1] neg_lo:[0,1,0] neg_hi:[0,1,0]\n"
    "v_pk_fma_f32 v[42:43], v[154:155], v[198:199], v[42:43] op_sel:[0,1,0] op_sel_hi:[1,1,1] neg_lo:[0,1,0] neg_hi:[0,1,0]\n"
    "v_pk_fma_f32 v[36:37], v[164:165], v[198:199], v[36:37] op_sel:[0,1,0] op_sel_hi:[1,1,1] neg_lo:[0,1,0] neg_hi:[0,1,0]\n"
    "v_pk_fma_f32 v[44:45], v[156:157], v[198:199], v[44:45] op_sel:[0,1,0] op_sel_hi:[1,1,1] neg_lo:[0,1,0] neg_hi:[0,1,0]\n"
    "v_pk_fma_f32 v[38:39], v[166:167], v[198:199], v[38:39] op_sel:[0,1,0] op_sel_hi:[1,1,1] neg_lo:[0,1,0] neg_hi:[0,1,0]\n"
    "v_pk_fma_f32 v[46:47], v[158:159], v[198:199], v[46:47] op_sel:[0,1,0] op_sel_hi:[1,1,1] neg_lo:[0,1,0] neg_hi:[0,1,0]\n"
    "v_cvt_pk_bf16_f32 v48, v32, v33\n"
    "v_cvt_pk_bf16_f32 v49, v34, v35\n"
    "v_cvt_pk_bf16_f32 v50, v36, v37\n"
    "v_cvt_pk_bf16_f32 v51, v38, v39\n"
    "ds_write_b128 v112, v[48:51]\n"
    "ds_read_b64_tr_b16 v[232:233], v113 offset:0\n"
    "ds_read_b64_tr_b16 v[234:235], v113 offset:256\n"
    "v_cvt_pk_bf16_f32 v52, v40, v41\n"
    "v_cvt_pk_bf16_f32 v53, v42, v43\n"
    "v_cvt_pk_bf16_f32 v54, v44, v45\n"
    "v_cvt_pk_bf16_f32 v55, v46, v47\n"
    "s_waitcnt lgkmcnt(11)\n"
    "ds_write_b128 v112, v[52:55]\n"
    "ds_read_b64_tr_b16 v[236:237], v113 offset:0\n"
    "ds_read_b64_tr_b16 v[238:239], v113 offset:256\n"
    "v_pk_mul_f32 v[32:33], v[168:169], v[214:215] op_sel_hi:[1,0]\n"
    "v_pk_mul_f32 v[40:41], v[176:177], v[214:215] op_sel_hi:[1,0] neg_lo:[0,1] neg_hi:[0,1]\n"
    "s_waitcnt vmcnt(4)\n"
    "v_pk_mul_f32 v[34:35], v[170:171], v[214:215] op_sel_hi:[1,0]\n"
    "v_pk_mul_f32 v[42:43], v[178:179], v[214:215] op_sel_hi:[1,0] neg_lo:[0,1] neg_hi:[0,1]\n"
    "v_pk_mul_f32 v[36:37], v[172:173], v[214:215] op_sel_hi:[1,0]\n"
    "ds_read_b128 a[240:243], v18 offset:0\n"
    "v_pk_mul_f32 v[44:45], v[180:181], v[214:215] op_sel_hi:[1,0] neg_lo:[0,1] neg_hi:[0,1]\n"
    "v_pk_mul_f32 v[38:39], v[174:175], v[214:215] op_sel_hi:[1,0]\n"
    "v_pk_mul_f32 v[46:47], v[182:183], v[214:215] op_sel_hi:[1,0] neg_lo:[0,1] neg_hi:[0,1]\n"
    "s_waitcnt lgkmcnt(11)\n"
    "ds_read_b128 a[244:247], v19 offset:0\n"
    "v_pk_fma_f32 v[32:33], v[176:177], v[214:215], v[32:33] op_sel:[0,1,0] op_sel_hi:[1,1,1] neg_lo:[0,1,0] neg_hi:[0,1,0]\n"
    "v_pk_fma_f32 v[40:41], v[168:169], v[214:215], v[40:41] op_sel:[0,1,0] op_sel_hi:[1,1,1] neg_lo:[0,1,0] neg_hi:[0,1,0]\n"
    "v_pk_fma_f32 v[34:35], v[178:179], v[214:215], v[34:35] op_sel:[0,1,0] op_sel_hi:[1,1,1] neg_lo:[0,1,0] neg_hi:[0,1,0]\n"
    "ds_read_b128 a[248:251], v20 offset:0\n"
    "v_pk_fma_f32 v[42:43], v[170:171], v[214:215], v[42:43] op_sel:[0,1,0] op_sel_hi:[1,1,1] neg_lo:[0,1,0] neg_hi:[0,1,0]\n"
    "v_pk_fma_f32 v[36:37], v[180:181], v[214:215], v[36:37] op_sel:[0,1,0] op_sel_hi:[1,1,1] neg_lo:[0,1,0] neg_hi:[0,1,0]\n"
    "v_pk_fma_f32 v[44:45], v[172:173], v[214:215], v[44:45] op_sel:[0,1,0] op_sel_hi:[1,1,1] neg_lo:[0,1,0] neg_hi:[0,1,0]\n"
    "ds_read_b128 a[252:255], v21 offset:0\n"
    "v_pk_fma_f32 v[38:39], v[182:183], v[214:215], v[38:39] op_sel:[0,1,0] op_sel_hi:[1,1,1] neg_lo:[0,1,0] neg_hi:[0,1,0]\n"
    "v_pk_fma_f32 v[46:47], v[174:175], v[214:215], v[46:47] op_sel:[0,1,0] op_sel_hi:[1,1,1] neg_lo:[0,1,0] neg_hi:[0,1,0]\n"
    "v_cvt_pk_bf16_f32 v48, v32, v33\n"
    "s_add_u32 m0, s67, 8192\n"
    "v_cvt_pk_bf16_f32 v49, v34, v35\n"
    "v_cvt_pk_bf16_f32 v50, v36, v37\n"
    "v_cvt_pk_bf16_f32 v51, v38, v39\n"
    "buffer_load_dwordx4 v5, s[52:55], s69 offen offset:0 lds\n"
    "ds_write_b128 v112, v[48:51]\n"
    "s_waitcnt lgkmcnt(11)\n"
    "ds_read_b64_tr_b16 v[240:241], v113 offset:0\n"
    "ds_read_b64_tr_b16 v[242:243], v113 offset:256\n"
    "v_cvt_pk_bf16_f32 v52, v40, v41\n"
    "v_cvt_pk_bf16_f32 v53, v42, v43\n"
    "v_cvt_pk_bf16_f32 v54, v44, v45\n"
    "buffer_load_dwordx4 v6, s[52:55], s69 offen offset:1024 lds\n"
    "v_cvt_pk_bf16_f32 v55, v46, v47\n"
    "ds_write_b128 v112, v[52:55]\n"
    "ds_read_b64_tr_b16 v[244:245], v113 offset:0\n"
    "s_waitcnt lgkmcnt(11)\n"
    "ds_read_b64_tr_b16 v[246:247], v113 offset:256\n"
    "buffer_load_dwordx4 v7, s[52:55], s69 offen offset:2048 lds\n"
    "buffer_load_dwordx4 v8, s[52:55], s69 offen offset:3072 lds\n"
    "buffer_store_dwordx4 v[232:235], v22, s[56:59], s97 offen offset:0 sc1\n"
    "s_waitcnt lgkmcnt(10)\n"
    "buffer_store_dwordx4 v[236:239], v22, s[56:59], s93 offen offset:0 sc1\n"
    "s_waitcnt lgkmcnt(3)\n"
    "buffer_store_dwordx4 v[240:243], v22, s[56:59], s97 offen offset:1024 sc1\n"
    "s_waitcnt lgkmcnt(0)\n"
    "buffer_store_dwordx4 v[244:247], v22, s[56:59], s93 offen offset:1024 sc1\n"
    "s_add_u32 m0, s67, 12288\n"
    "s_nop 0\n"
    "buffer_load_dwordx4 v5, s[52:55], s70 offen offset:0 lds\n"
    "buffer_load_dwordx4 v6, s[52:55], s70 offen offset:1024 lds\n"
    "buffer_load_dwordx4 v7, s[52:55], s70 offen offset:2048 lds\n"
    "buffer_load_dwordx4 v8, s[52:55], s70 offen offset:3072 lds\n"
    "s_add_u32 m0, s67, 16384\n"
    "s_nop 0\n"
    "buffer_load_dwordx4 v5, s[52:55], s71 offen offset:0 lds\n"
    "buffer_load_dwordx4 v6, s[52:55], s71 offen offset:1024 lds\n"
    "buffer_load_dwordx4 v7, s[52:55], s71 offen offset:2048 lds\n"
    "buffer_load_dwordx4 v8, s[52:55], s71 offen offset:3072 lds\n"
    "s_add_u32 m0, s67, 0\n"
    "s_nop 0\n"
    "buffer_load_dwordx4 v5, s[52:55], s72 offen offset:0 lds\n"
    "buffer_load_dwordx4 v6, s[52:55], s72 offen offset:1024 lds\n"
    "s_waitcnt vmcnt(10)\n"
    "s_barrier\n"
    "s_add_u32 s80, s14, 0x9000\n"
    "buffer_store_dwordx2 v[24:25], v23, s[56:59], s80 offen sc1\n"
    "buffer_load_dwordx4 v7, s[52:55], s72 offen offset:2048 lds\n"
    "buffer_load_dwordx4 v8, s[52:55], s72 offen offset:3072 lds\n"
    "ds_read_b128 a[0:3], v18 offset:4096\n"
    "ds_read_b128 a[4:7], v19 offset:4096\n"
    "ds_read_b128 a[8:11], v20 offset:4096\n"
    "ds_read_b128 a[12:15], v21 offset:4096\n"
    "s_waitcnt lgkmcnt(0)\n"
    "s_add_u32 m0, s67, 4096\n"
    "s_nop 0\n"
    "buffer_load_dwordx4 v5, s[52:55], s73 offen offset:0 lds\n"
    "buffer_load_dwordx4 v6, s[52:55], s73 offen offset:1024 lds\n"
    "buffer_load_dwordx4 v7, s[52:55], s73 offen offset:2048 lds\n"
    "buffer_load_dwordx4 v8, s[52:55], s73 offen offset:3072 lds\n"
    "ds_read_b128 a[16:19], v18 offset:8192\n"
    "ds_read_b128 a[20:23], v19 offset:8192\n"
    "ds_read_b128 a[24:27], v20 offset:8192\n"
    "ds_read_b128 a[28:31], v21 offset:8192\n"
    "s_waitcnt lgkmcnt(0)\n"
    "s_add_u32 m0, s67, 8192\n"
    "s_nop 0\n"
    "buffer_load_dwordx4 v5, s[52:55], s74 offen offset:0 lds\n"
    "buffer_load_dwordx4 v6, s[52:55], s74 offen offset:1024 lds\n"
    "buffer_load_dwordx4 v7, s[52:55], s74 offen offset:2048 lds\n"
    "buffer_load_dwordx4 v8, s[52:55], s74 offen offset:3072 lds\n"
    "s_waitcnt vmcnt(17)\n"
    "ds_read_b128 a[32:35], v18 offset:12288\n"
    "ds_read_b128 a[36:39], v19 offset:12288\n"
    "ds_read_b128 a[40:43], v20 offset:12288\n"
    "ds_read_b128 a[44:47], v21 offset:12288\n"
    "s_waitcnt lgkmcnt(0)\n"
    "s_add_u32 m0, s67, 12288\n"
    "s_nop 0\n"
    "buffer_load_dwordx4 v5, s[52:55], s75 offen offset:0 lds\n"
    "buffer_load_dwordx4 v6, s[52:55], s75 offen offset:1024 lds\n"
    "buffer_load_dwordx4 v7, s[52:55], s75 offen offset:2048 lds\n"
    "buffer_load_dwordx4 v8, s[52:55], s75 offen offset:3072 lds\n"
    "buffer_load_dwordx2 v[2:3], v254, s[56:59], 0 offen sc1\n"
    "s_barrier\n"
    "s_and_b32 s77, s61, 12\n"
    "s_lshl1_add_u32 s77, s77, s90\n"
    "s_lshl2_add_u32 s95, s63, s77\n"
    "s_lshr_b32 s65, s60, 1\n"
    "s_and_b32 s82, s60, 1\n"
    "s_lshl1_add_u32 s66, s95, s65\n"
    "s_mov_b32 s44, s30\n"
    "s_and_b32 s45, s31, 0xffff\n"
    "s_mov_b32 s46, 0xac000\n"
    "s_mov_b32 s52, s32\n"
    "s_and_b32 s53, s33, 0xffff\n"
    "s_mov_b32 s54, 0x400000\n"
    "s_mov_b32 s10, 0x76543210\n"
    "s_cmp_eq_u32 s90, 1\n"
    "s_cselect_b32 s10, 0x98765432, s10\n"
    "s_cmp_eq_u32 s90, 2\n"
    "s_cselect_b32 s10, 0x10a98765, s10\n"
    "s_cmp_eq_u32 s90, 3\n"
    "s_cselect_b32 s10, 0x43210a98, s10\n"
    "s_mov_b32 s11, 0xa98\n"
    "s_cmp_eq_u32 s90, 1\n"
    "s_cselect_b32 s11, 0x10a, s11\n"
    "s_cmp_eq_u32 s90, 2\n"
    "s_cselect_b32 s11, 0x432, s11\n"
    "s_cmp_eq_u32 s90, 3\n"
    "s_cselect_b32 s11, 0x765, s11\n"
    "v_lshrrev_b32 v26, 3, v4\n"
    "v_bfe_u32 v27, v4, 4, 1\n"
    "v_lshlrev_b32 v27, 2, v27\n"
    "v_and_b32 v31, 7, v4\n"
    "v_xor_b32 v27, v27, v31\n"
    "v_lshlrev_b32 v27, 4, v27\n"
    "v_lshl_add_u32 v15, v26, 10, v27\n"
    "s_lshl_b32 s77, s60, 7\n"
    "s_add_u32 s77, s77, s62\n"
    "s_lshl_b32 s77, s77, 7\n"
    "v_add_u32 v15, s77, v15\n"
    "v_add_u32 v16, 7168, v15\n"
    "s_lshl_b32 s87, s60, 11\n"
    "s_lshr_b32 s77, s95, 1\n"
    "s_lshl_b32 s77, s77, 4\n"
    "v_and_b32 v13, 15, v4\n"
    "v_min_u32 v13, 11, v13\n"
    "v_add_u32 v13, s77, v13\n"
    "v_lshlrev_b32 v13, 7, v13\n"
    "v_add_u32 v13, 0x11000, v13\n"
    "s_add_u32 s77, s77, 15\n"
    "v_mov_b32 v27, s77\n"
    "s_and_b32 s76, s62, 2\n"
    "s_add_u32 s77, s77, s76\n"
    "s_lshr_b32 s76, s62, 2\n"
    "s_add_u32 s77, s77, s76\n"
    "s_sub_u32 s77, s77, 3\n"
    "v_mov_b32 v14, s77\n"
    "v_cndmask_b32 v14, v14, v27, s[16:17]\n"
    "v_lshlrev_b32 v14, 7, v14\n"
    "v_add_u32 v14, 0x11000, v14\n"
    "s_mul_i32 s77, s66, 22\n"
    "s_add_u32 s77, s77, s82\n"
    "s_lshl_b32 s77, s77, 11\n"
    "s_add_u32 s77, s77, 0xb00000\n"
    "v_add_u32 v28, s77, v22\n"
    "v_lshrrev_b32 v26, 4, v4\n"
    "s_lshl_b32 s77, s66, 5\n"
    "s_lshl2_add_u32 s77, s82, s77\n"
    "v_add_u32 v26, s77, v26\n"
    "v_and_b32 v27, 15, v4\n"
    "s_lshl_b32 s77, s62, 7\n"
    "v_lshl_add_u32 v27, v27, 3, s77\n"
    "global_load_dwordx2 v[184:185], v27, s[34:35]\n"
    "v_lshl_add_u32 v27, v26, 10, v27\n"
    "v_lshlrev_b32 v9, 1, v27\n"
    "s_mov_b32 s76, 0\n"
    "buffer_load_dwordx2 v[176:177], v27, s[40:43], s76 offen\n"
    "s_mov_b32 s76, 8192\n"
    "buffer_load_dwordx2 v[178:179], v27, s[40:43], s76 offen\n"
    "s_mov_b32 s76, 16384\n"
    "buffer_load_dwordx2 v[180:181], v27, s[40:43], s76 offen\n"
    "s_mov_b32 s76, 24576\n"
    "buffer_load_dwordx2 v[182:183], v27, s[40:43], s76 offen\n"
    "s_waitcnt vmcnt(23)\n"
    "ds_read_b128 a[48:51], v18 offset:16384\n"
    "ds_read_b128 a[52:55], v19 offset:16384\n"
    "ds_read_b128 a[56:59], v20 offset:16384\n"
    "ds_read_b128 a[60:63], v21 offset:16384\n"
    "s_waitcnt lgkmcnt(0)\n"
    "s_waitcnt vmcnt(18)\n"
    "ds_read_b128 a[64:67], v18 offset:0\n"
    "ds_read_b128 a[68:71], v19 offset:0\n"
    "ds_read_b128 a[72:75], v20 offset:0\n"
    "ds_read_b128 a[76:79], v21 offset:0\n"
    "s_waitcnt lgkmcnt(0)\n"
    "s_waitcnt vmcnt(14)\n"
    "ds_read_b128 a[80:83], v18 offset:4096\n"
    "ds_read_b128 a[84:87], v19 offset:4096\n"
    "ds_read_b128 a[88:91], v20 offset:4096\n"
    "ds_read_b128 a[92:95], v21 offset:4096\n"
    "s_waitcnt lgkmcnt(0)\n"
    "s_waitcnt vmcnt(10)\n"
    "ds_read_b128 a[96:99], v18 offset:8192\n"
    "ds_read_b128 a[100:103], v19 offset:8192\n"
    "ds_read_b128 a[104:107], v20 offset:8192\n"
    "ds_read_b128 a[108:111], v21 offset:8192\n"
    "s_waitcnt lgkmcnt(0)\n"
    "s_waitcnt vmcnt(5)\n"
    "v_xor_b32 v0, s64, v2\n"
    "v_bitop3_b32 v0, v0, v3, s64 bitop3:0xf6\n"
    "v_cmp_eq_u32 vcc, 0, v0\n"
    "s_nop 0\n"
    "s_cmp_eq_u64 vcc, -1\n"
    "s_cbranch_scc1 L1ok5_%=\n"
    "s_mov_b32 s80, 0x4000\n"
    "buffer_load_dwordx2 v[26:27], v254, s[56:59], 0 offen sc1\n"
    "L1spin6_%=:\n"
    "s_sleep 4\n"
    "buffer_load_dwordx2 v[2:3], v254, s[56:59], 0 offen sc1\n"
    "s_waitcnt vmcnt(1)\n"
    "v_xor_b32 v0, s64, v26\n"
    "v_bitop3_b32 v0, v0, v27, s64 bitop3:0xf6\n"
    "v_cmp_eq_u32 vcc, 0, v0\n"
    "s_nop 0\n"
    "s_cmp_eq_u64 vcc, -1\n"
    "s_cbranch_scc1 L1ok7_%=\n"
    "s_sleep 4\n"
    "buffer_load_dwordx2 v[26:27], v254, s[56:59], 0 offen sc1\n"
    "s_waitcnt vmcnt(1)\n"
    "v_xor_b32 v0, s64, v2\n"
    "v_bitop3_b32 v0, v0, v3, s64 bitop3:0xf6\n"
    "v_cmp_eq_u32 vcc, 0, v0\n"
    "s_nop 0\n"
    "s_cmp_eq_u64 vcc, -1\n"
    "s_cbranch_scc1 L1ok7_%=\n"
    "s_sub_u32 s80, s80, 1\n"
    "s_cmp_lg_u32 s80, 0\n"
    "s_cbranch_scc1 L1spin6_%=\n"
    "L1ok7_%=:\n"
    "L1ok5_%=:\n"
    "s_add_u32 m0, s81, 0\n"
    "s_lshl_b32 s76, s68, 5\n"
    "buffer_load_dwordx4 v248, s[56:59], s76 offen offset:0 sc1 lds\n"
    "buffer_load_dwordx4 v249, s[56:59], s76 offen offset:1024 sc1 lds\n"
    "s_add_u32 m0, s81, 8192\n"
    "s_lshl_b32 s76, s69, 5\n"
    "buffer_load_dwordx4 v248, s[56:59], s76 offen offset:0 sc1 lds\n"
    "buffer_load_dwordx4 v249, s[56:59], s76 offen offset:1024 sc1 lds\n"
    "s_add_u32 m0, s81, 16384\n"
    "s_lshl_b32 s76, s70, 5\n"
    "buffer_load_dwordx4 v248, s[56:59], s76 offen offset:0 sc1 lds\n"
    "buffer_load_dwordx4 v249, s[56:59], s76 offen offset:1024 sc1 lds\n"
    "s_add_u32 m0, s81, 24576\n"
    "s_lshl_b32 s76, s71, 5\n"
    "buffer_load_dwordx4 v248, s[56:59], s76 offen offset:0 sc1 lds\n"
    "buffer_load_dwordx4 v249, s[56:59], s76 offen offset:1024 sc1 lds\n"
    "s_add_u32 m0, s81, 32768\n"
    "s_lshl_b32 s76, s72, 5\n"
    "buffer_load_dwordx4 v248, s[56:59], s76 offen offset:0 sc1 lds\n"
    "buffer_load_dwordx4 v249, s[56:59], s76 offen offset:1024 sc1 lds\n"
    "s_add_u32 m0, s81, 40960\n"
    "s_lshl_b32 s76, s73, 5\n"
    "buffer_load_dwordx4 v248, s[56:59], s76 offen offset:0 sc1 lds\n"
    "buffer_load_dwordx4 v249, s[56:59], s76 offen offset:1024 sc1 lds\n"
    "s_lshl_b32 s79, s61, 6\n"
    "s_add_u32 s79, s79, s83\n"
    "s_lshl_b32 s79, s79, 11\n"
    "s_add_u32 s97, s79, 0xe80000\n"
    "s_add_u32 m0, s81, 49152\n"
    "s_lshl_b32 s76, s74, 5\n"
    "buffer_load_dwordx4 v248, s[56:59], s76 offen offset:0 sc1 lds\n"
    "buffer_load_dwordx4 v249, s[56:59], s76 offen offset:1024 sc1 lds\n"
    "s_add_u32 m0, s81, 57344\n"
    "s_lshl_b32 s76, s75, 5\n"
    "s_waitcnt vmcnt(10)\n"
    "s_barrier\n"
    "ds_read_b128 v[96:99], v252 offset:0\n"
    "ds_read_b128 v[100:103], v252 offset:1024\n"
    "ds_read_b128 v[104:107], v252 offset:2048\n"
    "ds_read_b128 v[108:111], v252 offset:3072\n"
    "ds_read_b128 v[112:115], v252 offset:4096\n"
    "buffer_load_dwordx4 v248, s[56:59], s76 offen offset:0 sc1 lds\n"
    "buffer_load_dwordx4 v249, s[56:59], s76 offen offset:1024 sc1 lds\n"
    "s_waitcnt lgkmcnt(3)\n"
    "v_mfma_f32_32x32x16_bf16 v[32:47], a[128:131], v[96:99], 0\n"
    "ds_read_b128 v[116:119], v252 offset:5120\n"
    "ds_read_b128 a[112:115], v18 offset:12288\n"
    "ds_read_b128 a[116:119], v19 offset:12288\n"
    "ds_read_b128 a[120:123], v20 offset:12288\n"
    "v_mfma_f32_32x32x16_bf16 v[48:63], a[128:131], v[100:103], 0\n"
    "ds_read_b128 v[96:99], v252 offset:6144\n"
    "ds_read_b128 a[124:127], v21 offset:12288\n"
    "s_waitcnt lgkmcnt(7)\n"
    "v_mfma_f32_32x32x16_bf16 v[32:47], a[132:135], v[104:107], v[32:47]\n"
    "ds_read_b128 v[100:103], v252 offset:7168\n"
    "v_mfma_f32_32x32x16_bf16 v[48:63], a[132:135], v[108:111], v[48:63]\n"
    "ds_read_b128 v[104:107], v252 offset:8192\n"
    "s_waitcnt lgkmcnt(7)\n"
    "v_mfma_f32_32x32x16_bf16 v[32:47], a[136:139], v[112:115], v[32:47]\n"
    "ds_read_b128 v[108:111], v252 offset:9216\n"
    "v_mfma_f32_32x32x16_bf16 v[48:63], a[136:139], v[116:119], v[48:63]\n"
    "ds_read_b128 v[112:115], v252 offset:10240\n"
    "s_waitcnt lgkmcnt(3)\n"
    "v_mfma_f32_32x32x16_bf16 v[32:47], a[140:143], v[96:99], v[32:47]\n"
    "ds_read_b128 v[116:119], v252 offset:11264\n"
    "v_mfma_f32_32x32x16_bf16 v[48:63], a[140:143], v[100:103], v[48:63]\n"
    "ds_read_b128 v[96:99], v252 offset:12288\n"
    "s_waitcnt lgkmcnt(3)\n"
    "v_mfma_f32_32x32x16_bf16 v[32:47], a[144:147], v[104:107], v[32:47]\n"
    "ds_read_b128 v[100:103], v252 offset:13312\n"
    "v_mfma_f32_32x32x16_bf16 v[48:63], a[144:147], v[108:111], v[48:63]\n"
    "ds_read_b128 v[104:107], v252 offset:14336\n"
    "s_waitcnt lgkmcnt(3)\n"
    "v_mfma_f32_32x32x16_bf16 v[32:47], a[148:151], v[112:115], v[32:47]\n"
    "ds_read_b128 v[108:111], v252 offset:15360\n"
    "v_mfma_f32_32x32x16_bf16 v[48:63], a[148:151], v[116:119], v[48:63]\n"
    "s_waitcnt vmcnt(8)\n"
    "s_barrier\n"
    "ds_read_b128 v[112:115], v252 offset:16384\n"
    "s_waitcnt lgkmcnt(3)\n"
    "v_mfma_f32_32x32x16_bf16 v[32:47], a[152:155], v[96:99], v[32:47]\n"
    "ds_read_b128 v[116:119], v252 offset:17408\n"
    "s_add_u32 m0, s81, 0\n"
    "s_lshl_b32 s76, s68, 5\n"
    "s_add_u32 s76, s76, 0x8000\n"
    "v_mfma_f32_32x32x16_bf16 v[48:63], a[152:155], v[100:103], v[48:63]\n"
    "ds_read_b128 v[96:99], v252 offset:18432\n"
    "buffer_load_dwordx4 v248, s[56:59], s76 offen offset:0 sc1 lds\n"
    "buffer_load_dwordx4 v249, s[56:59], s76 offen offset:1024 sc1 lds\n"
    "s_waitcnt lgkmcnt(3)\n"
    "v_mfma_f32_32x32x16_bf16 v[32:47], a[156:159], v[104:107], v[32:47]\n"
    "ds_read_b128 v[100:103], v252 offset:19456\n"
    "v_mfma_f32_32x32x16_bf16 v[48:63], a[156:159], v[108:111], v[48:63]\n"
    "ds_read_b128 v[104:107], v252 offset:20480\n"
    "s_waitcnt lgkmcnt(3)\n"
    "v_mfma_f32_32x32x16_bf16 v[32:47], a[160:163], v[112:115], v[32:47]\n"
    "ds_read_b128 v[108:111], v252 offset:21504\n"
    "v_mfma_f32_32x32x16_bf16 v[48:63], a[160:163], v[116:119], v[48:63]\n"
    "ds_read_b128 v[112:115], v252 offset:22528\n"
    "s_waitcnt lgkmcnt(3)\n"
    "v_mfma_f32_32x32x16_bf16 v[32:47], a[164:167], v[96:99], v[32:47]\n"
    "ds_read_b128 v[116:119], v252 offset:23552\n"
    "v_mfma_f32_32x32x16_bf16 v[48:63], a[164:167], v[100:103], v[48:63]\n"
    "ds_read_b128 v[96:99], v252 offset:24576\n"
    "s_waitcnt lgkmcnt(3)\n"
    "v_mfma_f32_32x32x16_bf16 v[32:47], a[168:171], v[104:107], v[32:47]\n"
    "ds_read_b128 v[100:103], v252 offset:25600\n"
    "v_mfma_f32_32x32x16_bf16 v[48:63], a[168:171], v[108:111], v[48:63]\n"
    "ds_read_b128 v[104:107], v252 offset:26624\n"
    "s_waitcnt lgkmcnt(3)\n"
    "v_mfma_f32_32x32x16_bf16 v[32:47], a[172:175], v[112:115], v[32:47]\n"
    "ds_read_b128 v[108:111], v252 offset:27648\n"
    "v_mfma_f32_32x32x16_bf16 v[48:63], a[172:175], v[116:119], v[48:63]\n"
    "ds_read_b128 v[112:115], v252 offset:28672\n"
    "s_waitcnt lgkmcnt(3)\n"
    "v_mfma_f32_32x32x16_bf16 v[32:47], a[176:179], v[96:99], v[32:47]\n"
    "ds_read_b128 v[116:119], v252 offset:29696\n"
    "v_mfma_f32_32x32x16_bf16 v[48:63], a[176:179], v[100:103], v[48:63]\n"
    "ds_read_b128 v[96:99], v252 offset:30720\n"
    "s_waitcnt lgkmcnt(3)\n"
    "v_mfma_f32_32x32x16_bf16 v[32:47], a[180:183], v[104:107], v[32:47]\n"
    "ds_read_b128 v[100:103], v252 offset:31744\n"
    "v_mfma_f32_32x32x16_bf16 v[48:63], a[180:183], v[108:111], v[48:63]\n"
    "s_waitcnt vmcnt(6)\n"
    "s_barrier\n"
    "ds_read_b128 v[104:107], v252 offset:32768\n"
    "s_waitcnt lgkmcnt(3)\n"
    "v_mfma_f32_32x32x16_bf16 v[32:47], a[184:187], v[112:115], v[32:47]\n"
    "ds_read_b128 v[108:111], v252 offset:33792\n"
    "s_add_u32 m0, s81, 8192\n"
    "s_lshl_b32 s76, s69, 5\n"
    "s_add_u32 s76, s76, 0x8000\n"
    "v_mfma_f32_32x32x16_bf16 v[48:63], a[184:187], v[116:119], v[48:63]\n"
    "ds_read_b128 v[112:115], v252 offset:34816\n"
    "buffer_load_dwordx4 v248, s[56:59], s76 offen offset:0 sc1 lds\n"
    "buffer_load_dwordx4 v249, s[56:59], s76 offen offset:1024 sc1 lds\n"
    "s_waitcnt lgkmcnt(3)\n"
    "v_mfma_f32_32x32x16_bf16 v[32:47], a[188:191], v[96:99], v[32:47]\n"
    "ds_read_b128 v[116:119], v252 offset:35840\n"
    "s_add_u32 m0, s81, 16384\n"
    "s_lshl_b32 s76, s70, 5\n"
    "s_add_u32 s76, s76, 0x8000\n"
    "v_mfma_f32_32x32x16_bf16 v[48:63], a[188:191], v[100:103], v[48:63]\n"
    "ds_read_b128 v[96:99], v252 offset:36864\n"
    "buffer_load_dwordx4 v248, s[56:59], s76 offen offset:0 sc1 lds\n"
    "buffer_load_dwordx4 v249, s[56:59], s76 offen offset:1024 sc1 lds\n"
    "s_waitcnt lgkmcnt(3)\n"
    "v_mfma_f32_32x32x16_bf16 v[32:47], a[192:195], v[104:107], v[32:47]\n"
    "ds_read_b128 v[100:103], v252 offset:37888\n"
    "v_mfma_f32_32x32x16_bf16 v[48:63], a[192:195], v[108:111], v[48:63]\n"
    "ds_read_b128 v[104:107], v252 offset:38912\n"
    "s_waitcnt lgkmcnt(3)\n"
    "v_mfma_f32_32x32x16_bf16 v[32:47], a[196:199], v[112:115], v[32:47]\n"
    "ds_read_b128 v[108:111], v252 offset:39936\n"
    "v_mfma_f32_32x32x16_bf16 v[48:63], a[196:199], v[116:119], v[48:63]\n"
    "ds_read_b128 v[112:115], v252 offset:40960\n"
    "s_waitcnt lgkmcnt(3)\n"
    "v_mfma_f32_32x32x16_bf16 v[32:47], a[200:203], v[96:99], v[32:47]\n"
    "ds_read_b128 v[116:119], v252 offset:41984\n"
    "v_mfma_f32_32x32x16_bf16 v[48:63], a[200:203], v[100:103], v[48:63]\n"
    "ds_read_b128 v[96:99], v252 offset:43008\n"
    "s_waitcnt lgkmcnt(3)\n"
    "v_mfma_f32_32x32x16_bf16 v[32:47], a[204:207], v[104:107], v[32:47]\n"
    "ds_read_b128 v[100:103], v252 offset:44032\n"
    "v_mfma_f32_32x32x16_bf16 v[48:63], a[204:207], v[108:111], v[48:63]\n"
    "ds_read_b128 v[104:107], v252 offset:45056\n"
    "s_waitcnt lgkmcnt(3)\n"
    "v_mfma_f32_32x32x16_bf16 v[32:47], a[208:211], v[112:115], v[32:47]\n"
    "ds_read_b128 v[108:111], v252 offset:46080\n"
    "v_mfma_f32_32x32x16_bf16 v[48:63], a[208:211], v[116:119], v[48:63]\n"
    "ds_read_b128 v[112:115], v252 offset:47104\n"
    "s_waitcnt lgkmcnt(3)\n"
    "v_mfma_f32_32x32x16_bf16 v[32:47], a[212:215], v[96:99], v[32:47]\n"
    "ds_read_b128 v[116:119], v252 offset:48128\n"
    "v_mfma_f32_32x32x16_bf16 v[48:63], a[212:215], v[100:103], v[48:63]\n"
    "s_waitcnt vmcnt(6)\n"
    "s_barrier\n"
    "ds_read_b128 v[96:99], v252 offset:49152\n"
    "s_waitcnt lgkmcnt(3)\n"
    "v_mfma_f32_32x32x16_bf16 v[32:47], a[216:219], v[104:107], v[32:47]\n"
    "ds_read_b128 v[100:103], v252 offset:50176\n"
    "s_add_u32 m0, s81, 24576\n"
    "s_lshl_b32 s76, s71, 5\n"
    "s_add_u32 s76, s76, 0x8000\n"
    "v_mfma_f32_32x32x16_bf16 v[48:63], a[216:219], v[108:111], v[48:63]\n"
    "ds_read_b128 v[104:107], v252 offset:51200\n"
    "buffer_load_dwordx4 v248, s[56:59], s76 offen offset:0 sc1 lds\n"
    "buffer_load_dwordx4 v249, s[56:59], s76 offen offset:1024 sc1 lds\n"
    "s_waitcnt lgkmcnt(3)\n"
    "v_mfma_f32_32x32x16_bf16 v[32:47], a[220:223], v[112:115], v[32:47]\n"
    "ds_read_b128 v[108:111], v252 offset:52224\n"
    "s_add_u32 m0, s81, 32768\n"
    "s_lshl_b32 s76, s72, 5\n"
    "s_add_u32 s76, s76, 0x8000\n"
    "v_mfma_f32_32x32x16_bf16 v[48:63], a[220:223], v[116:119], v[48:63]\n"
    "ds_read_b128 v[112:115], v252 offset:53248\n"
    "buffer_load_dwordx4 v248, s[56:59], s76 offen offset:0 sc1 lds\n"
    "buffer_load_dwordx4 v249, s[56:59], s76 offen offset:1024 sc1 lds\n"
    "s_waitcnt lgkmcnt(3)\n"
    "v_mfma_f32_32x32x16_bf16 v[32:47], a[224:227], v[96:99], v[32:47]\n"
    "ds_read_b128 v[116:119], v252 offset:54272\n"
    "v_mfma_f32_32x32x16_bf16 v[48:63], a[224:227], v[100:103], v[48:63]\n"
    "ds_read_b128 v[96:99], v252 offset:55296\n"
    "s_waitcnt lgkmcnt(3)\n"
    "v_mfma_f32_32x32x16_bf16 v[32:47], a[228:231], v[104:107], v[32:47]\n"
    "ds_read_b128 v[100:103], v252 offset:56320\n"
    "v_mfma_f32_32x32x16_bf16 v[48:63], a[228:231], v[108:111], v[48:63]\n"
    "ds_read_b128 v[104:107], v252 offset:57344\n"
    "s_waitcnt lgkmcnt(3)\n"
    "v_mfma_f32_32x32x16_bf16 v[32:47], a[232:235], v[112:115], v[32:47]\n"
    "ds_read_b128 v[108:111], v252 offset:58368\n"
    "v_mfma_f32_32x32x16_bf16 v[48:63], a[232:235], v[116:119], v[48:63]\n"
    "ds_read_b128 v[112:115], v252 offset:59392\n"
    "s_waitcnt lgkmcnt(3)\n"
    "v_mfma_f32_32x32x16_bf16 v[32:47], a[236:239], v[96:99], v[32:47]\n"
    "ds_read_b128 v[116:119], v252 offset:60416\n"
    "v_mfma_f32_32x32x16_bf16 v[48:63], a[236:239], v[100:103], v[48:63]\n"
    "ds_read_b128 v[96:99], v252 offset:61440\n"
    "s_waitcnt lgkmcnt(3)\n"
    "v_mfma_f32_32x32x16_bf16 v[32:47], a[240:243], v[104:107], v[32:47]\n"
    "ds_read_b128 v[100:103], v252 offset:62464\n"
    "v_mfma_f32_32x32x16_bf16 v[48:63], a[240:243], v[108:111], v[48:63]\n"
    "ds_read_b128 v[104:107], v252 offset:63488\n"
    "s_waitcnt lgkmcnt(3)\n"
    "v_mfma_f32_32x32x16_bf16 v[32:47], a[244:247], v[112:115], v[32:47]\n"
    "ds_read_b128 v[108:111], v252 offset:64512\n"
    "v_mfma_f32_32x32x16_bf16 v[48:63], a[244:247], v[116:119], v[48:63]\n"
    "s_waitcnt vmcnt(6)\n"
    "s_barrier\n"
    "ds_read_b128 v[112:115], v252 offset:0\n"
    "s_waitcnt lgkmcnt(3)\n"
    "v_mfma_f32_32x32x16_bf16 v[32:47], a[248:251], v[96:99], v[32:47]\n"
    "ds_read_b128 v[116:119], v252 offset:1024\n"
    "s_add_u32 m0, s81, 40960\n"
    "s_lshl_b32 s76, s73, 5\n"
    "s_add_u32 s76, s76, 0x8000\n"
    "v_mfma_f32_32x32x16_bf16 v[48:63], a[248:251], v[100:103], v[48:63]\n"
    "ds_read_b128 v[96:99], v252 offset:2048\n"
    "buffer_load_dwordx4 v248, s[56:59], s76 offen offset:0 sc1 lds\n"
    "buffer_load_dwordx4 v249, s[56:59], s76 offen offset:1024 sc1 lds\n"
    "s_waitcnt lgkmcnt(3)\n"
    "v_mfma_f32_32x32x16_bf16 v[32:47], a[252:255], v[104:107], v[32:47]\n"
    "ds_read_b128 v[100:103], v252 offset:3072\n"
    "s_add_u32 m0, s81, 49152\n"
    "s_lshl_b32 s76, s74, 5\n"
    "s_add_u32 s76, s76, 0x8000\n"
    "v_mfma_f32_32x32x16_bf16 v[48:63], a[252:255], v[108:111], v[48:63]\n"
    "ds_read_b128 v[104:107], v252 offset:4096\n"
    "buffer_load_dwordx4 v248, s[56:59], s76 offen offset:0 sc1 lds\n"
    "buffer_load_dwordx4 v249, s[56:59], s76 offen offset:1024 sc1 lds\n"
    "s_waitcnt lgkmcnt(3)\n"
    "v_mfma_f32_32x32x16_bf16 v[32:47], a[0:3], v[112:115], v[32:47]\n"
    "ds_read_b128 v[108:111], v252 offset:5120\n"
    "v_mfma_f32_32x32x16_bf16 v[48:63], a[0:3], v[116:119], v[48:63]\n"
    "ds_read_b128 v[112:115], v252 offset:6144\n"
    "s_waitcnt lgkmcnt(3)\n"
    "v_mfma_f32_32x32x16_bf16 v[32:47], a[4:7], v[96:99], v[32:47]\n"
    "ds_read_b128 v[116:119], v252 offset:7168\n"
    "v_mfma_f32_32x32x16_bf16 v[48:63], a[4:7], v[100:103], v[48:63]\n"
    "ds_read_b128 v[96:99], v252 offset:8192\n"
    "s_waitcnt lgkmcnt(3)\n"
    "v_mfma_f32_32x32x16_bf16 v[32:47], a[8:11], v[104:107], v[32:47]\n"
    "ds_read_b128 v[100:103], v252 offset:9216\n"
    "v_mfma_f32_32x32x16_bf16 v[48:63], a[8:11], v[108:111], v[48:63]\n"
    "ds_read_b128 v[104:107], v252 offset:10240\n"
    "s_waitcnt lgkmcnt(3)\n"
    "v_mfma_f32_32x32x16_bf16 v[32:47], a[12:15], v[112:115], v[32:47]\n"
    "ds_read_b128 v[108:111], v252 offset:11264\n"
    "v_mfma_f32_32x32x16_bf16 v[48:63], a[12:15], v[116:119], v[48:63]\n"
    "ds_read_b128 v[112:115], v252 offset:12288\n"
    "s_waitcnt lgkmcnt(3)\n"
    "v_mfma_f32_32x32x16_bf16 v[32:47], a[16:19], v[96:99], v[32:47]\n"
    "ds_read_b128 v[116:119], v252 offset:13312\n"
    "v_mfma_f32_32x32x16_bf16 v[48:63], a[16:19], v[100:103], v[48:63]\n"
    "ds_read_b128 v[96:99], v252 offset:14336\n"
    "s_waitcnt lgkmcnt(3)\n"
    "v_mfma_f32_32x32x16_bf16 v[32:47], a[20:23], v[104:107], v[32:47]\n"
    "ds_read_b128 v[100:103], v252 offset:15360\n"
    "v_mfma_f32_32x32x16_bf16 v[48:63], a[20:23], v[108:111], v[48:63]\n"
    "s_waitcnt vmcnt(6)\n"
    "s_barrier\n"
    "ds_read_b128 v[104:107], v252 offset:16384\n"
    "s_waitcnt lgkmcnt(3)\n"
    "v_mfma_f32_32x32x16_bf16 v[32:47], a[24:27], v[112:115], v[32:47]\n"
    "ds_read_b128 v[108:111], v252 offset:17408\n"
    "s_add_u32 m0, s81, 57344\n"
    "s_lshl_b32 s76, s75, 5\n"
    "s_add_u32 s76, s76, 0x8000\n"
    "v_mfma_f32_32x32x16_bf16 v[48:63], a[24:27], v[116:119], v[48:63]\n"
    "ds_read_b128 v[112:115], v252 offset:18432\n"
    "buffer_load_dwordx4 v248, s[56:59], s76 offen offset:0 sc1 lds\n"
    "buffer_load_dwordx4 v249, s[56:59], s76 offen offset:1024 sc1 lds\n"
    "s_waitcnt lgkmcnt(3)\n"
    "v_mfma_f32_32x32x16_bf16 v[32:47], a[28:31], v[96:99], v[32:47]\n"
    "ds_read_b128 v[116:119], v252 offset:19456\n"
    "s_add_u32 m0, s81, 0\n"
    "s_lshl_b32 s76, s68, 5\n"
    "buffer_load_dwordx4 v250, s[56:59], s76 offen offset:0 sc1 lds\n"
    "v_mfma_f32_32x32x16_bf16 v[48:63], a[28:31], v[100:103], v[48:63]\n"
    "ds_read_b128 v[96:99], v252 offset:20480\n"
    "buffer_load_dwordx4 v251, s[56:59], s76 offen offset:1024 sc1 lds\n"
    "s_waitcnt lgkmcnt(3)\n"
    "v_mfma_f32_32x32x16_bf16 v[32:47], a[32:35], v[104:107], v[32:47]\n"
    "ds_read_b128 v[100:103], v252 offset:21504\n"
    "v_mfma_f32_32x32x16_bf16 v[48:63], a[32:35], v[108:111], v[48:63]\n"
    "ds_read_b128 v[104:107], v252 offset:22528\n"
    "s_waitcnt lgkmcnt(3)\n"
    "v_mfma_f32_32x32x16_bf16 v[32:47], a[36:39], v[112:115], v[32:47]\n"
    "ds_read_b128 v[108:111], v252 offset:23552\n"
    "v_mfma_f32_32x32x16_bf16 v[48:63], a[36:39], v[116:119], v[48:63]\n"
    "ds_read_b128 v[112:115], v252 offset:24576\n"
    "s_waitcnt lgkmcnt(3)\n"
    "v_mfma_f32_32x32x16_bf16 v[32:47], a[40:43], v[96:99], v[32:47]\n"
    "ds_read_b128 v[116:119], v252 offset:25600\n"
    "v_mfma_f32_32x32x16_bf16 v[48:63], a[40:43], v[100:103], v[48:63]\n"
    "ds_read_b128 v[96:99], v252 offset:26624\n"
    "s_waitcnt lgkmcnt(3)\n"
    "v_mfma_f32_32x32x16_bf16 v[32:47], a[44:47], v[104:107], v[32:47]\n"
    "ds_read_b128 v[100:103], v252 offset:27648\n"
    "v_mfma_f32_32x32x16_bf16 v[48:63], a[44:47], v[108:111], v[48:63]\n"
    "ds_read_b128 v[104:107], v252 offset:28672\n"
    "s_waitcnt lgkmcnt(3)\n"
    "v_mfma_f32_32x32x16_bf16 v[32:47], a[48:51], v[112:115], v[32:47]\n"
    "ds_read_b128 v[108:111], v252 offset:29696\n"
    "v_mfma_f32_32x32x16_bf16 v[48:63], a[48:51], v[116:119], v[48:63]\n"
    "ds_read_b128 v[112:115], v252 offset:30720\n"
    "s_waitcnt lgkmcnt(3)\n"
    "v_mfma_f32_32x32x16_bf16 v[32:47], a[52:55], v[96:99], v[32:47]\n"
    "ds_read_b128 v[116:119], v252 offset:31744\n"
    "v_mfma_f32_32x32x16_bf16 v[48:63], a[52:55], v[100:103], v[48:63]\n"
    "s_waitcnt vmcnt(6)\n"
    "s_barrier\n"
    "ds_read_b128 v[96:99], v252 offset:32768\n"
    "s_waitcnt lgkmcnt(3)\n"
    "v_mfma_f32_32x32x16_bf16 v[32:47], a[56:59], v[104:107], v[32:47]\n"
    "ds_read_b128 v[100:103], v252 offset:33792\n"
    "s_add_u32 m0, s81, 8192\n"
    "s_lshl_b32 s76, s69, 5\n"
    "buffer_load_dwordx4 v250, s[56:59], s76 offen offset:0 sc1 lds\n"
    "v_mfma_f32_32x32x16_bf16 v[48:63], a[56:59], v[108:111], v[48:63]\n"
    "ds_read_b128 v[104:107], v252 offset:34816\n"
    "buffer_load_dwordx4 v251, s[56:59], s76 offen offset:1024 sc1 lds\n"
    "s_add_u32 m0, s81, 16384\n"
    "s_lshl_b32 s76, s70, 5\n"
    "s_waitcnt lgkmcnt(3)\n"
    "v_mfma_f32_32x32x16_bf16 v[32:47], a[60:63], v[112:115], v[32:47]\n"
    "ds_read_b128 v[108:111], v252 offset:35840\n"
    "buffer_load_dwordx4 v250, s[56:59], s76 offen offset:0 sc1 lds\n"
    "buffer_load_dwordx4 v251, s[56:59], s76 offen offset:1024 sc1 lds\n"
    "v_mfma_f32_32x32x16_bf16 v[48:63], a[60:63], v[116:119], v[48:63]\n"
    "ds_read_b128 v[112:115], v252 offset:36864\n"
    "s_waitcnt lgkmcnt(3)\n"
    "v_mfma_f32_32x32x16_bf16 v[32:47], a[64:67], v[96:99], v[32:47]\n"
    "ds_read_b128 v[116:119], v252 offset:37888\n"
    "v_mfma_f32_32x32x16_bf16 v[48:63], a[64:67], v[100:103], v[48:63]\n"
    "ds_read_b128 v[96:99], v252 offset:38912\n"
    "s_waitcnt lgkmcnt(3)\n"
    "v_mfma_f32_32x32x16_bf16 v[32:47], a[68:71], v[104:107], v[32:47]\n"
    "ds_read_b128 v[100:103], v252 offset:39936\n"
    "v_mfma_f32_32x32x16_bf16 v[48:63], a[68:71], v[108:111], v[48:63]\n"
    "ds_read_b128 v[104:107], v252 offset:40960\n"
    "s_waitcnt lgkmcnt(3)\n"
    "v_mfma_f32_32x32x16_bf16 v[32:47], a[72:75], v[112:115], v[32:47]\n"
    "ds_read_b128 v[108:111], v252 offset:41984\n"
    "v_mfma_f32_32x32x16_bf16 v[48:63], a[72:75], v[116:119], v[48:63]\n"
    "ds_read_b128 v[112:115], v252 offset:43008\n"
    "s_waitcnt lgkmcnt(3)\n"
    "v_mfma_f32_32x32x16_bf16 v[32:47], a[76:79], v[96:99], v[32:47]\n"
    "ds_read_b128 v[116:119], v252 offset:44032\n"
    "v_mfma_f32_32x32x16_bf16 v[48:63], a[76:79], v[100:103], v[48:63]\n"
    "ds_read_b128 v[96:99], v252 offset:45056\n"
    "s_waitcnt lgkmcnt(3)\n"
    "v_mfma_f32_32x32x16_bf16 v[32:47], a[80:83], v[104:107], v[32:47]\n"
    "ds_read_b128 v[100:103], v252 offset:46080\n"
    "v_mfma_f32_32x32x16_bf16 v[48:63], a[80:83], v[108:111], v[48:63]\n"
    "ds_read_b128 v[104:107], v252 offset:47104\n"
    "s_waitcnt lgkmcnt(3)\n"
    "v_mfma_f32_32x32x16_bf16 v[32:47], a[84:87], v[112:115], v[32:47]\n"
    "ds_read_b128 v[108:111], v252 offset:48128\n"
    "v_mfma_f32_32x32x16_bf16 v[48:63], a[84:87], v[116:119], v[48:63]\n"
    "s_waitcnt vmcnt(6)\n"
    "s_barrier\n"
    "ds_read_b128 v[112:115], v252 offset:49152\n"
    "s_waitcnt lgkmcnt(3)\n"
    "v_mfma_f32_32x32x16_bf16 v[32:47], a[88:91], v[96:99], v[32:47]\n"
    "ds_read_b128 v[116:119], v252 offset:50176\n"
    "s_add_u32 m0, s81, 24576\n"
    "s_lshl_b32 s76, s71, 5\n"
    "buffer_load_dwordx4 v250, s[56:59], s76 offen offset:0 sc1 lds\n"
    "v_mfma_f32_32x32x16_bf16 v[48:63], a[88:91], v[100:103], v[48:63]\n"
    "ds_read_b128 v[96:99], v252 offset:51200\n"
    "buffer_load_dwordx4 v251, s[56:59], s76 offen offset:1024 sc1 lds\n"
    "s_add_u32 m0, s81, 32768\n"
    "s_lshl_b32 s76, s72, 5\n"
    "s_waitcnt lgkmcnt(3)\n"
    "v_mfma_f32_32x32x16_bf16 v[32:47], a[92:95], v[104:107], v[32:47]\n"
    "ds_read_b128 v[100:103], v252 offset:52224\n"
    "buffer_load_dwordx4 v250, s[56:59], s76 offen offset:0 sc1 lds\n"
    "buffer_load_dwordx4 v251, s[56:59], s76 offen offset:1024 sc1 lds\n"
    "v_mfma_f32_32x32x16_bf16 v[48:63], a[92:95], v[108:111], v[48:63]\n"
    "ds_read_b128 v[104:107], v252 offset:53248\n"
    "s_waitcnt lgkmcnt(3)\n"
    "v_mfma_f32_32x32x16_bf16 v[32:47], a[96:99], v[112:115], v[32:47]\n"
    "ds_read_b128 v[108:111], v252 offset:54272\n"
    "v_mfma_f32_32x32x16_bf16 v[48:63], a[96:99], v[116:119], v[48:63]\n"
    "ds_read_b128 v[112:115], v252 offset:55296\n"
    "s_waitcnt lgkmcnt(3)\n"
    "v_mfma_f32_32x32x16_bf16 v[32:47], a[100:103], v[96:99], v[32:47]\n"
    "ds_read_b128 v[116:119], v252 offset:56320\n"
    "v_mfma_f32_32x32x16_bf16 v[48:63], a[100:103], v[100:103], v[48:63]\n"
    "ds_read_b128 v[96:99], v252 offset:57344\n"
    "s_waitcnt lgkmcnt(3)\n"
    "v_mfma_f32_32x32x16_bf16 v[32:47], a[104:107], v[104:107], v[32:47]\n"
    "ds_read_b128 v[100:103], v252 offset:58368\n"
    "v_mfma_f32_32x32x16_bf16 v[48:63], a[104:107], v[108:111], v[48:63]\n"
    "ds_read_b128 v[104:107], v252 offset:59392\n"
    "s_waitcnt lgkmcnt(3)\n"
    "v_mfma_f32_32x32x16_bf16 v[32:47], a[108:111], v[112:115], v[32:47]\n"
    "ds_read_b128 v[108:111], v252 offset:60416\n"
    "v_mfma_f32_32x32x16_bf16 v[48:63], a[108:111], v[116:119], v[48:63]\n"
    "ds_read_b128 v[112:115], v252 offset:61440\n"
    "s_waitcnt lgkmcnt(3)\n"
    "v_mfma_f32_32x32x16_bf16 v[32:47], a[112:115], v[96:99], v[32:47]\n"
    "ds_read_b128 v[116:119], v252 offset:62464\n"
    "v_mfma_f32_32x32x16_bf16 v[48:63], a[112:115], v[100:103], v[48:63]\n"
    "ds_read_b128 v[96:99], v252 offset:63488\n"
    "s_waitcnt lgkmcnt(3)\n"
    "v_mfma_f32_32x32x16_bf16 v[32:47], a[116:119], v[104:107], v[32:47]\n"
    "ds_read_b128 v[100:103], v252 offset:64512\n"
    "v_mfma_f32_32x32x16_bf16 v[48:63], a[116:119], v[108:111], v[48:63]\n"
    "s_waitcnt vmcnt(6)\n"
    "s_barrier\n"
    "ds_read_b128 v[104:107], v252 offset:0\n"
    "s_waitcnt lgkmcnt(3)\n"
    "v_mfma_f32_32x32x16_bf16 v[32:47], a[120:123], v[112:115], v[32:47]\n"
    "ds_read_b128 v[108:111], v252 offset:1024\n"
    "s_add_u32 m0, s81, 40960\n"
    "s_lshl_b32 s76, s73, 5\n"
    "buffer_load_dwordx4 v250, s[56:59], s76 offen offset:0 sc1 lds\n"
    "v_mfma_f32_32x32x16_bf16 v[48:63], a[120:123], v[116:119], v[48:63]\n"
    "ds_read_b128 v[112:115], v252 offset:2048\n"
    "buffer_load_dwordx4 v251, s[56:59], s76 offen offset:1024 sc1 lds\n"
    "s_add_u32 m0, s81, 49152\n"
    "s_lshl_b32 s76, s74, 5\n"
    "s_waitcnt lgkmcnt(3)\n"
    "v_mfma_f32_32x32x16_bf16 v[32:47], a[124:127], v[96:99], v[32:47]\n"
    "ds_read_b128 v[116:119], v252 offset:3072\n"
    "buffer_load_dwordx4 v250, s[56:59], s76 offen offset:0 sc1 lds\n"
    "buffer_load_dwordx4 v251, s[56:59], s76 offen offset:1024 sc1 lds\n"
    "v_mfma_f32_32x32x16_bf16 v[48:63], a[124:127], v[100:103], v[48:63]\n"
    "ds_read_b128 v[96:99], v252 offset:4096\n"
    "s_waitcnt lgkmcnt(3)\n"
    "v_mfma_f32_32x32x16_bf16 v[64:79], a[128:131], v[104:107], 0\n"
    "ds_read_b128 v[100:103], v252 offset:5120\n"
    "v_mfma_f32_32x32x16_bf16 v[80:95], a[128:131], v[108:111], 0\n"
    "ds_read_b128 v[104:107], v252 offset:6144\n"
    "s_waitcnt lgkmcnt(3)\n"
    "v_mfma_f32_32x32x16_bf16 v[64:79], a[132:135], v[112:115], v[64:79]\n"
    "ds_read_b128 v[108:111], v252 offset:7168\n"
    "v_mfma_f32_32x32x16_bf16 v[80:95], a[132:135], v[116:119], v[80:95]\n"
    "ds_read_b128 v[112:115], v252 offset:8192\n"
    "v_cvt_pk_bf16_f32 v32, v32, v33\n"
    "v_cvt_pk_bf16_f32 v33, v34, v35\n"
    "v_cvt_pk_bf16_f32 v34, v36, v37\n"
    "v_cvt_pk_bf16_f32 v35, v38, v39\n"
    "s_waitcnt lgkmcnt(3)\n"
    "v_mfma_f32_32x32x16_bf16 v[64:79], a[136:139], v[96:99], v[64:79]\n"
    "ds_read_b128 v[116:119], v252 offset:9216\n"
    "v_cvt_pk_bf16_f32 v36, v40, v41\n"
    "v_cvt_pk_bf16_f32 v37, v42, v43\n"
    "v_cvt_pk_bf16_f32 v38, v44, v45\n"
    "v_cvt_pk_bf16_f32 v39, v46, v47\n"
    "v_mfma_f32_32x32x16_bf16 v[80:95], a[136:139], v[100:103], v[80:95]\n"
    "ds_read_b128 v[96:99], v252 offset:10240\n"
    "buffer_store_dwordx4 v[32:35], v22, s[56:59], s97 offen offset:0 sc1\n"
    "buffer_store_dwordx4 v[36:39], v22, s[56:59], s97 offen offset:1024 sc1\n"
    "s_add_u32 s97, s97, 0x8000\n"
    "v_cvt_pk_bf16_f32 v48, v48, v49\n"
    "s_waitcnt lgkmcnt(3)\n"
    "v_mfma_f32_32x32x16_bf16 v[64:79], a[140:143], v[104:107], v[64:79]\n"
    "ds_read_b128 v[100:103], v252 offset:11264\n"
    "v_cvt_pk_bf16_f32 v49, v50, v51\n"
    "v_cvt_pk_bf16_f32 v50, v52, v53\n"
    "v_cvt_pk_bf16_f32 v51, v54, v55\n"
    "v_cvt_pk_bf16_f32 v52, v56, v57\n"
    "v_mfma_f32_32x32x16_bf16 v[80:95], a[140:143], v[108:111], v[80:95]\n"
    "ds_read_b128 v[104:107], v252 offset:12288\n"
    "v_cvt_pk_bf16_f32 v53, v58, v59\n"
    "v_cvt_pk_bf16_f32 v54, v60, v61\n"
    "v_cvt_pk_bf16_f32 v55, v62, v63\n"
    "buffer_store_dwordx4 v[48:51], v22, s[56:59], s97 offen offset:0 sc1\n"
    "s_waitcnt lgkmcnt(3)\n"
    "v_mfma_f32_32x32x16_bf16 v[64:79], a[144:147], v[112:115], v[64:79]\n"
    "ds_read_b128 v[108:111], v252 offset:13312\n"
    "buffer_store_dwordx4 v[52:55], v22, s[56:59], s97 offen offset:1024 sc1\n"
    "s_add_u32 s97, s97, 0x8000\n"
    "v_mfma_f32_32x32x16_bf16 v[80:95], a[144:147], v[116:119], v[80:95]\n"
    "ds_read_b128 v[112:115], v252 offset:14336\n"
    "s_waitcnt lgkmcnt(3)\n"
    "v_mfma_f32_32x32x16_bf16 v[64:79], a[148:151], v[96:99], v[64:79]\n"
    "ds_read_b128 v[116:119], v252 offset:15360\n"
    "v_mfma_f32_32x32x16_bf16 v[80:95], a[148:151], v[100:103], v[80:95]\n"
    "s_waitcnt vmcnt(10)\n"
    "s_barrier\n"
    "ds_read_b128 v[96:99], v252 offset:16384\n"
    "s_waitcnt lgkmcnt(3)\n"
    "v_mfma_f32_32x32x16_bf16 v[64:79], a[152:155], v[104:107], v[64:79]\n"
    "ds_read_b128 v[100:103], v252 offset:17408\n"
    "v_mfma_f32_32x32x16_bf16 v[80:95], a[152:155], v[108:111], v[80:95]\n"
    "ds_read_b128 v[104:107], v252 offset:18432\n"
    "s_waitcnt lgkmcnt(3)\n"
    "v_mfma_f32_32x32x16_bf16 v[64:79], a[156:159], v[112:115], v[64:79]\n"
    "ds_read_b128 v[108:111], v252 offset:19456\n"
    "v_mfma_f32_32x32x16_bf16 v[80:95], a[156:159], v[116:119], v[80:95]\n"
    "ds_read_b128 v[112:115], v252 offset:20480\n"
    "s_add_u32 m0, s81, 57344\n"
    "s_lshl_b32 s76, s75, 5\n"
    "buffer_load_dwordx4 v250, s[56:59], s76 offen offset:0 sc1 lds\n"
    "s_waitcnt lgkmcnt(3)\n"
    "v_mfma_f32_32x32x16_bf16 v[64:79], a[160:163], v[96:99], v[64:79]\n"
    "ds_read_b128 v[116:119], v252 offset:21504\n"
    "v_mfma_f32_32x32x16_bf16 v[80:95], a[160:163], v[100:103], v[80:95]\n"
    "ds_read_b128 v[96:99], v252 offset:22528\n"
    "s_waitcnt lgkmcnt(3)\n"
    "v_mfma_f32_32x32x16_bf16 v[64:79], a[164:167], v[104:107], v[64:79]\n"
    "ds_read_b128 v[100:103], v252 offset:23552\n"
    "v_mfma_f32_32x32x16_bf16 v[80:95], a[164:167], v[108:111], v[80:95]\n"
    "ds_read_b128 v[104:107], v252 offset:24576\n"
    "s_waitcnt lgkmcnt(3)\n"
    "v_mfma_f32_32x32x16_bf16 v[64:79], a[168:171], v[112:115], v[64:79]\n"
    "ds_read_b128 v[108:111], v252 offset:25600\n"
    "buffer_load_dwordx4 v251, s[56:59], s76 offen offset:1024 sc1 lds\n"
    "v_mfma_f32_32x32x16_bf16 v[80:95], a[168:171], v[116:119], v[80:95]\n"
    "ds_read_b128 v[112:115], v252 offset:26624\n"
    "s_waitcnt lgkmcnt(3)\n"
    "v_mfma_f32_32x32x16_bf16 v[64:79], a[172:175], v[96:99], v[64:79]\n"
    "ds_read_b128 v[116:119], v252 offset:27648\n"
    "v_mfma_f32_32x32x16_bf16 v[80:95], a[172:175], v[100:103], v[80:95]\n"
    "ds_read_b128 v[96:99], v252 offset:28672\n"
    "s_waitcnt lgkmcnt(3)\n"
    "v_mfma_f32_32x32x16_bf16 v[64:79], a[176:179], v[104:107], v[64:79]\n"
    "ds_read_b128 v[100:103], v252 offset:29696\n"
    "s_add_u32 m0, s81, 0\n"
    "s_lshl_b32 s76, s68, 5\n"
    "s_add_u32 s76, s76, 0x8000\n"
    "v_mfma_f32_32x32x16_bf16 v[80:95], a[176:179], v[108:111], v[80:95]\n"
    "ds_read_b128 v[104:107], v252 offset:30720\n"
    "buffer_load_dwordx4 v250, s[56:59], s76 offen offset:0 sc1 lds\n"
    "s_waitcnt lgkmcnt(3)\n"
    "v_mfma_f32_32x32x16_bf16 v[64:79], a[180:183], v[112:115], v[64:79]\n"
    "ds_read_b128 v[108:111], v252 offset:31744\n"
    "v_mfma_f32_32x32x16_bf16 v[80:95], a[180:183], v[116:119], v[80:95]\n"
    "buffer_load_dwordx4 v251, s[56:59], s76 offen offset:1024 sc1 lds\n"
    "s_waitcnt vmcnt(10)\n"
    "s_barrier\n"
    "ds_read_b128 v[112:115], v252 offset:32768\n"
    "s_waitcnt lgkmcnt(3)\n"
    "v_mfma_f32_32x32x16_bf16 v[64:79], a[184:187], v[96:99], v[64:79]\n"
    "ds_read_b128 v[116:119], v252 offset:33792\n"
    "v_mfma_f32_32x32x16_bf16 v[80:95], a[184:187], v[100:103], v[80:95]\n"
    "ds_read_b128 v[96:99], v252 offset:34816\n"
    "s_waitcnt lgkmcnt(3)\n"
    "v_mfma_f32_32x32x16_bf16 v[64:79], a[188:191], v[104:107], v[64:79]\n"
    "ds_read_b128 v[100:103], v252 offset:35840\n"
    "s_add_u32 m0, s81, 8192\n"
    "s_lshl_b32 s76, s69, 5\n"
    "s_add_u32 s76, s76, 0x8000\n"
    "v_mfma_f32_32x32x16_bf16 v[80:95], a[188:191], v[108:111], v[80:95]\n"
    "ds_read_b128 v[104:107], v252 offset:36864\n"
    "buffer_load_dwordx4 v250, s[56:59], s76 offen offset:0 sc1 lds\n"
    "s_waitcnt lgkmcnt(3)\n"
    "v_mfma_f32_32x32x16_bf16 v[64:79], a[192:195], v[112:115], v[64:79]\n"
    "ds_read_b128 v[108:111], v252 offset:37888\n"
    "v_mfma_f32_32x32x16_bf16 v[80:95], a[192:195], v[116:119], v[80:95]\n"
    "ds_read_b128 v[112:115], v252 offset:38912\n"
    "s_waitcnt lgkmcnt(3)\n"
    "v_mfma_f32_32x32x16_bf16 v[64:79], a[196:199], v[96:99], v[64:79]\n"
    "ds_read_b128 v[116:119], v252 offset:39936\n"
    "v_mfma_f32_32x32x16_bf16 v[80:95], a[196:199], v[100:103], v[80:95]\n"
    "ds_read_b128 v[96:99], v252 offset:40960\n"
    "s_waitcnt lgkmcnt(3)\n"
    "v_mfma_f32_32x32x16_bf16 v[64:79], a[200:203], v[104:107], v[64:79]\n"
    "ds_read_b128 v[100:103], v252 offset:41984\n"
    "v_mfma_f32_32x32x16_bf16 v[80:95], a[200:203], v[108:111], v[80:95]\n"
    "ds_read_b128 v[104:107], v252 offset:43008\n"
    "s_waitcnt lgkmcnt(3)\n"
    "v_mfma_f32_32x32x16_bf16 v[64:79], a[204:207], v[112:115], v[64:79]\n"
    "ds_read_b128 v[108:111], v252 offset:44032\n"
    "v_mfma_f32_32x32x16_bf16 v[80:95], a[204:207], v[116:119], v[80:95]\n"
    "ds_read_b128 v[112:115], v252 offset:45056\n"
    "s_waitcnt lgkmcnt(3)\n"
    "v_mfma_f32_32x32x16_bf16 v[64:79], a[208:211], v[96:99], v[64:79]\n"
    "ds_read_b128 v[116:119], v252 offset:46080\n"
    "buffer_load_dwordx4 v251, s[56:59], s76 offen offset:1024 sc1 lds\n"
    "v_mfma_f32_32x32x16_bf16 v[80:95], a[208:211], v[100:103], v[80:95]\n"
    "ds_read_b128 v[96:99], v252 offset:47104\n"
    "s_waitcnt lgkmcnt(3)\n"
    "v_mfma_f32_32x32x16_bf16 v[64:79], a[212:215], v[104:107], v[64:79]\n"
    "ds_read_b128 v[100:103], v252 offset:48128\n"
    "v_mfma_f32_32x32x16_bf16 v[80:95], a[212:215], v[108:111], v[80:95]\n"
    "s_add_u32 m0, s81, 16384\n"
    "s_lshl_b32 s76, s70, 5\n"
    "s_add_u32 s76, s76, 0x8000\n"
    "buffer_load_dwordx4 v250, s[56:59], s76 offen offset:0 sc1 lds\n"
    "buffer_load_dwordx4 v251, s[56:59], s76 offen offset:1024 sc1 lds\n"
    "s_waitcnt vmcnt(6)\n"
    "s_barrier\n"
    "ds_read_b128 v[104:107], v252 offset:49152\n"
    "s_waitcnt lgkmcnt(3)\n"
    "v_mfma_f32_32x32x16_bf16 v[64:79], a[216:219], v[112:115], v[64:79]\n"
    "ds_read_b128 v[108:111], v252 offset:50176\n"
    "s_add_u32 m0, s81, 24576\n"
    "s_lshl_b32 s76, s71, 5\n"
    "s_add_u32 s76, s76, 0x8000\n"
    "v_mfma_f32_32x32x16_bf16 v[80:95], a[216:219], v[116:119], v[80:95]\n"
    "ds_read_b128 v[112:115], v252 offset:51200\n"
    "buffer_load_dwordx4 v250, s[56:59], s76 offen offset:0 sc1 lds\n"
    "buffer_load_dwordx4 v251, s[56:59], s76 offen offset:1024 sc1 lds\n"
    "s_waitcnt lgkmcnt(3)\n"
    "v_mfma_f32_32x32x16_bf16 v[64:79], a[220:223], v[96:99], v[64:79]\n"
    "ds_read_b128 v[116:119], v252 offset:52224\n"
    "s_add_u32 m0, s81, 32768\n"
    "s_lshl_b32 s76, s72, 5\n"
    "s_add_u32 s76, s76, 0x8000\n"
    "v_mfma_f32_32x32x16_bf16 v[80:95], a[220:223], v[100:103], v[80:95]\n"
    "ds_read_b128 v[96:99], v252 offset:53248\n"
    "buffer_load_dwordx4 v250, s[56:59], s76 offen offset:0 sc1 lds\n"
    "buffer_load_dwordx4 v251, s[56:59], s76 offen offset:1024 sc1 lds\n"
    "s_waitcnt lgkmcnt(3)\n"
    "v_mfma_f32_32x32x16_bf16 v[64:79], a[224:227], v[104:107], v[64:79]\n"
    "ds_read_b128 v[100:103], v252 offset:54272\n"
    "v_mfma_f32_32x32x16_bf16 v[80:95], a[224:227], v[108:111], v[80:95]\n"
    "ds_read_b128 v[104:107], v252 offset:55296\n"
    "s_waitcnt lgkmcnt(3)\n"
    "v_mfma_f32_32x32x16_bf16 v[64:79], a[228:231], v[112:115], v[64:79]\n"
    "ds_read_b128 v[108:111], v252 offset:56320\n"
    "v_mfma_f32_32x32x16_bf16 v[80:95], a[228:231], v[116:119], v[80:95]\n"
    "ds_read_b128 v[112:115], v252 offset:57344\n"
    "s_waitcnt lgkmcnt(3)\n"
    "v_mfma_f32_32x32x16_bf16 v[64:79], a[232:235], v[96:99], v[64:79]\n"
    "ds_read_b128 v[116:119], v252 offset:58368\n"
    "v_mfma_f32_32x32x16_bf16 v[80:95], a[232:235], v[100:103], v[80:95]\n"
    "ds_read_b128 v[96:99], v252 offset:59392\n"
    "s_waitcnt lgkmcnt(3)\n"
    "v_mfma_f32_32x32x16_bf16 v[64:79], a[236:239], v[104:107], v[64:79]\n"
    "ds_read_b128 v[100:103], v252 offset:60416\n"
    "v_mfma_f32_32x32x16_bf16 v[80:95], a[236:239], v[108:111], v[80:95]\n"
    "ds_read_b128 v[104:107], v252 offset:61440\n"
    "s_waitcnt lgkmcnt(3)\n"
    "v_mfma_f32_32x32x16_bf16 v[64:79], a[240:243], v[112:115], v[64:79]\n"
    "ds_read_b128 v[108:111], v252 offset:62464\n"
    "v_mfma_f32_32x32x16_bf16 v[80:95], a[240:243], v[116:119], v[80:95]\n"
    "ds_read_b128 v[112:115], v252 offset:63488\n"
    "s_waitcnt lgkmcnt(3)\n"
    "v_mfma_f32_32x32x16_bf16 v[64:79], a[244:247], v[96:99], v[64:79]\n"
    "ds_read_b128 v[116:119], v252 offset:64512\n"
    "v_mfma_f32_32x32x16_bf16 v[80:95], a[244:247], v[100:103], v[80:95]\n"
    "s_waitcnt vmcnt(6)\n"
    "s_barrier\n"
    "ds_read_b128 v[96:99], v252 offset:0\n"
    "s_waitcnt lgkmcnt(3)\n"
    "v_mfma_f32_32x32x16_bf16 v[64:79], a[248:251], v[104:107], v[64:79]\n"
    "ds_read_b128 v[100:103], v252 offset:1024\n"
    "s_add_u32 m0, s81, 40960\n"
    "s_lshl_b32 s76, s73, 5\n"
    "s_add_u32 s76, s76, 0x8000\n"
    "v_mfma_f32_32x32x16_bf16 v[80:95], a[248:251], v[108:111], v[80:95]\n"
    "ds_read_b128 v[104:107], v252 offset:2048\n"
    "buffer_load_dwordx4 v250, s[56:59], s76 offen offset:0 sc1 lds\n"
    "buffer_load_dwordx4 v251, s[56:59], s76 offen offset:1024 sc1 lds\n"
    "s_waitcnt lgkmcnt(3)\n"
    "v_mfma_f32_32x32x16_bf16 v[64:79], a[252:255], v[112:115], v[64:79]\n"
    "ds_read_b128 v[108:111], v252 offset:3072\n"
    "s_add_u32 m0, s81, 49152\n"
    "s_lshl_b32 s76, s74, 5\n"
    "s_add_u32 s76, s76, 0x8000\n"
    "v_mfma_f32_32x32x16_bf16 v[80:95], a[252:255], v[116:119], v[80:95]\n"
    "ds_read_b128 v[112:115], v252 offset:4096\n"
    "buffer_load_dwordx4 v250, s[56:59], s76 offen offset:0 sc1 lds\n"
    "buffer_load_dwordx4 v251, s[56:59], s76 offen offset:1024 sc1 lds\n"
    "s_waitcnt lgkmcnt(3)\n"
    "v_mfma_f32_32x32x16_bf16 v[64:79], a[0:3], v[96:99], v[64:79]\n"
    "ds_read_b128 v[116:119], v252 offset:5120\n"
    "v_mfma_f32_32x32x16_bf16 v[80:95], a[0:3], v[100:103], v[80:95]\n"
    "ds_read_b128 v[96:99], v252 offset:6144\n"
    "s_waitcnt lgkmcnt(3)\n"
    "v_mfma_f32_32x32x16_bf16 v[64:79], a[4:7], v[104:107], v[64:79]\n"
    "ds_read_b128 v[100:103], v252 offset:7168\n"
    "v_mfma_f32_32x32x16_bf16 v[80:95], a[4:7], v[108:111], v[80:95]\n"
    "ds_read_b128 v[104:107], v252 offset:8192\n"
    "s_waitcnt lgkmcnt(3)\n"
    "v_mfma_f32_32x32x16_bf16 v[64:79], a[8:11], v[112:115], v[64:79]\n"
    "ds_read_b128 v[108:111], v252 offset:9216\n"
    "v_mfma_f32_32x32x16_bf16 v[80:95], a[8:11], v[116:119], v[80:95]\n"
    "ds_read_b128 v[112:115], v252 offset:10240\n"
    "s_waitcnt lgkmcnt(3)\n"
    "v_mfma_f32_32x32x16_bf16 v[64:79], a[12:15], v[96:99], v[64:79]\n"
    "ds_read_b128 v[116:119], v252 offset:11264\n"
    "v_mfma_f32_32x32x16_bf16 v[80:95], a[12:15], v[100:103], v[80:95]\n"
    "ds_read_b128 v[96:99], v252 offset:12288\n"
    "s_waitcnt lgkmcnt(3)\n"
    "v_mfma_f32_32x32x16_bf16 v[64:79], a[16:19], v[104:107], v[64:79]\n"
    "ds_read_b128 v[100:103], v252 offset:13312\n"
    "v_mfma_f32_32x32x16_bf16 v[80:95], a[16:19], v[108:111], v[80:95]\n"
    "ds_read_b128 v[104:107], v252 offset:14336\n"
    "s_waitcnt lgkmcnt(3)\n"
    "v_mfma_f32_32x32x16_bf16 v[64:79], a[20:23], v[112:115], v[64:79]\n"
    "ds_read_b128 v[108:111], v252 offset:15360\n"
    "v_mfma_f32_32x32x16_bf16 v[80:95], a[20:23], v[116:119], v[80:95]\n"
    "s_waitcnt vmcnt(6)\n"
    "s_barrier\n"
    "ds_read_b128 v[112:115], v252 offset:16384\n"
    "s_waitcnt lgkmcnt(3)\n"
    "v_mfma_f32_32x32x16_bf16 v[64:79], a[24:27], v[96:99], v[64:79]\n"
    "ds_read_b128 v[116:119], v252 offset:17408\n"
    "s_add_u32 m0, s81, 57344\n"
    "s_lshl_b32 s76, s75, 5\n"
    "s_add_u32 s76, s76, 0x8000\n"
    "v_mfma_f32_32x32x16_bf16 v[80:95], a[24:27], v[100:103], v[80:95]\n"
    "ds_read_b128 v[96:99], v252 offset:18432\n"
    "buffer_load_dwordx4 v250, s[56:59], s76 offen offset:0 sc1 lds\n"
    "buffer_load_dwordx4 v251, s[56:59], s76 offen offset:1024 sc1 lds\n"
    "s_waitcnt lgkmcnt(3)\n"
    "v_mfma_f32_32x32x16_bf16 v[64:79], a[28:31], v[104:107], v[64:79]\n"
    "ds_read_b128 v[100:103], v252 offset:19456\n"
    "v_mfma_f32_32x32x16_bf16 v[80:95], a[28:31], v[108:111], v[80:95]\n"
    "ds_read_b128 v[104:107], v252 offset:20480\n"
    "s_waitcnt lgkmcnt(3)\n"
    "v_mfma_f32_32x32x16_bf16 v[64:79], a[32:35], v[112:115], v[64:79]\n"
    "ds_read_b128 v[108:111], v252 offset:21504\n"
    "v_mfma_f32_32x32x16_bf16 v[80:95], a[32:35], v[116:119], v[80:95]\n"
    "ds_read_b128 v[112:115], v252 offset:22528\n"
    "s_waitcnt lgkmcnt(3)\n"
    "v_mfma_f32_32x32x16_bf16 v[64:79], a[36:39], v[96:99], v[64:79]\n"
    "ds_read_b128 v[116:119], v252 offset:23552\n"
    "v_mfma_f32_32x32x16_bf16 v[80:95], a[36:39], v[100:103], v[80:95]\n"
    "ds_read_b128 v[96:99], v252 offset:24576\n"
    "s_waitcnt lgkmcnt(3)\n"
    "v_mfma_f32_32x32x16_bf16 v[64:79], a[40:43], v[104:107], v[64:79]\n"
    "ds_read_b128 v[100:103], v252 offset:25600\n"
    "v_mfma_f32_32x32x16_bf16 v[80:95], a[40:43], v[108:111], v[80:95]\n"
    "ds_read_b128 v[104:107], v252 offset:26624\n"
    "s_waitcnt lgkmcnt(3)\n"
    "v_mfma_f32_32x32x16_bf16 v[64:79], a[44:47], v[112:115], v[64:79]\n"
    "ds_read_b128 v[108:111], v252 offset:27648\n"
    "v_mfma_f32_32x32x16_bf16 v[80:95], a[44:47], v[116:119], v[80:95]\n"
    "ds_read_b128 v[112:115], v252 offset:28672\n"
    "s_waitcnt lgkmcnt(3)\n"
    "v_mfma_f32_32x32x16_bf16 v[64:79], a[48:51], v[96:99], v[64:79]\n"
    "ds_read_b128 v[116:119], v252 offset:29696\n"
    "v_mfma_f32_32x32x16_bf16 v[80:95], a[48:51], v[100:103], v[80:95]\n"
    "ds_read_b128 v[96:99], v252 offset:30720\n"
    "s_waitcnt lgkmcnt(3)\n"
    "v_mfma_f32_32x32x16_bf16 v[64:79], a[52:55], v[104:107], v[64:79]\n"
    "ds_read_b128 v[100:103], v252 offset:31744\n"
    "v_mfma_f32_32x32x16_bf16 v[80:95], a[52:55], v[108:111], v[80:95]\n"
    "s_waitcnt vmcnt(4)\n"
    "s_barrier\n"
    "ds_read_b128 v[104:107], v252 offset:32768\n"
    "s_waitcnt lgkmcnt(3)\n"
    "v_mfma_f32_32x32x16_bf16 v[64:79], a[56:59], v[112:115], v[64:79]\n"
    "ds_read_b128 v[108:111], v252 offset:33792\n"
    "v_mfma_f32_32x32x16_bf16 v[80:95], a[56:59], v[116:119], v[80:95]\n"
    "ds_read_b128 v[112:115], v252 offset:34816\n"
    "s_waitcnt lgkmcnt(3)\n"
    "v_mfma_f32_32x32x16_bf16 v[64:79], a[60:63], v[96:99], v[64:79]\n"
    "ds_read_b128 v[116:119], v252 offset:35840\n"
    "v_mfma_f32_32x32x16_bf16 v[80:95], a[60:63], v[100:103], v[80:95]\n"
    "ds_read_b128 v[96:99], v252 offset:36864\n"
    "s_waitcnt lgkmcnt(3)\n"
    "v_mfma_f32_32x32x16_bf16 v[64:79], a[64:67], v[104:107], v[64:79]\n"
    "ds_read_b128 v[100:103], v252 offset:37888\n"
    "v_mfma_f32_32x32x16_bf16 v[80:95], a[64:67], v[108:111], v[80:95]\n"
    "ds_read_b128 v[104:107], v252 offset:38912\n"
    "s_waitcnt lgkmcnt(3)\n"
    "v_mfma_f32_32x32x16_bf16 v[64:79], a[68:71], v[112:115], v[64:79]\n"
    "ds_read_b128 v[108:111], v252 offset:39936\n"
    "v_mfma_f32_32x32x16_bf16 v[80:95], a[68:71], v[116:119], v[80:95]\n"
    "ds_read_b128 v[112:115], v252 offset:40960\n"
    "s_waitcnt lgkmcnt(3)\n"
    "v_mfma_f32_32x32x16_bf16 v[64:79], a[72:75], v[96:99], v[64:79]\n"
    "ds_read_b128 v[116:119], v252 offset:41984\n"
    "v_mfma_f32_32x32x16_bf16 v[80:95], a[72:75], v[100:103], v[80:95]\n"
    "ds_read_b128 v[96:99], v252 offset:43008\n"
    "s_waitcnt lgkmcnt(3)\n"
    "v_mfma_f32_32x32x16_bf16 v[64:79], a[76:79], v[104:107], v[64:79]\n"
    "ds_read_b128 v[100:103], v252 offset:44032\n"
    "v_mfma_f32_32x32x16_bf16 v[80:95], a[76:79], v[108:111], v[80:95]\n"
    "ds_read_b128 v[104:107], v252 offset:45056\n"
    "s_waitcnt lgkmcnt(3)\n"
    "v_mfma_f32_32x32x16_bf16 v[64:79], a[80:83], v[112:115], v[64:79]\n"
    "ds_read_b128 v[108:111], v252 offset:46080\n"
    "v_mfma_f32_32x32x16_bf16 v[80:95], a[80:83], v[116:119], v[80:95]\n"
    "ds_read_b128 v[112:115], v252 offset:47104\n"
    "s_waitcnt lgkmcnt(3)\n"
    "v_mfma_f32_32x32x16_bf16 v[64:79], a[84:87], v[96:99], v[64:79]\n"
    "ds_read_b128 v[116:119], v252 offset:48128\n"
    "v_mfma_f32_32x32x16_bf16 v[80:95], a[84:87], v[100:103], v[80:95]\n"
    "s_waitcnt vmcnt(0)\n"
    "s_barrier\n"
    "ds_read_b128 v[96:99], v252 offset:49152\n"
    "s_waitcnt lgkmcnt(3)\n"
    "v_mfma_f32_32x32x16_bf16 v[64:79], a[88:91], v[104:107], v[64:79]\n"
    "ds_read_b128 v[100:103], v252 offset:50176\n"
    "v_mfma_f32_32x32x16_bf16 v[80:95], a[88:91], v[108:111], v[80:95]\n"
    "ds_read_b128 v[104:107], v252 offset:51200\n"
    "s_waitcnt lgkmcnt(3)\n"
    "v_mfma_f32_32x32x16_bf16 v[64:79], a[92:95], v[112:115], v[64:79]\n"
    "ds_read_b128 v[108:111], v252 offset:52224\n"
    "v_mfma_f32_32x32x16_bf16 v[80:95], a[92:95], v[116:119], v[80:95]\n"
    "ds_read_b128 v[112:115], v252 offset:53248\n"
    "s_waitcnt lgkmcnt(3)\n"
    "v_mfma_f32_32x32x16_bf16 v[64:79], a[96:99], v[96:99], v[64:79]\n"
    "ds_read_b128 v[116:119], v252 offset:54272\n"
    "v_mfma_f32_32x32x16_bf16 v[80:95], a[96:99], v[100:103], v[80:95]\n"
    "ds_read_b128 v[96:99], v252 offset:55296\n"
    "s_waitcnt lgkmcnt(3)\n"
    "v_mfma_f32_32x32x16_bf16 v[64:79], a[100:103], v[104:107], v[64:79]\n"
    "ds_read_b128 v[100:103], v252 offset:56320\n"
    "v_mfma_f32_32x32x16_bf16 v[80:95], a[100:103], v[108:111], v[80:95]\n"
    "ds_read_b128 v[104:107], v252 offset:57344\n"
    "s_waitcnt lgkmcnt(3)\n"
    "v_mfma_f32_32x32x16_bf16 v[64:79], a[104:107], v[112:115], v[64:79]\n"
    "ds_read_b128 v[108:111], v252 offset:58368\n"
    "v_mfma_f32_32x32x16_bf16 v[80:95], a[104:107], v[116:119], v[80:95]\n"
    "ds_read_b128 v[112:115], v252 offset:59392\n"
    "s_waitcnt lgkmcnt(3)\n"
    "v_mfma_f32_32x32x16_bf16 v[64:79], a[108:111], v[96:99], v[64:79]\n"
    "ds_read_b128 v[116:119], v252 offset:60416\n"
    "v_mfma_f32_32x32x16_bf16 v[80:95], a[108:111], v[100:103], v[80:95]\n"
    "ds_read_b128 v[96:99], v252 offset:61440\n"
    "s_waitcnt lgkmcnt(3)\n"
    "v_mfma_f32_32x32x16_bf16 v[64:79], a[112:115], v[104:107], v[64:79]\n"
    "ds_read_b128 v[100:103], v252 offset:62464\n"
    "v_mfma_f32_32x32x16_bf16 v[80:95], a[112:115], v[108:111], v[80:95]\n"
    "ds_read_b128 v[104:107], v252 offset:63488\n"
    "s_waitcnt lgkmcnt(3)\n"
    "v_mfma_f32_32x32x16_bf16 v[64:79], a[116:119], v[112:115], v[64:79]\n"
    "ds_read_b128 v[108:111], v252 offset:64512\n"
    "v_mfma_f32_32x32x16_bf16 v[80:95], a[116:119], v[116:119], v[80:95]\n"
    "s_waitcnt lgkmcnt(2)\n"
    "v_mfma_f32_32x32x16_bf16 v[64:79], a[120:123], v[96:99], v[64:79]\n"
    "v_mfma_f32_32x32x16_bf16 v[80:95], a[120:123], v[100:103], v[80:95]\n"
    "s_waitcnt lgkmcnt(0)\n"
    "v_mfma_f32_32x32x16_bf16 v[64:79], a[124:127], v[104:107], v[64:79]\n"
    "v_mfma_f32_32x32x16_bf16 v[80:95], a[124:127], v[108:111], v[80:95]\n"
    "s_nop 10\n"
    "v_cvt_pk_bf16_f32 v64, v64, v65\n"
    "v_cvt_pk_bf16_f32 v65, v66, v67\n"
    "v_cvt_pk_bf16_f32 v66, v68, v69\n"
    "v_cvt_pk_bf16_f32 v67, v70, v71\n"
    "v_cvt_pk_bf16_f32 v68, v72, v73\n"
    "v_cvt_pk_bf16_f32 v69, v74, v75\n"
    "v_cvt_pk_bf16_f32 v70, v76, v77\n"
    "v_cvt_pk_bf16_f32 v71, v78, v79\n"
    "buffer_store_dwordx4 v[64:67], v22, s[56:59], s97 offen offset:0 sc1\n"
    "buffer_store_dwordx4 v[68:71], v22, s[56:59], s97 offen offset:1024 sc1\n"
    "s_add_u32 s97, s97, 0x8000\n"
    "v_cvt_pk_bf16_f32 v80, v80, v81\n"
    "v_cvt_pk_bf16_f32 v81, v82, v83\n"
    "v_cvt_pk_bf16_f32 v82, v84, v85\n"
    "v_cvt_pk_bf16_f32 v83, v86, v87\n"
    "v_cvt_pk_bf16_f32 v84, v88, v89\n"
    "v_cvt_pk_bf16_f32 v85, v90, v91\n"
    "v_cvt_pk_bf16_f32 v86, v92, v93\n"
    "v_cvt_pk_bf16_f32 v87, v94, v95\n"
    "buffer_store_dwordx4 v[80:83], v22, s[56:59], s97 offen offset:0 sc1\n"
    "buffer_store_dwordx4 v[84:87], v22, s[56:59], s97 offen offset:1024 sc1\n"
    "s_add_u32 s97, s97, 0x8000\n"
    "v_lshrrev_b32 v5, 5, v4\n"
    "v_bfe_u32 v31, v4, 2, 2\n"
    "s_add_u32 m0, s87, 0\n"
    "v_lshl_add_u32 v5, v5, 3, v31\n"
    "v_lshlrev_b32 v5, 7, v5\n"
    "v_bfe_u32 v31, v4, 4, 1\n"
    "s_bfe_u32 s77, s10, 0x40000\n"
    "v_lshl_add_u32 v5, v31, 5, v5\n"
    "v_and_b32 v31, 3, v4\n"
    "v_lshl_add_u32 v5, v31, 3, v5\n"
    "s_add_u32 s77, s77, s62\n"
    "v_bfe_u32 v31, v4, 3, 1\n"
    "v_xor_b32 v31, s82, v31\n"
    "v_lshl_add_u32 v5, v31, 6, v5\n"
    "s_sub_u32 s76, s77, 11\n"
    "s_lshl_b32 s79, s82, 12\n"
    "v_add_u32 v5, s79, v5\n"
    "v_xor_b32 v6, 64, v5\n"
    "s_min_u32 s77, s77, s76\n"
    "v_add_u32 v7, 49152, v5\n"
    "v_add_u32 v8, 49152, v6\n"
    "v_and_b32 v80, 31, v4\n"
    "s_lshl_b32 s76, s77, 16\n"
    "v_and_b32 v81, 7, v4\n"
    "v_lshrrev_b32 v31, 5, v4\n"
    "v_xor_b32 v81, v81, v31\n"
    "buffer_load_dwordx4 v15, s[44:47], s76 offen offset:0 lds\n"
    "v_lshlrev_b32 v81, 4, v81\n"
    "v_lshl_add_u32 v80, v80, 8, v81\n"
    "s_lshl1_add_u32 s78, s82, s65\n"
    "buffer_load_dwordx4 v16, s[44:47], s76 offen offset:1024 lds\n"
    "s_lshl_b32 s78, s78, 6\n"
    "s_add_u32 s78, s78, s82\n"
    "s_lshl_b32 s78, s78, 7\n"
    "s_add_u32 m0, s87, 8192\n"
    "s_add_u32 s78, s78, 98304\n"
    "v_add_u32 v80, s78, v80\n"
    "v_xor_b32 v81, 32, v80\n"
    "s_bfe_u32 s77, s10, 0x40004\n"
    "v_xor_b32 v82, 64, v80\n"
    "v_xor_b32 v83, 96, v80\n"
    "v_xor_b32 v84, 128, v80\n"
    "s_add_u32 s77, s77, s62\n"
    "v_xor_b32 v85, 160, v80\n"
    "v_xor_b32 v86, 192, v80\n"
    "v_xor_b32 v87, 224, v80\n"
    "s_sub_u32 s76, s77, 11\n"
    "v_lshrrev_b32 v31, 4, v4\n"
    "v_and_b32 v88, 15, v4\n"
    "v_xor_b32 v88, v88, v31\n"
    "s_min_u32 s77, s77, s76\n"
    "v_lshlrev_b32 v88, 4, v88\n"
    "v_lshl_add_u32 v88, v31, 8, v88\n"
    "s_lshl_b32 s78, s60, 12\n"
    "s_lshl_b32 s76, s77, 16\n"
    "s_add_u32 s78, s78, 98304\n"
    "v_add_u32 v88, s78, v88\n"
    "v_xor_b32 v89, 64, v88\n"
    "buffer_load_dwordx4 v15, s[44:47], s76 offen offset:0 lds\n"
    "s_lshl_b32 s78, s60, 8\n"
    "s_add_u32 s93, s78, 90112\n"
    "v_lshl_add_u32 v90, v253, 2, s93\n"
    "buffer_load_dwordx4 v16, s[44:47], s76 offen offset:1024 lds\n"
    "s_lshl4_add_u32 s78, s82, s93\n"
    "v_lshl_add_u32 v91, v31, 2, s78\n"
    "v_lshlrev_b32 v31, 2, v4\n"
    "s_add_u32 m0, s87, 16384\n"
    "s_lshl_b32 s78, s66, 7\n"
    "s_add_u32 s83, s78, 0x30000\n"
    "s_lshl1_add_u32 s78, s62, s82\n"
    "s_bfe_u32 s77, s10, 0x40008\n"
    "s_lshl4_add_u32 s78, s66, s78\n"
    "s_lshl_b32 s78, s78, 11\n"
    "s_add_u32 s97, s78, 0xe80000\n"
    "s_add_u32 s77, s77, s62\n"
    "s_mov_b32 s79, 0xffff0000\n"
    "v_lshlrev_b32 v204, 16, v184\n"
    "v_and_b32 v205, s79, v184\n"
    "s_sub_u32 s76, s77, 11\n"
    "v_lshlrev_b32 v206, 16, v185\n"
    "v_and_b32 v207, s79, v185\n"
    "s_min_u32 s77, s77, s76\n"
    "s_lshl_b32 s76, s77, 16\n"
    "buffer_load_dwordx4 v15, s[44:47], s76 offen offset:0 lds\n"
    "buffer_load_dwordx4 v16, s[44:47], s76 offen offset:1024 lds\n"
    "s_add_u32 m0, s87, 24576\n"
    "s_bfe_u32 s77, s10, 0x4000c\n"
    "s_add_u32 s77, s77, s62\n"
    "s_sub_u32 s76, s77, 11\n"
    "s_min_u32 s77, s77, s76\n"
    "s_lshl_b32 s76, s77, 16\n"
    "buffer_load_dwordx4 v15, s[44:47], s76 offen offset:0 lds\n"
    "buffer_load_dwordx4 v16, s[44:47], s76 offen offset:1024 lds\n"
    "s_add_u32 m0, s87, 32768\n"
    "s_bfe_u32 s77, s10, 0x40010\n"
    "s_add_u32 s77, s77, s62\n"
    "s_sub_u32 s76, s77, 11\n"
    "s_min_u32 s77, s77, s76\n"
    "s_lshl_b32 s76, s77, 16\n"
    "buffer_load_dwordx4 v15, s[44:47], s76 offen offset:0 lds\n"
    "buffer_load_dwordx4 v16, s[44:47], s76 offen offset:1024 lds\n"
    "s_add_u32 m0, s87, 40960\n"
    "s_bfe_u32 s77, s10, 0x40014\n"
    "s_add_u32 s77, s77, s62\n"
    "s_sub_u32 s76, s77, 11\n"
    "s_min_u32 s77, s77, s76\n"
    "s_lshl_b32 s76, s77, 16\n"
    "s_waitcnt vmcnt(10)\n"
    "s_barrier\n"
    "s_add_u32 s80, s14, 0x11000\n"
    "buffer_store_dwordx2 v[24:25], v23, s[56:59], s80 offen sc1\n"
    "v_lshlrev_b32 v188, 16, v176\n"
    "v_and_b32 v189, s79, v176\n"
    "v_lshlrev_b32 v190, 16, v177\n"
    "v_and_b32 v191, s79, v177\n"
    "v_mul_f32 v188, v188, v204\n"
    "v_mul_f32 v189, v189, v205\n"
    "v_mul_f32 v190, v190, v206\n"
    "v_mul_f32 v191, v191, v207\n"
    "v_lshlrev_b32 v192, 16, v178\n"
    "v_and_b32 v193, s79, v178\n"
    "v_lshlrev_b32 v194, 16, v179\n"
    "v_and_b32 v195, s79, v179\n"
    "v_mul_f32 v192, v192, v204\n"
    "v_mul_f32 v193, v193, v205\n"
    "v_mul_f32 v194, v194, v206\n"
    "v_mul_f32 v195, v195, v207\n"
    "v_lshlrev_b32 v196, 16, v180\n"
    "v_and_b32 v197, s79, v180\n"
    "v_lshlrev_b32 v198, 16, v181\n"
    "v_and_b32 v199, s79, v181\n"
    "v_mul_f32 v196, v196, v204\n"
    "v_mul_f32 v197, v197, v205\n"
    "v_mul_f32 v198, v198, v206\n"
    "v_mul_f32 v199, v199, v207\n"
    "v_lshlrev_b32 v200, 16, v182\n"
    "v_and_b32 v201, s79, v182\n"
    "v_lshlrev_b32 v202, 16, v183\n"
    "v_and_b32 v203, s79, v183\n"
    "v_mul_f32 v200, v200, v204\n"
    "v_mul_f32 v201, v201, v205\n"
    "v_mul_f32 v202, v202, v206\n"
    "v_mul_f32 v203, v203, v207\n"
    "buffer_load_dwordx4 v15, s[44:47], s76 offen offset:0 lds\n"
    "buffer_load_dwordx4 v16, s[44:47], s76 offen offset:1024 lds\n"
    "s_add_u32 m0, s87, 49152\n"
    "s_bfe_u32 s77, s10, 0x40018\n"
    "s_add_u32 s77, s77, s62\n"
    "s_sub_u32 s76, s77, 11\n"
    "s_min_u32 s77, s77, s76\n"
    "s_lshl_b32 s76, s77, 16\n"
    "buffer_load_dwordx4 v15, s[44:47], s76 offen offset:0 lds\n"
    "buffer_load_dwordx4 v16, s[44:47], s76 offen offset:1024 lds\n"
    "s_add_u32 m0, s87, 57344\n"
    "s_bfe_u32 s77, s10, 0x4001c\n"
    "s_add_u32 s77, s77, s62\n"
    "s_sub_u32 s76, s77, 11\n"
    "s_min_u32 s77, s77, s76\n"
    "s_lshl_b32 s76, s77, 16\n"
    "buffer_load_dwordx4 v15, s[44:47], s76 offen offset:0 lds\n"
    "buffer_load_dwordx4 v16, s[44:47], s76 offen offset:1024 lds\n"
    "s_add_u32 m0, s87, 65536\n"
    "s_bfe_u32 s77, s11, 0x40000\n"
    "s_add_u32 s77, s77, s62\n"
    "s_sub_u32 s76, s77, 11\n"
    "s_min_u32 s77, s77, s76\n"
    "s_lshl_b32 s76, s77, 16\n"
    "buffer_load_dwordx4 v15, s[44:47], s76 offen offset:0 lds\n"
    "buffer_load_dwordx4 v16, s[44:47], s76 offen offset:1024 lds\n"
    "s_add_u32 m0, s87, 73728\n"
    "s_bfe_u32 s77, s11, 0x40004\n"
    "s_add_u32 s77, s77, s62\n"
    "s_sub_u32 s76, s77, 11\n"
    "s_min_u32 s77, s77, s76\n"
    "s_lshl_b32 s76, s77, 16\n"
    "buffer_load_dwordx4 v15, s[44:47], s76 offen offset:0 lds\n"
    "buffer_load_dwordx4 v16, s[44:47], s76 offen offset:1024 lds\n"
    "s_add_u32 m0, s87, 81920\n"
    "s_bfe_u32 s77, s11, 0x40008\n"
    "s_add_u32 s77, s77, s62\n"
    "s_sub_u32 s76, s77, 11\n"
    "s_min_u32 s77, s77, s76\n"
    "s_lshl_b32 s76, s77, 16\n"
    "buffer_load_dwordx4 v15, s[44:47], s76 offen offset:0 lds\n"
    "buffer_load_dwordx4 v16, s[44:47], s76 offen offset:1024 lds\n"
    "buffer_load_dwordx2 v[2:3], v13, s[56:59], 0 offen sc1\n"
    "s_waitcnt vmcnt(1)\n"
    "s_barrier\n"
    "s_waitcnt vmcnt(0)\n"
    "v_xor_b32 v0, s64, v2\n"
    "v_bitop3_b32 v0, v0, v3, s64 bitop3:0xf6\n"
    "v_cmp_eq_u32 vcc, 0, v0\n"
    "s_nop 0\n"
    "s_cmp_eq_u64 vcc, -1\n"
    "s_cbranch_scc1 L1ok8_%=\n"
    "s_mov_b32 s80, 0x4000\n"
    "buffer_load_dwordx2 v[26:27], v13, s[56:59], 0 offen sc1\n"
    "L1spin9_%=:\n"
    "s_sleep 4\n"
    "buffer_load_dwordx2 v[2:3], v13, s[56:59], 0 offen sc1\n"
    "s_waitcnt vmcnt(1)\n"
    "v_xor_b32 v0, s64, v26\n"
    "v_bitop3_b32 v0, v0, v27, s64 bitop3:0xf6\n"
    "v_cmp_eq_u32 vcc, 0, v0\n"
    "s_nop 0\n"
    "s_cmp_eq_u64 vcc, -1\n"
    "s_cbranch_scc1 L1ok10_%=\n"
    "s_sleep 4\n"
    "buffer_load_dwordx2 v[26:27], v13, s[56:59], 0 offen sc1\n"
    "s_waitcnt vmcnt(1)\n"
    "v_xor_b32 v0, s64, v2\n"
    "v_bitop3_b32 v0, v0, v3, s64 bitop3:0xf6\n"
    "v_cmp_eq_u32 vcc, 0, v0\n"
    "s_nop 0\n"
    "s_cmp_eq_u64 vcc, -1\n"
    "s_cbranch_scc1 L1ok10_%=\n"
    "s_sub_u32 s80, s80, 1\n"
    "s_cmp_lg_u32 s80, 0\n"
    "s_cbranch_scc1 L1spin9_%=\n"
    "L1ok10_%=:\n"
    "L1ok8_%=:\n"
    "s_bfe_u32 s77, s10, 0x40000\n"
    "s_add_u32 s77, s77, s62\n"
    "s_sub_u32 s76, s77, 11\n"
    "s_min_u32 s77, s77, s76\n"
    "s_lshl_b32 s76, s77, 12\n"
    "buffer_load_dwordx4 a[0:3], v28, s[56:59], s76 offen offset:0 sc1\n"
    "buffer_load_dwordx4 a[4:7], v28, s[56:59], s76 offen offset:1024 sc1\n"
    "s_bfe_u32 s77, s10, 0x40004\n"
    "s_add_u32 s77, s77, s62\n"
    "s_sub_u32 s76, s77, 11\n"
    "s_min_u32 s77, s77, s76\n"
    "s_lshl_b32 s76, s77, 12\n"
    "buffer_load_dwordx4 a[8:11], v28, s[56:59], s76 offen offset:0 sc1\n"
    "buffer_load_dwordx4 a[12:15], v28, s[56:59], s76 offen offset:1024 sc1\n"
    "s_bfe_u32 s77, s10, 0x40008\n"
    "s_add_u32 s77, s77, s62\n"
    "s_sub_u32 s76, s77, 11\n"
    "s_min_u32 s77, s77, s76\n"
    "s_lshl_b32 s76, s77, 12\n"
    "buffer_load_dwordx4 a[16:19], v28, s[56:59], s76 offen offset:0 sc1\n"
    "buffer_load_dwordx4 a[20:23], v28, s[56:59], s76 offen offset:1024 sc1\n"
    "s_bfe_u32 s77, s10, 0x4000c\n"
    "s_add_u32 s77, s77, s62\n"
    "s_sub_u32 s76, s77, 11\n"
    "s_min_u32 s77, s77, s76\n"
    "s_lshl_b32 s76, s77, 12\n"
    "buffer_load_dwordx4 a[24:27], v28, s[56:59], s76 offen offset:0 sc1\n"
    "buffer_load_dwordx4 a[28:31], v28, s[56:59], s76 offen offset:1024 sc1\n"
    "s_bfe_u32 s77, s10, 0x40010\n"
    "s_add_u32 s77, s77, s62\n"
    "s_sub_u32 s76, s77, 11\n"
    "s_min_u32 s77, s77, s76\n"
    "s_lshl_b32 s76, s77, 12\n"
    "buffer_load_dwordx4 a[32:35], v28, s[56:59], s76 offen offset:0 sc1\n"
    "buffer_load_dwordx4 a[36:39], v28, s[56:59], s76 offen offset:1024 sc1\n"
    "ds_read_b64_tr_b16 v[32:33], v5 offset:0\n"
    "ds_read_b64_tr_b16 v[34:35], v5 offset:512\n"
    "ds_read_b64_tr_b16 v[36:37], v6 offset:0\n"
    "ds_read_b64_tr_b16 v[38:39], v6 offset:512\n"
    "ds_read_b64_tr_b16 v[40:41], v5 offset:2048\n"
    "ds_read_b64_tr_b16 v[42:43], v5 offset:2560\n"
    "ds_read_b64_tr_b16 v[44:45], v6 offset:2048\n"
    "ds_read_b64_tr_b16 v[46:47], v6 offset:2560\n"
    "ds_read_b64_tr_b16 v[48:49], v5 offset:8192\n"
    "ds_read_b64_tr_b16 v[50:51], v5 offset:8704\n"
    "s_waitcnt lgkmcnt(6)\n"
    "s_bfe_u32 s77, s10, 0x40014\n"
    "s_add_u32 s77, s77, s62\n"
    "s_sub_u32 s76, s77, 11\n"
    "s_min_u32 s77, s77, s76\n"
    "s_lshl_b32 s76, s77, 12\n"
    "buffer_load_dwordx4 a[40:43], v28, s[56:59], s76 offen offset:0 sc1\n"
    "s_waitcnt vmcnt(10)\n"
    "v_mfma_f32_32x32x16_bf16 v[96:111], v[32:35], a[0:3], 0\n"
    "ds_read_b64_tr_b16 v[52:53], v6 offset:8192\n"
    "ds_read_b64_tr_b16 v[54:55], v6 offset:8704\n"
    "v_mfma_f32_32x32x16_bf16 v[112:127], v[36:39], a[0:3], 0\n"
    "ds_read_b64_tr_b16 v[32:33], v5 offset:10240\n"
    "ds_read_b64_tr_b16 v[34:35], v5 offset:10752\n"
    "s_waitcnt lgkmcnt(6)\n"
    "s_waitcnt vmcnt(9)\n"
    "v_mfma_f32_32x32x16_bf16 v[96:111], v[40:43], a[4:7], v[96:111]\n"
    "ds_read_b64_tr_b16 v[36:37], v6 offset:10240\n"
    "ds_read_b64_tr_b16 v[38:39], v6 offset:10752\n"
    "v_mfma_f32_32x32x16_bf16 v[112:127], v[44:47], a[4:7], v[112:127]\n"
    "ds_read_b64_tr_b16 v[40:41], v5 offset:16384\n"
    "ds_read_b64_tr_b16 v[42:43], v5 offset:16896\n"
    "buffer_load_dwordx4 a[44:47], v28, s[56:59], s76 offen offset:1024 sc1\n"
    "buffer_load_dwordx2 v[2:3], v14, s[56:59], 0 offen sc1\n"
    "s_waitcnt lgkmcnt(6)\n"
    "s_waitcnt vmcnt(10)\n"
    "v_mfma_f32_32x32x16_bf16 v[96:111], v[48:51], a[8:11], v[96:111]\n"
    "ds_read_b64_tr_b16 v[44:45], v6 offset:16384\n"
    "ds_read_b64_tr_b16 v[46:47], v6 offset:16896\n"
    "v_mfma_f32_32x32x16_bf16 v[112:127], v[52:55], a[8:11], v[112:127]\n"
    "ds_read_b64_tr_b16 v[48:49], v5 offset:18432\n"
    "ds_read_b64_tr_b16 v[50:51], v5 offset:18944\n"
    "s_waitcnt lgkmcnt(6)\n"
    "s_waitcnt vmcnt(9)\n"
    "v_mfma_f32_32x32x16_bf16 v[96:111], v[32:35], a[12:15], v[96:111]\n"
    "ds_read_b64_tr_b16 v[52:53], v6 offset:18432\n"
    "ds_read_b64_tr_b16 v[54:55], v6 offset:18944\n"
    "s_bfe_u32 s77, s10, 0x40018\n"
    "s_add_u32 s77, s77, s62\n"
    "s_sub_u32 s76, s77, 11\n"
    "v_mfma_f32_32x32x16_bf16 v[112:127], v[36:39], a[12:15], v[112:127]\n"
    "ds_read_b64_tr_b16 v[32:33], v5 offset:24576\n"
    "ds_read_b64_tr_b16 v[34:35], v5 offset:25088\n"
    "s_min_u32 s77, s77, s76\n"
    "s_lshl_b32 s76, s77, 12\n"
    "buffer_load_dwordx4 a[48:51], v28, s[56:59], s76 offen offset:0 sc1\n"
    "buffer_load_dwordx4 a[52:55], v28, s[56:59], s76 offen offset:1024 sc1\n"
    "s_waitcnt lgkmcnt(6)\n"
    "s_waitcnt vmcnt(10)\n"
    "v_mfma_f32_32x32x16_bf16 v[96:111], v[40:43], a[16:19], v[96:111]\n"
    "ds_read_b64_tr_b16 v[36:37], v6 offset:24576\n"
    "ds_read_b64_tr_b16 v[38:39], v6 offset:25088\n"
    "v_mfma_f32_32x32x16_bf16 v[112:127], v[44:47], a[16:19], v[112:127]\n"
    "ds_read_b64_tr_b16 v[40:41], v5 offset:26624\n"
    "ds_read_b64_tr_b16 v[42:43], v5 offset:27136\n"
    "s_waitcnt lgkmcnt(6)\n"
    "s_waitcnt vmcnt(9)\n"
    "v_mfma_f32_32x32x16_bf16 v[96:111], v[48:51], a[20:23], v[96:111]\n"
    "ds_read_b64_tr_b16 v[44:45], v6 offset:26624\n"
    "ds_read_b64_tr_b16 v[46:47], v6 offset:27136\n"
    "s_bfe_u32 s77, s10, 0x4001c\n"
    "s_add_u32 s77, s77, s62\n"
    "s_sub_u32 s76, s77, 11\n"
    "v_mfma_f32_32x32x16_bf16 v[112:127], v[52:55], a[20:23], v[112:127]\n"
    "ds_read_b64_tr_b16 v[48:49], v5 offset:32768\n"
    "ds_read_b64_tr_b16 v[50:51], v5 offset:33280\n"
    "s_min_u32 s77, s77, s76\n"
    "s_lshl_b32 s76, s77, 12\n"
    "buffer_load_dwordx4 a[56:59], v28, s[56:59], s76 offen offset:0 sc1\n"
    "buffer_load_dwordx4 a[60:63], v28, s[56:59], s76 offen offset:1024 sc1\n"
    "s_waitcnt lgkmcnt(6)\n"
    "s_waitcnt vmcnt(10)\n"
    "v_mfma_f32_32x32x16_bf16 v[96:111], v[32:35], a[24:27], v[96:111]\n"
    "ds_read_b64_tr_b16 v[52:53], v6 offset:32768\n"
    "ds_read_b64_tr_b16 v[54:55], v6 offset:33280\n"
    "v_mfma_f32_32x32x16_bf16 v[112:127], v[36:39], a[24:27], v[112:127]\n"
    "ds_read_b64_tr_b16 v[32:33], v5 offset:34816\n"
    "ds_read_b64_tr_b16 v[34:35], v5 offset:35328\n"
    "s_waitcnt lgkmcnt(6)\n"
    "s_waitcnt vmcnt(9)\n"
    "v_mfma_f32_32x32x16_bf16 v[96:111], v[40:43], a[28:31], v[96:111]\n"
    "ds_read_b64_tr_b16 v[36:37], v6 offset:34816\n"
    "ds_read_b64_tr_b16 v[38:39], v6 offset:35328\n"
    "s_bfe_u32 s77, s11, 0x40000\n"
    "s_add_u32 s77, s77, s62\n"
    "s_sub_u32 s76, s77, 11\n"
    "v_mfma_f32_32x32x16_bf16 v[112:127], v[44:47], a[28:31], v[112:127]\n"
    "ds_read_b64_tr_b16 v[40:41], v5 offset:40960\n"
    "ds_read_b64_tr_b16 v[42:43], v5 offset:41472\n"
    "s_min_u32 s77, s77, s76\n"
    "s_lshl_b32 s76, s77, 12\n"
    "buffer_load_dwordx4 a[64:67], v28, s[56:59], s76 offen offset:0 sc1\n"
    "buffer_load_dwordx4 a[68:71], v28, s[56:59], s76 offen offset:1024 sc1\n"
    "s_bfe_u32 s77, s11, 0x40004\n"
    "s_add_u32 s77, s77, s62\n"
    "s_sub_u32 s76, s77, 11\n"
    "s_min_u32 s77, s77, s76\n"
    "s_lshl_b32 s76, s77, 12\n"
    "buffer_load_dwordx4 a[72:75], v28, s[56:59], s76 offen offset:0 sc1\n"
    "buffer_load_dwordx4 a[76:79], v28, s[56:59], s76 offen offset:1024 sc1\n"
    "s_bfe_u32 s77, s11, 0x40008\n"
    "s_add_u32 s77, s77, s62\n"
    "s_sub_u32 s76, s77, 11\n"
    "s_min_u32 s77, s77, s76\n"
    "s_lshl_b32 s76, s77, 12\n"
    "buffer_load_dwordx4 a[80:83], v28, s[56:59], s76 offen offset:0 sc1\n"
    "buffer_load_dwordx4 a[84:87], v28, s[56:59], s76 offen offset:1024 sc1\n"
    "s_waitcnt vmcnt(10)\n"
    "v_xor_b32 v0, s64, v2\n"
    "v_bitop3_b32 v0, v0, v3, s64 bitop3:0xf6\n"
    "v_cmp_eq_u32 vcc, 0, v0\n"
    "s_nop 0\n"
    "s_cmp_eq_u64 vcc, -1\n"
    "s_cbranch_scc1 L1ok11_%=\n"
    "s_mov_b32 s80, 0x4000\n"
    "buffer_load_dwordx2 v[26:27], v14, s[56:59], 0 offen sc1\n"
    "L1spin12_%=:\n"
    "s_sleep 4\n"
    "buffer_load_dwordx2 v[2:3], v14, s[56:59], 0 offen sc1\n"
    "s_waitcnt vmcnt(1)\n"
    "v_xor_b32 v0, s64, v26\n"
    "v_bitop3_b32 v0, v0, v27, s64 bitop3:0xf6\n"
    "v_cmp_eq_u32 vcc, 0, v0\n"
    "s_nop 0\n"
    "s_cmp_eq_u64 vcc, -1\n"
    "s_cbranch_scc1 L1ok13_%=\n"
    "s_sleep 4\n"
    "buffer_load_dwordx2 v[26:27], v14, s[56:59], 0 offen sc1\n"
    "s_waitcnt vmcnt(1)\n"
    "v_xor_b32 v0, s64, v2\n"
    "v_bitop3_b32 v0, v0, v3, s64 bitop3:0xf6\n"
    "v_cmp_eq_u32 vcc, 0, v0\n"
    "s_nop 0\n"
    "s_cmp_eq_u64 vcc, -1\n"
    "s_cbranch_scc1 L1ok13_%=\n"
    "s_sub_u32 s80, s80, 1\n"
    "s_cmp_lg_u32 s80, 0\n"
    "s_cbranch_scc1 L1spin12_%=\n"
    "L1ok13_%=:\n"
    "L1ok11_%=:\n"
    "s_mov_b32 m0, s93\n"
    "buffer_load_dwordx4 v[56:59], v22, s[56:59], s97 offen sc1\n"
    "buffer_load_dwordx4 v[60:63], v22, s[56:59], s97 offen offset:1024 sc1\n"
    "buffer_load_dword v31, s[56:59], s83 offen sc1 lds\n"
    "s_waitcnt lgkmcnt(6)\n"
    "v_mfma_f32_32x32x16_bf16 v[96:111], v[48:51], a[32:35], v[96:111]\n"
    "ds_read_b64_tr_b16 v[44:45], v6 offset:40960\n"
    "ds_read_b64_tr_b16 v[46:47], v6 offset:41472\n"
    "v_mfma_f32_32x32x16_bf16 v[112:127], v[52:55], a[32:35], v[112:127]\n"
    "ds_read_b64_tr_b16 v[48:49], v5 offset:43008\n"
    "ds_read_b64_tr_b16 v[50:51], v5 offset:43520\n"
    "s_waitcnt lgkmcnt(6)\n"
    "v_mfma_f32_32x32x16_bf16 v[96:111], v[32:35], a[36:39], v[96:111]\n"
    "ds_read_b64_tr_b16 v[52:53], v6 offset:43008\n"
    "ds_read_b64_tr_b16 v[54:55], v6 offset:43520\n"
    "v_mfma_f32_32x32x16_bf16 v[112:127], v[36:39], a[36:39], v[112:127]\n"
    "ds_read_b64_tr_b16 v[32:33], v7 offset:0\n"
    "ds_read_b64_tr_b16 v[34:35], v7 offset:512\n"
    "s_waitcnt lgkmcnt(6)\n"
    "v_mfma_f32_32x32x16_bf16 v[96:111], v[40:43], a[40:43], v[96:111]\n"
    "ds_read_b64_tr_b16 v[36:37], v8 offset:0\n"
    "ds_read_b64_tr_b16 v[38:39], v8 offset:512\n"
    "v_mfma_f32_32x32x16_bf16 v[112:127], v[44:47], a[40:43], v[112:127]\n"
    "ds_read_b64_tr_b16 v[40:41], v7 offset:2048\n"
    "ds_read_b64_tr_b16 v[42:43], v7 offset:2560\n"
    "s_waitcnt lgkmcnt(6)\n"
    "v_mfma_f32_32x32x16_bf16 v[96:111], v[48:51], a[44:47], v[96:111]\n"
    "ds_read_b64_tr_b16 v[44:45], v8 offset:2048\n"
    "ds_read_b64_tr_b16 v[46:47], v8 offset:2560\n"
    "v_mfma_f32_32x32x16_bf16 v[112:127], v[52:55], a[44:47], v[112:127]\n"
    "ds_read_b64_tr_b16 v[48:49], v7 offset:8192\n"
    "ds_read_b64_tr_b16 v[50:51], v7 offset:8704\n"
    "s_waitcnt lgkmcnt(6)\n"
    "s_waitcnt vmcnt(12)\n"
    "v_mfma_f32_32x32x16_bf16 v[96:111], v[32:35], a[48:51], v[96:111]\n"
    "ds_read_b64_tr_b16 v[52:53], v8 offset:8192\n"
    "ds_read_b64_tr_b16 v[54:55], v8 offset:8704\n"
    "v_mfma_f32_32x32x16_bf16 v[112:127], v[36:39], a[48:51], v[112:127]\n"
    "ds_read_b64_tr_b16 v[32:33], v7 offset:10240\n"
    "ds_read_b64_tr_b16 v[34:35], v7 offset:10752\n"
    "s_waitcnt lgkmcnt(6)\n"
    "s_waitcnt vmcnt(11)\n"
    "v_mfma_f32_32x32x16_bf16 v[96:111], v[40:43], a[52:55], v[96:111]\n"
    "ds_read_b64_tr_b16 v[36:37], v8 offset:10240\n"
    "ds_read_b64_tr_b16 v[38:39], v8 offset:10752\n"
    "v_mfma_f32_32x32x16_bf16 v[112:127], v[44:47], a[52:55], v[112:127]\n"
    "ds_read_b64_tr_b16 v[40:41], v7 offset:16384\n"
    "ds_read_b64_tr_b16 v[42:43], v7 offset:16896\n"
    "s_waitcnt lgkmcnt(6)\n"
    "s_waitcnt vmcnt(10)\n"
    "v_mfma_f32_32x32x16_bf16 v[96:111], v[48:51], a[56:59], v[96:111]\n"
    "ds_read_b64_tr_b16 v[44:45], v8 offset:16384\n"
    "ds_read_b64_tr_b16 v[46:47], v8 offset:16896\n"
    "v_mfma_f32_32x32x16_bf16 v[112:127], v[52:55], a[56:59], v[112:127]\n"
    "ds_read_b64_tr_b16 v[48:49], v7 offset:18432\n"
    "ds_read_b64_tr_b16 v[50:51], v7 offset:18944\n"
    "s_waitcnt lgkmcnt(6)\n"
    "s_waitcnt vmcnt(9)\n"
    "v_mfma_f32_32x32x16_bf16 v[96:111], v[32:35], a[60:63], v[96:111]\n"
    "ds_read_b64_tr_b16 v[52:53], v8 offset:18432\n"
    "ds_read_b64_tr_b16 v[54:55], v8 offset:18944\n"
    "v_mfma_f32_32x32x16_bf16 v[112:127], v[36:39], a[60:63], v[112:127]\n"
    "ds_read_b64_tr_b16 v[32:33], v7 offset:24576\n"
    "ds_read_b64_tr_b16 v[34:35], v7 offset:25088\n"
    "s_waitcnt lgkmcnt(6)\n"
    "s_waitcnt vmcnt(8)\n"
    "v_mfma_f32_32x32x16_bf16 v[96:111], v[40:43], a[64:67], v[96:111]\n"
    "ds_read_b64_tr_b16 v[36:37], v8 offset:24576\n"
    "ds_read_b64_tr_b16 v[38:39], v8 offset:25088\n"
    "v_mfma_f32_32x32x16_bf16 v[112:127], v[44:47], a[64:67], v[112:127]\n"
    "ds_read_b64_tr_b16 v[40:41], v7 offset:26624\n"
    "ds_read_b64_tr_b16 v[42:43], v7 offset:27136\n"
    "s_waitcnt vmcnt(0)\n"
    "s_waitcnt lgkmcnt(6)\n"
    "v_mfma_f32_32x32x16_bf16 v[96:111], v[48:51], a[68:71], v[96:111]\n"
    "ds_read_b64_tr_b16 v[44:45], v8 offset:26624\n"
    "ds_read_b64_tr_b16 v[46:47], v8 offset:27136\n"
    "ds_read_b32 v170, v90\n"
    "ds_read_b32 v172, v91 offset:0\n"
    "ds_read_b32 v173, v91 offset:32\n"
    "v_mfma_f32_32x32x16_bf16 v[112:127], v[52:55], a[68:71], v[112:127]\n"
    "ds_read_b64_tr_b16 v[48:49], v7 offset:32768\n"
    "ds_read_b64_tr_b16 v[50:51], v7 offset:33280\n"
    "ds_read_b32 v174, v91 offset:64\n"
    "ds_read_b32 v175, v91 offset:96\n"
    "v_lshlrev_b32 v64, 16, v56\n"
    "s_waitcnt lgkmcnt(11)\n"
    "v_mfma_f32_32x32x16_bf16 v[96:111], v[32:35], a[72:75], v[96:111]\n"
    "ds_read_b64_tr_b16 v[52:53], v8 offset:32768\n"
    "ds_read_b64_tr_b16 v[54:55], v8 offset:33280\n"
    "v_and_b32 v65, s79, v56\n"
    "v_lshlrev_b32 v66, 16, v57\n"
    "v_and_b32 v67, s79, v57\n"
    "v_mfma_f32_32x32x16_bf16 v[112:127], v[36:39], a[72:75], v[112:127]\n"
    "ds_read_b64_tr_b16 v[32:33], v7 offset:34816\n"
    "ds_read_b64_tr_b16 v[34:35], v7 offset:35328\n"
    "v_lshlrev_b32 v68, 16, v58\n"
    "v_and_b32 v69, s79, v58\n"
    "v_lshlrev_b32 v70, 16, v59\n"
    "s_waitcnt lgkmcnt(11)\n"
    "v_mfma_f32_32x32x16_bf16 v[96:111], v[40:43], a[76:79], v[96:111]\n"
    "ds_read_b64_tr_b16 v[36:37], v8 offset:34816\n"
    "ds_read_b64_tr_b16 v[38:39], v8 offset:35328\n"
    "v_and_b32 v71, s79, v59\n"
    "v_lshlrev_b32 v72, 16, v60\n"
    "v_and_b32 v73, s79, v60\n"
    "v_mfma_f32_32x32x16_bf16 v[112:127], v[44:47], a[76:79], v[112:127]\n"
    "v_lshlrev_b32 v74, 16, v61\n"
    "v_and_b32 v75, s79, v61\n"
    "v_lshlrev_b32 v76, 16, v62\n"
    "v_and_b32 v77, s79, v62\n"
    "v_lshlrev_b32 v78, 16, v63\n"
    "s_waitcnt lgkmcnt(4)\n"
    "v_mfma_f32_32x32x16_bf16 v[96:111], v[48:51], a[80:83], v[96:111]\n"
    "v_and_b32 v79, s79, v63\n"
    "v_sub_f32 v169, 1.0, v170\n"
    "v_mov_b32 v168, v170\n"
    "v_mul_f32 v64, v170, v64\n"
    "v_mul_f32 v65, v170, v65\n"
    "v_mfma_f32_32x32x16_bf16 v[112:127], v[52:55], a[80:83], v[112:127]\n"
    "v_mul_f32 v66, v170, v66\n"
    "v_mul_f32 v67, v170, v67\n"
    "v_mul_f32 v68, v170, v68\n"
    "v_mul_f32 v69, v170, v69\n"
    "v_mul_f32 v70, v170, v70\n"
    "s_waitcnt lgkmcnt(0)\n"
    "v_mfma_f32_32x32x16_bf16 v[96:111], v[32:35], a[84:87], v[96:111]\n"
    "v_mul_f32 v71, v170, v71\n"
    "v_mul_f32 v72, v170, v72\n"
    "v_mul_f32 v73, v170, v73\n"
    "v_mul_f32 v74, v170, v74\n"
    "v_mul_f32 v75, v170, v75\n"
    "v_mfma_f32_32x32x16_bf16 v[112:127], v[36:39], a[84:87], v[112:127]\n"
    "v_mul_f32 v76, v170, v76\n"
    "v_mul_f32 v77, v170, v77\n"
    "v_mul_f32 v78, v170, v78\n"
    "v_mul_f32 v79, v170, v79\n"
    "s_nop 1\n"
    "v_pk_fma_f32 v[96:97], v[96:97], v[168:169], v[64:65] op_sel:[0,1,0] op_sel_hi:[1,1,1]\n"
    "v_pk_fma_f32 v[98:99], v[98:99], v[168:169], v[66:67] op_sel:[0,1,0] op_sel_hi:[1,1,1]\n"
    "ds_write_b128 v80, v[96:99]\n"
    "v_pk_fma_f32 v[100:101], v[100:101], v[168:169], v[68:69] op_sel:[0,1,0] op_sel_hi:[1,1,1]\n"
    "v_pk_fma_f32 v[102:103], v[102:103], v[168:169], v[70:71] op_sel:[0,1,0] op_sel_hi:[1,1,1]\n"
    "ds_write_b128 v81, v[100:103]\n"
    "v_pk_fma_f32 v[104:105], v[104:105], v[168:169], v[72:73] op_sel:[0,1,0] op_sel_hi:[1,1,1]\n"
    "v_pk_fma_f32 v[106:107], v[106:107], v[168:169], v[74:75] op_sel:[0,1,0] op_sel_hi:[1,1,1]\n"
    "ds_write_b128 v82, v[104:107]\n"
    "v_pk_fma_f32 v[108:109], v[108:109], v[168:169], v[76:77] op_sel:[0,1,0] op_sel_hi:[1,1,1]\n"
    "v_pk_fma_f32 v[110:111], v[110:111], v[168:169], v[78:79] op_sel:[0,1,0] op_sel_hi:[1,1,1]\n"
    "ds_write_b128 v83, v[108:111]\n"
    "v_pk_mul_f32 v[112:113], v[112:113], v[168:169] op_sel:[0,1] op_sel_hi:[1,1]\n"
    "v_pk_mul_f32 v[114:115], v[114:115], v[168:169] op_sel:[0,1] op_sel_hi:[1,1]\n"
    "ds_write_b128 v84, v[112:115]\n"
    "v_pk_mul_f32 v[116:117], v[116:117], v[168:169] op_sel:[0,1] op_sel_hi:[1,1]\n"
    "v_pk_mul_f32 v[118:119], v[118:119], v[168:169] op_sel:[0,1] op_sel_hi:[1,1]\n"
    "ds_write_b128 v85, v[116:119]\n"
    "v_pk_mul_f32 v[120:121], v[120:121], v[168:169] op_sel:[0,1] op_sel_hi:[1,1]\n"
    "v_pk_mul_f32 v[122:123], v[122:123], v[168:169] op_sel:[0,1] op_sel_hi:[1,1]\n"
    "ds_write_b128 v86, v[120:123]\n"
    "v_pk_mul_f32 v[124:125], v[124:125], v[168:169] op_sel:[0,1] op_sel_hi:[1,1]\n"
    "v_pk_mul_f32 v[126:127], v[126:127], v[168:169] op_sel:[0,1] op_sel_hi:[1,1]\n"
    "ds_write_b128 v87, v[124:127]\n"
    "s_waitcnt lgkmcnt(0)\n"
    "s_barrier\n"
    "ds_read_b128 v[208:211], v88 offset:0\n"
    "ds_read_b128 v[212:215], v88 offset:16384\n"
    "ds_read_b128 v[216:219], v89 offset:1024\n"
    "ds_read_b128 v[220:223], v89 offset:17408\n"
    "ds_read_b128 v[224:227], v88 offset:2048\n"
    "ds_read_b128 v[228:231], v88 offset:18432\n"
    "ds_read_b128 v[232:235], v89 offset:3072\n"
    "ds_read_b128 v[236:239], v89 offset:19456\n"
    "s_waitcnt lgkmcnt(7)\n"
    "v_fma_f32 v188, v188, v172, v208\n"
    "v_fma_f32 v189, v189, v172, v209\n"
    "v_fma_f32 v190, v190, v172, v210\n"
    "v_fma_f32 v191, v191, v172, v211\n"
    "s_waitcnt lgkmcnt(6)\n"
    "v_pk_add_f32 v[188:189], v[188:189], v[212:213]\n"
    "v_pk_add_f32 v[190:191], v[190:191], v[214:215]\n"
    "s_mov_b32 s76, 0\n"
    "buffer_store_dwordx4 v[188:191], v9, s[52:55], s76 offen\n"
    "s_waitcnt lgkmcnt(5)\n"
    "v_fma_f32 v192, v192, v173, v216\n"
    "v_fma_f32 v193, v193, v173, v217\n"
    "v_fma_f32 v194, v194, v173, v218\n"
    "v_fma_f32 v195, v195, v173, v219\n"
    "s_waitcnt lgkmcnt(4)\n"
    "v_pk_add_f32 v[192:193], v[192:193], v[220:221]\n"
    "v_pk_add_f32 v[194:195], v[194:195], v[222:223]\n"
    "s_mov_b32 s76, 16384\n"
    "buffer_store_dwordx4 v[192:195], v9, s[52:55], s76 offen\n"
    "s_waitcnt lgkmcnt(3)\n"
    "v_fma_f32 v196, v196, v174, v224\n"
    "v_fma_f32 v197, v197, v174, v225\n"
    "v_fma_f32 v198, v198, v174, v226\n"
    "v_fma_f32 v199, v199, v174, v227\n"
    "s_waitcnt lgkmcnt(2)\n"
    "v_pk_add_f32 v[196:197], v[196:197], v[228:229]\n"
    "v_pk_add_f32 v[198:199], v[198:199], v[230:231]\n"
    "s_mov_b32 s76, 32768\n"
    "buffer_store_dwordx4 v[196:199], v9, s[52:55], s76 offen\n"
    "s_waitcnt lgkmcnt(1)\n"
    "v_fma_f32 v200, v200, v175, v232\n"
    "v_fma_f32 v201, v201, v175, v233\n"
    "v_fma_f32 v202, v202, v175, v234\n"
    "v_fma_f32 v203, v203, v175, v235\n"
    "s_waitcnt lgkmcnt(0)\n"
    "v_pk_add_f32 v[200:201], v[200:201], v[236:237]\n"
    "v_pk_add_f32 v[202:203], v[202:203], v[238:239]\n"
    "s_mov_b32 s76, 49152\n"
    "buffer_store_dwordx4 v[200:203], v9, s[52:55], s76 offen\n"
    "Lend_%=:\n"
    :
    : "s"(__builtin_amdgcn_kernarg_segment_ptr()), "s"(blockIdx.x), "v"(threadIdx.x),
      "s"((unsigned)(size_t)(__attribute__((address_space(3))) char*)lds)
    :
      "v2","v3","v4","v5","v6","v7","v8","v9","v10","v11","v12","v13","v14","v15","v16","v17","v18","v19","v20","v21",
      "v22","v23","v24","v25","v26","v27","v28","v29","v30","v31","v32","v33","v34","v35","v36","v37","v38","v39","v40",
      "v41","v42","v43","v44","v45","v46","v47","v48","v49","v50","v51","v52","v53","v54","v55","v56","v57","v58","v59",
      "v60","v61","v62","v63","v64","v65","v66","v67","v68","v69","v70","v71","v72","v73","v74","v75","v76","v77","v78",
      "v79","v80","v81","v82","v83","v84","v85","v86","v87","v88","v89","v90","v91","v92","v93","v94","v95","v96","v97",
      "v98","v99","v100","v101","v102","v103","v104","v105","v106","v107","v108","v109","v110","v111","v112","v113",
      "v114","v115","v116","v117","v118","v119","v120","v121","v122","v123","v124","v125","v126","v127","v128","v129",
      "v130","v131","v132","v133","v134","v135","v136","v137","v138","v139","v140","v141","v142","v143","v144","v145",
      "v146","v147","v148","v149","v150","v151","v152","v153","v154","v155","v156","v157","v158","v159","v160","v161",
      "v162","v163","v164","v165","v166","v167","v168","v169","v170","v171","v172","v173","v174","v175","v176","v177",
      "v178","v179","v180","v181","v182","v183","v184","v185","v186","v187","v188","v189","v190","v191","v192","v193",
      "v194","v195","v196","v197","v198","v199","v200","v201","v202","v203","v204","v205","v206","v207","v208","v209",
      "v210","v211","v212","v213","v214","v215","v216","v217","v218","v219","v220","v221","v222","v223","v224","v225",
      "v226","v227","v228","v229","v230","v231","v232","v233","v234","v235","v236","v237","v238","v239","v240","v241",
      "v242","v243","v244","v245","v246","v247","v248","v249","v250","v251","v252","v253","v254","v255","a0","a1","a2",
      "a3","a4","a5","a6","a7","a8","a9","a10","a11","a12","a13","a14","a15","a16","a17","a18","a19","a20","a21","a22",
      "a23","a24","a25","a26","a27","a28","a29","a30","a31","a32","a33","a34","a35","a36","a37","a38","a39","a40","a41",
      "a42","a43","a44","a45","a46","a47","a48","a49","a50","a51","a52","a53","a54","a55","a56","a57","a58","a59","a60",
      "a61","a62","a63","a64","a65","a66","a67","a68","a69","a70","a71","a72","a73","a74","a75","a76","a77","a78","a79",
      "a80","a81","a82","a83","a84","a85","a86","a87","a88","a89","a90","a91","a92","a93","a94","a95","a96","a97","a98",
      "a99","a100","a101","a102","a103","a104","a105","a106","a107","a108","a109","a110","a111","a112","a113","a114",
      "a115","a116","a117","a118","a119","a120","a121","a122","a123","a124","a125","a126","a127","a128","a129","a130",
      "a131","a132","a133","a134","a135","a136","a137","a138","a139","a140","a141","a142","a143","a144","a145","a146",
      "a147","a148","a149","a150","a151","a152","a153","a154","a155","a156","a157","a158","a159","a160","a161","a162",
      "a163","a164","a165","a166","a167","a168","a169","a170","a171","a172","a173","a174","a175","a176","a177","a178",
      "a179","a180","a181","a182","a183","a184","a185","a186","a187","a188","a189","a190","a191","a192","a193","a194",
      "a195","a196","a197","a198","a199","a200","a201","a202","a203","a204","a205","a206","a207","a208","a209","a210",
      "a211","a212","a213","a214","a215","a216","a217","a218","a219","a220","a221","a222","a223","a224","a225","a226",
      "a227","a228","a229","a230","a231","a232","a233","a234","a235","a236","a237","a238","a239","a240","a241","a242",
      "a243","a244","a245","a246","a247","a248","a249","a250","a251","a252","a253","a254","a255","s8","s9","s10","s11",
      "s12","s13","s14","s15","s16","s17","s18","s19","s20","s21","s22","s23","s24","s25","s26","s27","s28","s29","s30",
      "s31","s33","s34","s35","s36","s37","s38","s39","s40","s41","s42","s43","s44","s45","s46","s47","s48","s49","s50",
      "s51","s52","s53","s54","s55","s56","s57","s58","s59","s60","s61","s62","s63","s64","s65","s66","s67","s68","s69",
      "s70","s71","s72","s73","s74","s75","s76","s77","s78","s79","s80","s81","s82","s83","s84","s85","s86","s87","s88",
      "s89","s90","s91","s92","s93","s94","s95","s96","s97","s98","s99","vcc","scc","memory");
}

extern "C" void kernel_launch(void* const* d_in, const int* in_sizes, int n_in, void* d_out, int out_size,
                              void* d_ws, size_t ws_size, hipStream_t stream) {
  gated_mix_s5_gmlp_kernel<<<256, 256, 0, stream>>>(d_in[0], d_in[10], d_in[3], d_in[4], d_ws, d_in[1], d_in[2],
                                                    d_in[7], d_in[11], d_in[5], d_in[6], d_in[13], d_out, d_in[8],
                                                    d_in[12], d_in[14]);
}
```
